```python
import torch

def _softmax(x, axis=-1):
    return torch.softmax(x, dim=axis)

def _gelu(x):
    return torch.nn.functional.gelu(x, approximate='tanh')

def _silu(x):
    return torch.nn.functional.silu(x)

def _sigmoid(x):
    return torch.sigmoid(x)

def _layernorm(x, axis=-1, eps=1e-05):
    m = x.mean(dim=axis, keepdim=True)
    v = x.var(dim=axis, keepdim=True, unbiased=False)
    return (x - m) / torch.sqrt(v + eps)

def _scatter_add(tgt, idx, src, axis=0):
    return tgt.clone().index_add_(axis, idx, src)

def _segment_sum(src, seg, n):
    out = torch.zeros((n,) + src.shape[1:], dtype=src.dtype)
    return out.index_add_(0, seg, src)

def _pad(x, pw):
    flat = tuple((v for pair in reversed(pw) for v in pair))
    return torch.nn.functional.pad(x, flat)
kernel_name = 'adaLN_cond__softmax_full_mha__mixer_gmlp__prompt'

def setup_inputs():

    _w = lambda a: (lambda t: t.to(torch.bfloat16) if t.is_floating_point() else t)(torch.as_tensor(a))

    def _A_setup():
        B, S, D = (1, 2048, 512)
        return {'x': _w((lambda *s: torch.randn(*s, dtype=torch.bfloat16) * 0.05)(B, S, D)), 'wqkv': _w((lambda *s: torch.randn(*s, dtype=torch.bfloat16) * 0.05)(D, 3 * D)), 'wo': _w((lambda *s: torch.randn(*s, dtype=torch.bfloat16) * 0.05)(D, D))}
    _A_in = _A_setup()

    def _B_setup():
        B, S, D, F = (1, 2048, 512, 1792)
        Fh = F // 2
        return {'x': _w((lambda *s: torch.randn(*s, dtype=torch.bfloat16) * 0.05)(B, S, D)), 'w_in': _w((lambda *s: torch.randn(*s, dtype=torch.bfloat16) * 0.4)(D, 2 * Fh)), 'w_sp': _w(0.01 * (lambda *s: torch.randn(*s, dtype=torch.bfloat16) * 0.4)(S, S)), 'b_sp': _w(1.0 + 0.01 * (lambda *s: torch.randn(*s, dtype=torch.bfloat16) * 0.05)(S)), 'w_out': _w((lambda *s: torch.randn(*s, dtype=torch.bfloat16) * 0.05)(Fh, D))}
    _B_in = _B_setup()
    return {'A_x': _A_in['x'], 'A_wqkv': _A_in['wqkv'], 'A_wo': _A_in['wo'], 'B_x': _B_in['x'], 'B_w_in': _B_in['w_in'], 'B_w_sp': _B_in['w_sp'], 'B_b_sp': _B_in['b_sp'], 'B_w_out': _B_in['w_out'], 'c': _w((lambda *s: torch.randn(*s, dtype=torch.bfloat16) * 0.1)(1, 512) * 8.0), 'w_mod': _w((lambda *s: torch.randn(*s, dtype=torch.bfloat16) * 0.1)(512, 6 * 512) * 512 ** (-0.5) * 8.0)}

def reference(A_x, A_wqkv, A_wo, B_x, B_w_in, B_w_sp, B_b_sp, B_w_out, c, w_mod):

    def _A_ref(x, wqkv, wo):
        B, S, D = x.shape
        H = 32
        Dh = D // H
        qkv = (lambda x, *d: x.permute(*d))((x @ wqkv).reshape(B, S, 3, H, Dh), 2, 0, 3, 1, 4)
        q, k, v = (qkv[0], qkv[1], qkv[2])
        scale = Dh ** (-0.5)
        scores = torch.einsum('bhqd,bhkd->bhqk', q, k) * scale
        p = _softmax(scores, axis=-1)
        o = torch.einsum('bhqk,bhkd->bhqd', p, v)
        return (lambda x, *d: x.permute(*d))(o, 0, 2, 1, 3).reshape(B, S, D) @ wo

    def _B_ref(x, w_in, w_sp, b_sp, w_out):
        B, S, D = x.shape
        F = w_in.shape[-1]
        Fh = F // 2
        z = _gelu(torch.einsum('bsd,df->bsf', x, w_in))
        z1 = z[..., :Fh].clone()
        z2 = _layernorm(z[..., Fh:], axis=-1)
        g = torch.einsum('ts,bsf->btf', w_sp, z2) + b_sp[None, :, None]
        return torch.einsum('bsf,fd->bsd', z1 * g, w_out)
    x = A_x
    mod = (_silu(c) @ w_mod).reshape(c.shape[0], 6, 1, -1)
    sa, ca, ga, sb, cb, gb = (mod[:, 0].clone(), mod[:, 1].clone(), mod[:, 2].clone(), mod[:, 3].clone(), mod[:, 4].clone(), mod[:, 5].clone())
    h = _layernorm(x, axis=-1) * (1 + ca) + sa
    x = x + ga * _A_ref(h, A_wqkv, A_wo)
    h = _layernorm(x, axis=-1) * (1 + cb) + sb
    x = x + gb * _B_ref(h, B_w_in, B_w_sp, B_b_sp, B_w_out)
    return x
```

```cpp
#include <hip/hip_runtime.h>
#include <hip/hip_bf16.h>
#include <stdint.h>
typedef unsigned short u16;
static __device__ __forceinline__ float bf2f(u16 v){ return __uint_as_float(((unsigned)v)<<16); }
static __device__ __forceinline__ u16 f2bf(float f){ unsigned u=__float_as_uint(f); u += 0x7fffu + ((u>>16)&1u); return (u16)(u>>16); }
constexpr int S=2048, D=512, H=32, DH=16, F=1792, FH=896;

__global__ void k_mod(const u16* c, const u16* wmod, float* mod){
  int j = blockIdx.x*blockDim.x+threadIdx.x; if(j>=6*D) return;
  float acc=0.f;
  for(int k=0;k<D;++k){ float cv=bf2f(c[k]); float s=cv/(1.f+__expf(-cv)); s=bf2f(f2bf(s)); acc+=s*bf2f(wmod[k*6*D+j]); }
  mod[j]=bf2f(f2bf(acc));
}
__global__ void k_ln(const float* x, int ld, int n, const float* sc, const float* sh, u16* out, int ldo){
  int row=blockIdx.x; int l=threadIdx.x;
  float s=0.f; for(int i=l;i<n;i+=64) s+=x[row*ld+i];
  for(int o=32;o;o>>=1) s+=__shfl_xor(s,o);
  float m=s/n; float v=0.f; for(int i=l;i<n;i+=64){float d=x[row*ld+i]-m; v+=d*d;}
  for(int o=32;o;o>>=1) v+=__shfl_xor(v,o);
  float r=rsqrtf(v/n+1e-5f);
  for(int i=l;i<n;i+=64){ float y=(x[row*ld+i]-m)*r; if(sc) y=y*(1.f+sc[i])+sh[i]; out[row*ldo+i]=f2bf(y);}
}
__global__ void k_cvt(const u16* in, float* out, int n){ int i=blockIdx.x*blockDim.x+threadIdx.x; if(i<n) out[i]=bf2f(in[i]); }
__global__ void k_gemm(const u16* A, int lda, const u16* B, int ldb, float* C, int ldc, int M,int N,int K){
  __shared__ float As[32][33], Bs[32][33];
  int tx=threadIdx.x&31, ty=threadIdx.x>>5;
  int r0=blockIdx.y*32, c0=blockIdx.x*32;
  float acc[4]={0,0,0,0};
  for(int k0=0;k0<K;k0+=32){
    for(int i=ty;i<32;i+=8){ As[i][tx]=bf2f(A[(r0+i)*lda+k0+tx]); Bs[i][tx]=bf2f(B[(k0+i)*ldb+c0+tx]); }
    __syncthreads();
    for(int k=0;k<32;++k){ float b=Bs[k][tx]; for(int j=0;j<4;++j) acc[j]+=As[ty*4+j][k]*b; }
    __syncthreads();
  }
  for(int j=0;j<4;++j) C[(r0+ty*4+j)*ldc+c0+tx]=acc[j];
}
__global__ void k_attn(const float* qkv, u16* o){
  int h=blockIdx.y; int q=blockIdx.x*64+threadIdx.x;
  float qv[DH]; for(int d=0;d<DH;++d) qv[d]=bf2f(f2bf(qkv[q*3*D+h*DH+d]))*0.25f;
  float m=-1e30f,l=0.f,acc[DH]; for(int d=0;d<DH;++d) acc[d]=0.f;
  __shared__ float ks[64][DH], vs[64][DH];
  for(int k0=0;k0<S;k0+=64){
    __syncthreads();
    for(int d=0;d<DH;++d){ ks[threadIdx.x][d]=bf2f(f2bf(qkv[(k0+threadIdx.x)*3*D+D+h*DH+d])); vs[threadIdx.x][d]=bf2f(f2bf(qkv[(k0+threadIdx.x)*3*D+2*D+h*DH+d])); }
    __syncthreads();
    for(int k=0;k<64;++k){ float s=0.f; for(int d=0;d<DH;++d) s+=qv[d]*ks[k][d];
      float mn=fmaxf(m,s); float a=__expf(m-mn), p=__expf(s-mn); l=l*a+p; for(int d=0;d<DH;++d) acc[d]=acc[d]*a+p*vs[k][d]; m=mn; }
  }
  for(int d=0;d<DH;++d) o[q*D+h*DH+d]=f2bf(acc[d]/l);
}
__global__ void k_res(const float* xin, const float* y, const float* g, float* xout, u16* obf, int n){
  int i=blockIdx.x*blockDim.x+threadIdx.x; if(i>=n) return; float v=xin[i]+g[i%D]*y[i]; xout[i]=v; if(obf) obf[i]=f2bf(v);
}
__global__ void k_gelu(float* z, int n){ int i=blockIdx.x*blockDim.x+threadIdx.x; if(i>=n) return; float x=z[i]; float t=tanhf(0.7978845608f*(x+0.044715f*x*x*x)); z[i]=0.5f*x*(1.f+t);}
__global__ void k_gate(const float* z, const float* g, const u16* bsp, u16* out){ int i=blockIdx.x*blockDim.x+threadIdx.x; if(i>=S*FH) return; int r=i/FH,c=i%FH; out[i]=f2bf(z[r*F+c]*(g[i]+bf2f(bsp[r]))); }

extern "C" void kernel_launch(void* const* d_in, const int* in_sizes, int n_in, void* d_out, int out_size, void* d_ws, size_t ws_size, hipStream_t stream){
  const u16* x=(const u16*)d_in[0]; const u16* wqkv=(const u16*)d_in[1]; const u16* wo=(const u16*)d_in[2];
  const u16* win=(const u16*)d_in[4]; const u16* wsp=(const u16*)d_in[5]; const u16* bsp=(const u16*)d_in[6]; const u16* wout=(const u16*)d_in[7];
  const u16* c=(const u16*)d_in[8]; const u16* wmod=(const u16*)d_in[9];
  char* ws=(char*)d_ws; size_t off=0; auto alloc=[&](size_t b){ void* p=ws+off; off+=(b+255)&~(size_t)255; return p; };
  float* mod=(float*)alloc(6*D*4); float* xf=(float*)alloc(S*D*4); u16* h=(u16*)alloc(S*D*2); float* qkv=(float*)alloc((size_t)S*3*D*4);
  u16* o=(u16*)alloc(S*D*2); float* y=(float*)alloc(S*D*4); float* x1=(float*)alloc(S*D*4); float* z=(float*)alloc((size_t)S*F*4);
  u16* z2=(u16*)alloc(S*FH*2); float* g=(float*)alloc(S*FH*4); u16* zg=(u16*)alloc(S*FH*2); float* x2=(float*)alloc(S*D*4);
  k_mod<<<12,256,0,stream>>>(c,wmod,mod);
  k_cvt<<<S*D/256,256,0,stream>>>(x,xf,S*D);
  k_ln<<<S,64,0,stream>>>(xf,D,D,mod+D,mod,h,D);
  k_gemm<<<dim3(3*D/32,S/32),256,0,stream>>>(h,D,wqkv,3*D,qkv,3*D,S,3*D,D);
  k_attn<<<dim3(S/64,H),64,0,stream>>>(qkv,o);
  k_gemm<<<dim3(D/32,S/32),256,0,stream>>>(o,D,wo,D,y,D,S,D,D);
  k_res<<<S*D/256,256,0,stream>>>(xf,y,mod+2*D,x1,nullptr,S*D);
  k_ln<<<S,64,0,stream>>>(x1,D,D,mod+4*D,mod+3*D,h,D);
  k_gemm<<<dim3(F/32,S/32),256,0,stream>>>(h,D,win,F,z,F,S,F,D);
  k_gelu<<<S*F/256,256,0,stream>>>(z,S*F);
  k_ln<<<S,64,0,stream>>>(z+FH,F,FH,nullptr,nullptr,z2,FH);
  k_gemm<<<dim3(FH/32,S/32),256,0,stream>>>(wsp,S,z2,FH,g,FH,S,FH,S);
  k_gate<<<S*FH/256,256,0,stream>>>(z,g,bsp,zg);
  k_gemm<<<dim3(D/32,S/32),256,0,stream>>>(zg,FH,wout,D,y,D,S,D,FH);
  k_res<<<S*D/256,256,0,stream>>>(x1,y,mod+5*D,x2,(u16*)d_out,S*D);
}
```

```cpp
#include <hip/hip_runtime.h>
typedef unsigned long long u64;

__global__ __launch_bounds__(256, 1) void adaln_block_kernel(
    void* ws, u64 m0, const void* x, u64 m1, const void* wqkv, u64 m2, const void* wo, u64 m3,
    const void* win, u64 m4, const void* wsp, u64 m5, const void* wout, u64 m6, const void* wmod, u64 m7,
    const void* c, u64 m8, void* out, u64 m9, const void* bsp, u64 m10) {
  const int wave = __builtin_amdgcn_readfirstlane(threadIdx.x >> 6);
  asm volatile(
      "Lstart_%=:\n"
      "s_load_dwordx16 s[8:23], %0, 0x0\n"
      "s_load_dwordx16 s[24:39], %0, 0x40\n"
      "s_load_dwordx8 s[40:47], %0, 0x80\n"
      "s_load_dwordx4 s[48:51], %0, 0xa0\n"
      "s_getpc_b64 s[68:69]\n"
      "Lpc_%=:\n"
      "v_mbcnt_lo_u32_b32 v255, -1, 0\n"
      "v_mbcnt_hi_u32_b32 v255, -1, v255\n"
      "s_lshr_b32 s55, %1, 3\n"
      "s_lshl2_add_u32 s63, s55, %2\n"
      "s_pack_ll_b32_b16 s69, s69, 0\n"
      "s_mov_b32 s70, Lcode_%=-Lpc_%=\n"
      "s_lshl_b32 s71, 1, 17\n"
      "v_lshl_add_u32 v0, v255, 7, s63\n"
      "v_lshlrev_b32 v0, 7, v0\n"
      "buffer_load_dword v250, v0, s[68:71], 0 offen\n"
      "s_mov_b32 s52, %1\n"
      "s_mov_b32 s53, %2\n"
      "s_and_b32 s54, %1, 7\n"
      "v_lshlrev_b32 v254, 4, v255\n"
      "s_mul_i32 s97, s63, 192\n"
      "s_add_u32 s97, s97, s54\n"
      "s_lshl_b32 s97, s97, 7\n"
      "v_lshrrev_b32 v4, 1, v255\n"
      "v_lshl_add_u32 v6, v4, 2, v255\n"
      "v_lshlrev_b32 v6, 10, v6\n"
      "s_and_b32 s91, s55, 7\n"
      "s_and_b32 s72, s54, 3\n"
      "s_lshl3_add_u32 s72, s72, s91\n"
      "s_lshr_b32 s66, s55, 3\n"
      "s_lshl2_add_u32 s71, s72, s53\n"
      "s_lshl2_add_u32 s71, s71, s66\n"
      "s_lshl_b32 s71, s71, 12\n"
      "v_lshlrev_b32 v2, 2, v255\n"
      "s_lshl_b32 s64, s53, 8\n"
      "s_cmp_lt_u32 s55, 2\n"
      "s_waitcnt lgkmcnt(0)\n"
      "s_bfm_b64 exec, 8, 0\n"
      "buffer_load_dword v250, v6, s[36:39], s97 offen\n"
      "s_mov_b64 exec, -1\n"
      "buffer_load_dwordx2 v[158:159], off, s[8:11], 0 sc1\n"
      "buffer_load_dword v5, v2, s[40:43], s64 offen\n"
      "s_cbranch_scc1 Lm1_%=\n"
      "buffer_load_dwordx4 v[160:163], v254, s[12:15], s71 offen offset:0\n"
      "buffer_load_dwordx4 v[164:167], v254, s[12:15], s71 offen offset:1024\n"
      "buffer_load_dwordx4 v[168:171], v254, s[12:15], s71 offen offset:2048\n"
      "buffer_load_dwordx4 v[172:175], v254, s[12:15], s71 offen offset:3072\n"
      "Lm1_%=:\n"
      "v_lshrrev_b32 v3, 3, v255\n"
      "v_mul_u32_u24 v0, 6144, v3\n"
      "v_and_b32 v8, 2, v3\n"
      "v_xor_b32 v8, v8, v255\n"
      "v_and_b32 v8, 7, v8\n"
      "v_lshl_add_u32 v0, v8, 4, v0\n"
      "v_xor_b32 v1, 64, v0\n"
      "s_lshr_b32 s63, s55, 2\n"
      "s_cmp_lt_u32 s55, 2\n"
      "s_cselect_b32 s63, s55, s63\n"
      "s_cselect_b32 s65, 0, s55\n"
      "s_and_b32 s65, s65, 3\n"
      "s_lshl3_add_u32 s75, s63, s54\n"
      "s_lshl2_add_u32 s75, s75, s65\n"
      "s_mul_i32 s98, s53, 786432\n"
      "s_lshl_b32 s65, s75, 5\n"
      "s_add_u32 s98, s98, s65\n"
      "s_cmp_ge_u32 s55, 2\n"
      "s_cbranch_scc1 Lm2_%=\n"
      "s_lshl_b32 m0, s53, 14\n"
      "s_nop 0\n"
      "buffer_load_dwordx4 v0, s[36:39], s98 offen lds\n"
      "s_add_u32 m0, m0, 1024\n"
      "s_add_u32 s98, s98, 49152\n"
      "buffer_load_dwordx4 v1, s[36:39], s98 offen lds\n"
      "s_add_u32 m0, m0, 1024\n"
      "s_add_u32 s98, s98, 49152\n"
      "buffer_load_dwordx4 v0, s[36:39], s98 offen lds\n"
      "s_add_u32 m0, m0, 1024\n"
      "s_add_u32 s98, s98, 49152\n"
      "buffer_load_dwordx4 v1, s[36:39], s98 offen lds\n"
      "s_add_u32 m0, m0, 1024\n"
      "s_add_u32 s98, s98, 49152\n"
      "buffer_load_dwordx4 v0, s[36:39], s98 offen lds\n"
      "s_add_u32 m0, m0, 1024\n"
      "s_add_u32 s98, s98, 49152\n"
      "buffer_load_dwordx4 v1, s[36:39], s98 offen lds\n"
      "Lm2_%=:\n"
      "s_and_b32 s73, s54, 4\n"
      "s_add_u32 s73, s73, s66\n"
      "s_lshl2_add_u32 s99, s91, s53\n"
      "s_sub_u32 s99, s99, 8\n"
      "s_cselect_b32 s65, 0, 33\n"
      "s_max_i32 s99, s99, 0\n"
      "s_mul_i32 s99, s99, 33792\n"
      "s_lshl_b32 s66, s73, 7\n"
      "s_add_u32 s99, s99, s66\n"
      "v_mov_b32 v7, 0x40000000\n"
      "v_lshlrev_b32 v9, 10, v255\n"
      "v_cmp_le_u32 vcc, s65, v255\n"
      "s_nop 1\n"
      "v_cndmask_b32 v9, v9, v7, vcc\n"
      "s_mul_i32 s63, s52, 256\n"
      "s_add_u32 s63, s63, 21561344\n"
      "s_lshl_b32 s65, s52, 7\n"
      "s_add_u32 s65, s65, 32768\n"
      "s_cmp_lt_u32 s52, 160\n"
      "s_cselect_b32 s63, s63, s65\n"
      "s_sub_u32 s65, s63, 4096\n"
      "s_lshr_b32 s67, s55, 1\n"
      "s_cmp_eq_u32 s67, 1\n"
      "s_cselect_b32 s65, s65, 0\n"
      "s_cmp_eq_u32 s67, 0\n"
      "s_cselect_b32 s63, 0, s63\n"
      "s_cmp_eq_u32 s53, 1\n"
      "s_cselect_b32 s63, s65, s63\n"
      "buffer_load_dword v250, off, s[8:11], s63 sc1\n"
      "buffer_load_dword v250, v9, s[16:19], s99 offen\n"
      "s_lshr_b32 s67, s55, 1\n"
      "s_cmp_lg_u32 s67, 4\n"
      "s_cbranch_scc1 Lm3_%=\n"
      "s_xor_b32 s67, s71, 4096\n"
      "v_lshlrev_b32 v10, 7, v255\n"
      "s_bfm_b64 exec, 32, 0\n"
      "buffer_load_dword v250, v10, s[12:15], s67 offen\n"
      "s_mov_b64 exec, -1\n"
      "Lm3_%=:\n"
      "s_lshr_b32 s60, s53, 1\n"
      "s_and_b32 s61, s53, 1\n"
      "s_mov_b32 s62, 400000\n"
      "s_and_b32 s58, s54, 3\n"
      "s_lshl_b32 s58, s58, 3\n"
      "s_lshr_b32 s59, s55, 2\n"
      "s_add_u32 s58, s58, s59\n"
      "s_and_b32 s59, s54, 4\n"
      "s_and_b32 s63, s55, 3\n"
      "s_add_u32 s59, s59, s63\n"
      "v_lshrrev_b32 v6, 2, v255\n"
      "v_and_b32 v6, 4, v6\n"
      "v_xor_b32 v6, v6, v255\n"
      "v_and_b32 v6, 7, v6\n"
      "v_lshlrev_b32 v6, 4, v6\n"
      "v_lshl_add_u32 v249, v3, 10, v6\n"
      "v_mul_u32_u24 v3, 3072, v3\n"
      "v_lshl_add_u32 v3, v8, 4, v3\n"
      "v_xor_b32 v248, 64, v3\n"
      "s_mul_i32 s74, s53, 49152\n"
      "s_add_u32 s74, s74, s66\n"
      "s_lshr_b32 s63, s75, 5\n"
      "s_cmp_eq_u32 s63, 1\n"
      "s_cselect_b32 s76, 1.0, 0\n"
      "s_cmp_eq_u32 s63, 4\n"
      "s_cselect_b32 s76, 1.0, s76\n"
      "s_mul_i32 s77, s53, 6144\n"
      "s_add_u32 s77, s77, 65536\n"
      "s_mov_b64 exec, 0\n"
      "s_cmp_eq_u32 s53, 0\n"
      "s_cbranch_scc1 Lhr0_%=\n"
      "s_cmp_eq_u32 s53, 1\n"
      "s_cbranch_scc1 Lhs0_0_%=\n"
      "s_cmp_eq_u32 s53, 2\n"
      "s_cbranch_scc1 Lhs0_1_%=\n"
      "s_branch Lhs0_2_%=\n"
      "Lhr0_%=:\n"
      "s_mov_b64 exec, -1\n"
      "v_and_b32 v20, 3, v255\n"
      "v_bfe_u32 v21, v255, 2, 2\n"
      "v_lshrrev_b32 v22, 4, v255\n"
      "v_mul_u32_u24 v27, 3072, v22\n"
      "v_lshl_add_u32 v27, v21, 7, v27\n"
      "v_lshrrev_b32 v28, 1, v21\n"
      "v_lshl_add_u32 v27, v28, 5, v27\n"
      "v_and_b32 v28, 1, v22\n"
      "v_xor_b32 v28, s61, v28\n"
      "v_lshl_add_u32 v27, v28, 6, v27\n"
      "v_lshl_add_u32 v27, v20, 3, v27\n"
      "v_add_u32 v6, 65536, v27\n"
      "v_xor_b32 v7, 32, v6\n"
      "v_add_u32 v18, 49152, v6\n"
      "v_add_u32 v19, 49152, v7\n"
      "s_lshl_b32 s64, s53, 14\n"
      "v_lshlrev_b32 v27, 8, v255\n"
      "v_and_b32 v28, 7, v255\n"
      "v_lshl_add_u32 v27, v28, 4, v27\n"
      "v_add_u32 v8, s64, v27\n"
      "s_lshr_b32 s65, s55, 3\n"
      "s_lshl_b32 s65, s65, 6\n"
      "v_xor_b32 v8, s65, v8\n"
      "v_xor_b32 v9, 16, v8\n"
      "v_xor_b32 v10, 32, v8\n"
      "v_xor_b32 v11, 48, v8\n"
      "v_xor_b32 v12, 64, v8\n"
      "v_xor_b32 v13, 80, v8\n"
      "v_xor_b32 v14, 96, v8\n"
      "v_xor_b32 v15, 112, v8\n"
      "s_lshl_b32 s63, s60, 15\n"
      "v_lshlrev_b32 v27, 4, v22\n"
      "v_xor_b32 v27, v27, v254\n"
      "v_add_u32 v16, s63, v27\n"
      "v_xor_b32 v17, 64, v16\n"
      "s_cmp_eq_u32 s53, 3\n"
      "s_cselect_b32 s63, 0xfffff900, 32\n"
      "v_lshrrev_b32 v150, 2, v255\n"
      "v_mul_u32_u24 v150, 3584, v150\n"
      "v_and_b32 v151, 1, v255\n"
      "v_lshl_add_u32 v150, v151, 4, v150\n"
      "v_bfe_u32 v152, v255, 1, 1\n"
      "v_lshl_add_u32 v225, v152, 5, v150\n"
      "v_mul_i32_i24 v152, s63, v152\n"
      "v_add_u32 v224, v150, v152\n"
      "v_add_u32 v224, 1792, v224\n"
      "v_lshrrev_b32 v155, 5, v255\n"
      "v_bfe_u32 v150, v255, 2, 3\n"
      "v_lshlrev_b32 v150, 6, v150\n"
      "v_lshl_add_u32 v150, v155, 5, v150\n"
      "v_lshl_add_u32 v150, v20, 3, v150\n"
      "v_add_u32 v226, s77, v150\n"
      "v_lshl_add_u32 v227, v155, 9, v254\n"
      "s_mul_i32 s63, s72, 57344\n"
      "s_mul_i32 s64, s73, 224\n"
      "s_add_u32 s63, s63, s64\n"
      "s_and_b32 s64, s73, 1\n"
      "s_cselect_b32 s65, 0, 192\n"
      "s_lshl_b32 s64, s64, 5\n"
      "s_min_u32 s66, s53, 2\n"
      "s_lshl_b32 s67, s66, 6\n"
      "s_add_u32 s64, s64, s67\n"
      "s_add_u32 s93, s63, s64\n"
      "s_cmp_eq_u32 s53, 3\n"
      "s_cselect_b32 s64, s65, s64\n"
      "s_add_u32 s92, s63, s64\n"
      "s_mul_i32 s63, s73, 229376\n"
      "s_add_u32 s63, s63, 19660800\n"
      "s_lshr_b32 s64, s72, 1\n"
      "s_lshl_b32 s65, s73, 1\n"
      "s_xor_b32 s64, s64, s65\n"
      "s_mul_i32 s64, s64, 14336\n"
      "s_and_b32 s65, s72, 1\n"
      "s_lshl_b32 s65, s65, 9\n"
      "s_add_u32 s64, s64, s65\n"
      "s_add_u32 s64, s64, s63\n"
      "s_lshl_b32 s65, s53, 11\n"
      "s_add_u32 s94, s64, s65\n"
      "s_lshl_b32 s65, s66, 11\n"
      "s_add_u32 s95, s64, s65\n"
      "s_add_u32 s95, s95, 8192\n"
      "s_lshr_b32 s63, s55, 3\n"
      "s_add_u32 s63, s63, 1\n"
      "s_and_b32 s63, s63, 2\n"
      "s_cselect_b32 s63, 0, 0x40000000\n"
      "v_lshrrev_b32 v31, 1, v255\n"
      "v_mul_u32_u24 v31, 6144, v31\n"
      "v_and_b32 v30, 1, v255\n"
      "v_lshl_add_u32 v31, v30, 4, v31\n"
      "v_or_b32 v31, s63, v31\n"
      "v_mov_b32 v30, 0x40000000\n"
      "v_lshrrev_b32 v23, 2, v255\n"
      "v_mul_u32_u24 v23, 6144, v23\n"
      "v_lshl_add_u32 v23, v20, 10, v23\n"
      "v_cmp_le_u32 vcc, 16, v255\n"
      "s_nop 1\n"
      "v_cndmask_b32 v23, v23, v30, vcc\n"
      "v_lshlrev_b32 v4, 10, v255\n"
      "v_cmp_le_u32 vcc, 24, v255\n"
      "s_nop 1\n"
      "v_cndmask_b32 v4, v4, v30, vcc\n"
      "s_lshl2_add_u32 s99, s91, s53\n"
      "s_mul_i32 s99, s99, 24576\n"
      "s_lshl_b32 s63, s73, 7\n"
      "s_add_u32 s99, s99, s63\n"
      "s_add_u32 s99, s99, 786432\n"
      "s_cmp_lt_u32 s55, 2\n"
      "s_cbranch_scc1 Lm4_%=\n"
      "s_waitcnt vmcnt(4)\n"
      "v_readfirstlane_b32 s65, v158\n"
      "v_readfirstlane_b32 s66, v159\n"
      "s_mul_i32 s56, s65, 0x9E3779B1\n"
      "s_add_u32 s56, s56, 0x7F4A7C15\n"
      "s_mul_i32 s57, s66, 0x85EBCA75\n"
      "s_add_u32 s57, s57, 0x2545F491\n"
      "v_lshlrev_b32 v28, 16, v5\n"
      "v_and_b32 v29, 0xffff0000, v5\n"
      "v_mul_f32 v24, 0xbfb8aa3b, v28\n"
      "v_mul_f32 v25, 0xbfb8aa3b, v29\n"
      "v_exp_f32 v24, v24\n"
      "v_exp_f32 v25, v25\n"
      "v_mov_b32 v252, s56\n"
      "v_mov_b32 v253, s57\n"
      "v_add_f32 v24, 1.0, v24\n"
      "v_add_f32 v25, 1.0, v25\n"
      "v_rcp_f32 v24, v24\n"
      "v_rcp_f32 v25, v25\n"
      "v_mul_f32 v28, v28, v24\n"
      "v_mul_f32 v29, v29, v25\n"
      "v_cvt_pk_bf16_f32 v5, v28, v29\n"
      "s_xor_b32 s69, s71, 4096\n"
      "buffer_load_dwordx4 v[176:179], v254, s[12:15], s69 offen offset:0\n"
      "buffer_load_dwordx4 v[180:183], v254, s[12:15], s69 offen offset:1024\n"
      "buffer_load_dwordx4 v[184:187], v254, s[12:15], s69 offen offset:2048\n"
      "buffer_load_dwordx4 v[188:191], v254, s[12:15], s69 offen offset:3072\n"
      "s_xor_b32 s69, s71, 8192\n"
      "buffer_load_dwordx4 v[192:195], v254, s[12:15], s69 offen offset:0\n"
      "s_waitcnt vmcnt(8)\n"
      "v_lshlrev_b32 v32, 16, v160\n"
      "v_and_b32 v33, 0xffff0000, v160\n"
      "v_lshlrev_b32 v34, 16, v161\n"
      "v_and_b32 v35, 0xffff0000, v161\n"
      "v_lshlrev_b32 v36, 16, v162\n"
      "v_and_b32 v37, 0xffff0000, v162\n"
      "v_lshlrev_b32_e64 v38, 16, v163\n"
      "v_and_b32 v39, 0xffff0000, v163\n"
      "v_pk_add_f32 v[20:21], v[32:33], v[34:35]\n"
      "v_pk_mul_f32 v[160:161], v[32:33], v[32:33]\n"
      "v_pk_add_f32 v[28:29], v[36:37], v[38:39]\n"
      "v_pk_fma_f32 v[160:161], v[34:35], v[34:35], v[160:161]\n"
      "v_pk_add_f32 v[20:21], v[20:21], v[28:29]\n"
      "v_pk_fma_f32 v[160:161], v[36:37], v[36:37], v[160:161]\n"
      "v_add_f32 v162, v20, v21\n"
      "v_pk_fma_f32 v[160:161], v[38:39], v[38:39], v[160:161]\n"
      "v_add_f32 v160, v160, v161\n"
      "Lm5_%=:\n"
      "buffer_load_dwordx4 v[196:199], v254, s[12:15], s69 offen offset:1024\n"
      "s_waitcnt vmcnt(8)\n"
      "v_lshlrev_b32 v40, 16, v164\n"
      "v_and_b32 v41, 0xffff0000, v164\n"
      "v_lshlrev_b32 v42, 16, v165\n"
      "v_and_b32 v43, 0xffff0000, v165\n"
      "v_lshlrev_b32 v44, 16, v166\n"
      "v_and_b32 v45, 0xffff0000, v166\n"
      "v_lshlrev_b32_e64 v46, 16, v167\n"
      "v_and_b32 v47, 0xffff0000, v167\n"
      "v_pk_add_f32 v[20:21], v[40:41], v[42:43]\n"
      "v_pk_mul_f32 v[164:165], v[40:41], v[40:41]\n"
      "v_pk_add_f32 v[28:29], v[44:45], v[46:47]\n"
      "v_pk_fma_f32 v[164:165], v[42:43], v[42:43], v[164:165]\n"
      "v_pk_add_f32 v[20:21], v[20:21], v[28:29]\n"
      "v_pk_fma_f32 v[164:165], v[44:45], v[44:45], v[164:165]\n"
      "v_add_f32 v166, v20, v21\n"
      "v_pk_fma_f32 v[164:165], v[46:47], v[46:47], v[164:165]\n"
      "v_add_f32 v164, v164, v165\n"
      "buffer_load_dwordx4 v[200:203], v254, s[12:15], s69 offen offset:2048\n"
      "s_waitcnt vmcnt(8)\n"
      "v_lshlrev_b32 v48, 16, v168\n"
      "v_and_b32 v49, 0xffff0000, v168\n"
      "v_lshlrev_b32 v50, 16, v169\n"
      "v_and_b32 v51, 0xffff0000, v169\n"
      "v_lshlrev_b32 v52, 16, v170\n"
      "v_and_b32 v53, 0xffff0000, v170\n"
      "v_lshlrev_b32_e64 v54, 16, v171\n"
      "v_and_b32 v55, 0xffff0000, v171\n"
      "v_pk_add_f32 v[20:21], v[48:49], v[50:51]\n"
      "v_pk_mul_f32 v[168:169], v[48:49], v[48:49]\n"
      "v_pk_add_f32 v[28:29], v[52:53], v[54:55]\n"
      "v_pk_fma_f32 v[168:169], v[50:51], v[50:51], v[168:169]\n"
      "v_pk_add_f32 v[20:21], v[20:21], v[28:29]\n"
      "v_pk_fma_f32 v[168:169], v[52:53], v[52:53], v[168:169]\n"
      "v_add_f32 v170, v20, v21\n"
      "v_pk_fma_f32 v[168:169], v[54:55], v[54:55], v[168:169]\n"
      "v_add_f32 v168, v168, v169\n"
      "buffer_load_dwordx4 v[204:207], v254, s[12:15], s69 offen offset:3072\n"
      "s_waitcnt vmcnt(8)\n"
      "v_lshlrev_b32 v56, 16, v172\n"
      "v_and_b32 v57, 0xffff0000, v172\n"
      "v_lshlrev_b32 v58, 16, v173\n"
      "v_and_b32 v59, 0xffff0000, v173\n"
      "v_lshlrev_b32 v60, 16, v174\n"
      "v_and_b32 v61, 0xffff0000, v174\n"
      "v_lshlrev_b32_e64 v62, 16, v175\n"
      "v_and_b32 v63, 0xffff0000, v175\n"
      "v_pk_add_f32 v[20:21], v[56:57], v[58:59]\n"
      "v_pk_mul_f32 v[172:173], v[56:57], v[56:57]\n"
      "v_pk_add_f32 v[28:29], v[60:61], v[62:63]\n"
      "v_pk_fma_f32 v[172:173], v[58:59], v[58:59], v[172:173]\n"
      "v_pk_add_f32 v[20:21], v[20:21], v[28:29]\n"
      "v_pk_fma_f32 v[172:173], v[60:61], v[60:61], v[172:173]\n"
      "v_add_f32 v174, v20, v21\n"
      "v_pk_fma_f32 v[172:173], v[62:63], v[62:63], v[172:173]\n"
      "v_add_f32 v172, v172, v173\n"
      "s_xor_b32 s69, s71, 12288\n"
      "buffer_load_dwordx4 v[208:211], v254, s[12:15], s69 offen offset:0\n"
      "s_waitcnt vmcnt(8)\n"
      "v_lshlrev_b32 v64, 16, v176\n"
      "v_and_b32 v65, 0xffff0000, v176\n"
      "v_lshlrev_b32 v66, 16, v177\n"
      "v_and_b32 v67, 0xffff0000, v177\n"
      "v_lshlrev_b32 v68, 16, v178\n"
      "v_and_b32 v69, 0xffff0000, v178\n"
      "v_lshlrev_b32_e64 v70, 16, v179\n"
      "v_and_b32 v71, 0xffff0000, v179\n"
      "v_pk_add_f32 v[20:21], v[64:65], v[66:67]\n"
      "v_pk_mul_f32 v[176:177], v[64:65], v[64:65]\n"
      "v_pk_add_f32 v[28:29], v[68:69], v[70:71]\n"
      "v_pk_fma_f32 v[176:177], v[66:67], v[66:67], v[176:177]\n"
      "v_pk_add_f32 v[20:21], v[20:21], v[28:29]\n"
      "v_pk_fma_f32 v[176:177], v[68:69], v[68:69], v[176:177]\n"
      "v_add_f32 v178, v20, v21\n"
      "v_pk_fma_f32 v[176:177], v[70:71], v[70:71], v[176:177]\n"
      "v_add_f32 v176, v176, v177\n"
      "buffer_load_dwordx4 v[212:215], v254, s[12:15], s69 offen offset:1024\n"
      "s_waitcnt vmcnt(8)\n"
      "v_lshlrev_b32 v72, 16, v180\n"
      "v_and_b32 v73, 0xffff0000, v180\n"
      "v_lshlrev_b32 v74, 16, v181\n"
      "v_and_b32 v75, 0xffff0000, v181\n"
      "v_lshlrev_b32 v76, 16, v182\n"
      "v_and_b32 v77, 0xffff0000, v182\n"
      "v_lshlrev_b32_e64 v78, 16, v183\n"
      "v_and_b32 v79, 0xffff0000, v183\n"
      "v_pk_add_f32 v[20:21], v[72:73], v[74:75]\n"
      "v_pk_mul_f32 v[180:181], v[72:73], v[72:73]\n"
      "v_pk_add_f32 v[28:29], v[76:77], v[78:79]\n"
      "v_pk_fma_f32 v[180:181], v[74:75], v[74:75], v[180:181]\n"
      "v_pk_add_f32 v[20:21], v[20:21], v[28:29]\n"
      "v_pk_fma_f32 v[180:181], v[76:77], v[76:77], v[180:181]\n"
      "v_add_f32 v182, v20, v21\n"
      "v_pk_fma_f32 v[180:181], v[78:79], v[78:79], v[180:181]\n"
      "v_add_f32 v180, v180, v181\n"
      "buffer_load_dwordx4 v[216:219], v254, s[12:15], s69 offen offset:2048\n"
      "s_waitcnt vmcnt(8)\n"
      "v_lshlrev_b32 v80, 16, v184\n"
      "v_and_b32 v81, 0xffff0000, v184\n"
      "v_lshlrev_b32 v82, 16, v185\n"
      "v_and_b32 v83, 0xffff0000, v185\n"
      "v_lshlrev_b32 v84, 16, v186\n"
      "v_and_b32 v85, 0xffff0000, v186\n"
      "v_lshlrev_b32_e64 v86, 16, v187\n"
      "v_and_b32 v87, 0xffff0000, v187\n"
      "v_pk_add_f32 v[20:21], v[80:81], v[82:83]\n"
      "v_pk_mul_f32 v[184:185], v[80:81], v[80:81]\n"
      "v_pk_add_f32 v[28:29], v[84:85], v[86:87]\n"
      "v_pk_fma_f32 v[184:185], v[82:83], v[82:83], v[184:185]\n"
      "v_pk_add_f32 v[20:21], v[20:21], v[28:29]\n"
      "v_pk_fma_f32 v[184:185], v[84:85], v[84:85], v[184:185]\n"
      "v_add_f32 v186, v20, v21\n"
      "v_pk_fma_f32 v[184:185], v[86:87], v[86:87], v[184:185]\n"
      "v_add_f32 v184, v184, v185\n"
      "buffer_load_dwordx4 v[220:223], v254, s[12:15], s69 offen offset:3072\n"
      "s_waitcnt vmcnt(8)\n"
      "v_lshlrev_b32 v88, 16, v188\n"
      "v_and_b32 v89, 0xffff0000, v188\n"
      "v_lshlrev_b32 v90, 16, v189\n"
      "v_and_b32 v91, 0xffff0000, v189\n"
      "v_lshlrev_b32 v92, 16, v190\n"
      "v_and_b32 v93, 0xffff0000, v190\n"
      "v_lshlrev_b32_e64 v94, 16, v191\n"
      "v_and_b32 v95, 0xffff0000, v191\n"
      "v_pk_add_f32 v[20:21], v[88:89], v[90:91]\n"
      "v_pk_mul_f32 v[188:189], v[88:89], v[88:89]\n"
      "v_pk_add_f32 v[28:29], v[92:93], v[94:95]\n"
      "v_pk_fma_f32 v[188:189], v[90:91], v[90:91], v[188:189]\n"
      "v_pk_add_f32 v[20:21], v[20:21], v[28:29]\n"
      "v_pk_fma_f32 v[188:189], v[92:93], v[92:93], v[188:189]\n"
      "v_add_f32 v190, v20, v21\n"
      "v_pk_fma_f32 v[188:189], v[94:95], v[94:95], v[188:189]\n"
      "v_add_f32 v188, v188, v189\n"
      "s_waitcnt vmcnt(7)\n"
      "v_lshlrev_b32 v96, 16, v192\n"
      "v_and_b32 v97, 0xffff0000, v192\n"
      "v_lshlrev_b32 v98, 16, v193\n"
      "v_and_b32 v99, 0xffff0000, v193\n"
      "v_lshlrev_b32 v100, 16, v194\n"
      "s_add_u32 m0, s77, 0\n"
      "s_add_u32 s70, s74, 0\n"
      "buffer_load_dwordx4 v3, s[16:19], s70 offen offset:0 lds\n"
      "v_and_b32 v101, 0xffff0000, v194\n"
      "v_lshlrev_b32_e64 v102, 16, v195\n"
      "v_and_b32 v103, 0xffff0000, v195\n"
      "v_pk_add_f32 v[20:21], v[96:97], v[98:99]\n"
      "v_pk_mul_f32 v[192:193], v[96:97], v[96:97]\n"
      "v_pk_add_f32 v[28:29], v[100:101], v[102:103]\n"
      "v_pk_fma_f32 v[192:193], v[98:99], v[98:99], v[192:193]\n"
      "v_pk_add_f32 v[20:21], v[20:21], v[28:29]\n"
      "v_pk_fma_f32 v[192:193], v[100:101], v[100:101], v[192:193]\n"
      "v_add_f32 v194, v20, v21\n"
      "v_pk_fma_f32 v[192:193], v[102:103], v[102:103], v[192:193]\n"
      "v_add_f32 v192, v192, v193\n"
      "buffer_load_dwordx4 v3, s[16:19], s70 offen offset:1024 lds\n"
      "s_waitcnt vmcnt(8)\n"
      "v_lshlrev_b32 v104, 16, v196\n"
      "v_and_b32 v105, 0xffff0000, v196\n"
      "v_lshlrev_b32 v106, 16, v197\n"
      "v_and_b32 v107, 0xffff0000, v197\n"
      "v_lshlrev_b32 v108, 16, v198\n"
      "v_and_b32 v109, 0xffff0000, v198\n"
      "v_lshlrev_b32_e64 v110, 16, v199\n"
      "v_and_b32 v111, 0xffff0000, v199\n"
      "v_pk_add_f32 v[20:21], v[104:105], v[106:107]\n"
      "v_pk_mul_f32 v[196:197], v[104:105], v[104:105]\n"
      "v_pk_add_f32 v[28:29], v[108:109], v[110:111]\n"
      "buffer_load_dwordx4 v3, s[16:19], s70 offen offset:2048 lds\n"
      "v_pk_fma_f32 v[196:197], v[106:107], v[106:107], v[196:197]\n"
      "v_pk_add_f32 v[20:21], v[20:21], v[28:29]\n"
      "v_pk_fma_f32 v[196:197], v[108:109], v[108:109], v[196:197]\n"
      "v_add_f32 v198, v20, v21\n"
      "v_pk_fma_f32 v[196:197], v[110:111], v[110:111], v[196:197]\n"
      "v_add_f32 v196, v196, v197\n"
      "s_waitcnt vmcnt(8)\n"
      "v_lshlrev_b32 v112, 16, v200\n"
      "v_and_b32 v113, 0xffff0000, v200\n"
      "v_lshlrev_b32 v114, 16, v201\n"
      "v_and_b32 v115, 0xffff0000, v201\n"
      "v_lshlrev_b32 v116, 16, v202\n"
      "s_add_u32 m0, s77, 3072\n"
      "s_add_u32 s70, s74, 24576\n"
      "buffer_load_dwordx4 v248, s[16:19], s70 offen offset:0 lds\n"
      "v_and_b32 v117, 0xffff0000, v202\n"
      "v_lshlrev_b32_e64 v118, 16, v203\n"
      "v_and_b32 v119, 0xffff0000, v203\n"
      "v_pk_add_f32 v[20:21], v[112:113], v[114:115]\n"
      "v_pk_mul_f32 v[200:201], v[112:113], v[112:113]\n"
      "v_pk_add_f32 v[28:29], v[116:117], v[118:119]\n"
      "v_pk_fma_f32 v[200:201], v[114:115], v[114:115], v[200:201]\n"
      "v_pk_add_f32 v[20:21], v[20:21], v[28:29]\n"
      "v_pk_fma_f32 v[200:201], v[116:117], v[116:117], v[200:201]\n"
      "v_add_f32 v202, v20, v21\n"
      "v_pk_fma_f32 v[200:201], v[118:119], v[118:119], v[200:201]\n"
      "v_add_f32 v200, v200, v201\n"
      "buffer_load_dwordx4 v248, s[16:19], s70 offen offset:1024 lds\n"
      "s_waitcnt vmcnt(9)\n"
      "v_lshlrev_b32 v120, 16, v204\n"
      "v_and_b32 v121, 0xffff0000, v204\n"
      "v_lshlrev_b32 v122, 16, v205\n"
      "v_and_b32 v123, 0xffff0000, v205\n"
      "v_lshlrev_b32 v124, 16, v206\n"
      "v_and_b32 v125, 0xffff0000, v206\n"
      "v_lshlrev_b32_e64 v126, 16, v207\n"
      "v_and_b32 v127, 0xffff0000, v207\n"
      "v_pk_add_f32 v[20:21], v[120:121], v[122:123]\n"
      "v_pk_mul_f32 v[204:205], v[120:121], v[120:121]\n"
      "v_pk_add_f32 v[28:29], v[124:125], v[126:127]\n"
      "buffer_load_dwordx4 v248, s[16:19], s70 offen offset:2048 lds\n"
      "v_pk_fma_f32 v[204:205], v[122:123], v[122:123], v[204:205]\n"
      "v_pk_add_f32 v[20:21], v[20:21], v[28:29]\n"
      "v_pk_fma_f32 v[204:205], v[124:125], v[124:125], v[204:205]\n"
      "v_add_f32 v206, v20, v21\n"
      "v_pk_fma_f32 v[204:205], v[126:127], v[126:127], v[204:205]\n"
      "v_add_f32 v204, v204, v205\n"
      "s_waitcnt vmcnt(9)\n"
      "v_lshlrev_b32 v128, 16, v208\n"
      "v_and_b32 v129, 0xffff0000, v208\n"
      "v_lshlrev_b32 v130, 16, v209\n"
      "v_and_b32 v131, 0xffff0000, v209\n"
      "v_lshlrev_b32 v132, 16, v210\n"
      "s_add_u32 m0, s77, 24576\n"
      "s_add_u32 s70, s74, 196608\n"
      "buffer_load_dwordx4 v3, s[16:19], s70 offen offset:0 lds\n"
      "v_and_b32 v133, 0xffff0000, v210\n"
      "v_lshlrev_b32_e64 v134, 16, v211\n"
      "v_and_b32 v135, 0xffff0000, v211\n"
      "v_pk_add_f32 v[20:21], v[128:129], v[130:131]\n"
      "v_pk_mul_f32 v[208:209], v[128:129], v[128:129]\n"
      "v_pk_add_f32 v[28:29], v[132:133], v[134:135]\n"
      "v_pk_fma_f32 v[208:209], v[130:131], v[130:131], v[208:209]\n"
      "v_pk_add_f32 v[20:21], v[20:21], v[28:29]\n"
      "v_pk_fma_f32 v[208:209], v[132:133], v[132:133], v[208:209]\n"
      "v_add_f32 v210, v20, v21\n"
      "v_pk_fma_f32 v[208:209], v[134:135], v[134:135], v[208:209]\n"
      "v_add_f32 v208, v208, v209\n"
      "buffer_load_dwordx4 v3, s[16:19], s70 offen offset:1024 lds\n"
      "s_waitcnt vmcnt(10)\n"
      "v_lshlrev_b32 v136, 16, v212\n"
      "v_and_b32 v137, 0xffff0000, v212\n"
      "v_lshlrev_b32 v138, 16, v213\n"
      "v_and_b32 v139, 0xffff0000, v213\n"
      "v_lshlrev_b32 v140, 16, v214\n"
      "v_and_b32 v141, 0xffff0000, v214\n"
      "v_lshlrev_b32_e64 v142, 16, v215\n"
      "v_and_b32 v143, 0xffff0000, v215\n"
      "v_pk_add_f32 v[20:21], v[136:137], v[138:139]\n"
      "v_pk_mul_f32 v[212:213], v[136:137], v[136:137]\n"
      "v_pk_add_f32 v[28:29], v[140:141], v[142:143]\n"
      "buffer_load_dwordx4 v3, s[16:19], s70 offen offset:2048 lds\n"
      "v_pk_fma_f32 v[212:213], v[138:139], v[138:139], v[212:213]\n"
      "v_pk_add_f32 v[20:21], v[20:21], v[28:29]\n"
      "v_pk_fma_f32 v[212:213], v[140:141], v[140:141], v[212:213]\n"
      "v_add_f32 v214, v20, v21\n"
      "v_pk_fma_f32 v[212:213], v[142:143], v[142:143], v[212:213]\n"
      "v_add_f32 v212, v212, v213\n"
      "s_waitcnt vmcnt(10)\n"
      "v_lshlrev_b32 v144, 16, v216\n"
      "v_and_b32 v145, 0xffff0000, v216\n"
      "v_lshlrev_b32 v146, 16, v217\n"
      "v_and_b32 v147, 0xffff0000, v217\n"
      "v_lshlrev_b32 v148, 16, v218\n"
      "s_add_u32 m0, s77, 27648\n"
      "s_add_u32 s70, s74, 221184\n"
      "buffer_load_dwordx4 v248, s[16:19], s70 offen offset:0 lds\n"
      "v_and_b32 v149, 0xffff0000, v218\n"
      "v_lshlrev_b32_e64 v150, 16, v219\n"
      "v_and_b32 v151, 0xffff0000, v219\n"
      "v_pk_add_f32 v[20:21], v[144:145], v[146:147]\n"
      "v_pk_mul_f32 v[216:217], v[144:145], v[144:145]\n"
      "v_pk_add_f32 v[28:29], v[148:149], v[150:151]\n"
      "v_pk_fma_f32 v[216:217], v[146:147], v[146:147], v[216:217]\n"
      "v_pk_add_f32 v[20:21], v[20:21], v[28:29]\n"
      "v_pk_fma_f32 v[216:217], v[148:149], v[148:149], v[216:217]\n"
      "v_add_f32 v218, v20, v21\n"
      "v_pk_fma_f32 v[216:217], v[150:151], v[150:151], v[216:217]\n"
      "v_add_f32 v216, v216, v217\n"
      "buffer_load_dwordx4 v248, s[16:19], s70 offen offset:1024 lds\n"
      "s_waitcnt vmcnt(11)\n"
      "v_lshlrev_b32 v152, 16, v220\n"
      "v_and_b32 v153, 0xffff0000, v220\n"
      "v_lshlrev_b32 v154, 16, v221\n"
      "v_and_b32 v155, 0xffff0000, v221\n"
      "v_lshlrev_b32 v156, 16, v222\n"
      "v_and_b32 v157, 0xffff0000, v222\n"
      "v_lshlrev_b32_e64 v158, 16, v223\n"
      "v_and_b32 v159, 0xffff0000, v223\n"
      "v_pk_add_f32 v[20:21], v[152:153], v[154:155]\n"
      "v_pk_mul_f32 v[220:221], v[152:153], v[152:153]\n"
      "v_pk_add_f32 v[28:29], v[156:157], v[158:159]\n"
      "buffer_load_dwordx4 v248, s[16:19], s70 offen offset:2048 lds\n"
      "v_pk_fma_f32 v[220:221], v[154:155], v[154:155], v[220:221]\n"
      "v_pk_add_f32 v[20:21], v[20:21], v[28:29]\n"
      "v_pk_fma_f32 v[220:221], v[156:157], v[156:157], v[220:221]\n"
      "v_add_f32 v222, v20, v21\n"
      "v_pk_fma_f32 v[220:221], v[158:159], v[158:159], v[220:221]\n"
      "v_add_f32 v220, v220, v221\n"
      "v_permlane32_swap_b32 v162, v194\n"
      "v_permlane32_swap_b32 v166, v198\n"
      "v_permlane32_swap_b32 v170, v202\n"
      "v_permlane32_swap_b32 v174, v206\n"
      "v_permlane32_swap_b32 v178, v210\n"
      "v_permlane32_swap_b32 v182, v214\n"
      "s_add_u32 m0, s77, 49152\n"
      "s_add_u32 s70, s74, 393216\n"
      "buffer_load_dwordx4 v3, s[16:19], s70 offen offset:0 lds\n"
      "v_permlane32_swap_b32 v186, v218\n"
      "v_permlane32_swap_b32 v190, v222\n"
      "v_add_f32 v162, v162, v194\n"
      "v_add_f32 v166, v166, v198\n"
      "v_add_f32 v170, v170, v202\n"
      "v_add_f32 v174, v174, v206\n"
      "v_add_f32 v178, v178, v210\n"
      "v_add_f32 v182, v182, v214\n"
      "v_add_f32 v186, v186, v218\n"
      "v_add_f32 v190, v190, v222\n"
      "v_permlane16_swap_b32 v162, v178\n"
      "v_permlane16_swap_b32 v166, v182\n"
      "buffer_load_dwordx4 v3, s[16:19], s70 offen offset:1024 lds\n"
      "v_permlane16_swap_b32 v170, v186\n"
      "v_permlane16_swap_b32 v174, v190\n"
      "v_add_f32 v162, v162, v178\n"
      "v_add_f32 v166, v166, v182\n"
      "v_add_f32 v170, v170, v186\n"
      "v_add_f32 v174, v174, v190\n"
      "v_permlane32_swap_b32 v160, v192\n"
      "v_permlane32_swap_b32 v164, v196\n"
      "v_permlane32_swap_b32 v168, v200\n"
      "v_permlane32_swap_b32 v172, v204\n"
      "v_permlane32_swap_b32 v176, v208\n"
      "v_permlane32_swap_b32 v180, v212\n"
      "buffer_load_dwordx4 v3, s[16:19], s70 offen offset:2048 lds\n"
      "v_permlane32_swap_b32 v184, v216\n"
      "v_permlane32_swap_b32 v188, v220\n"
      "v_add_f32 v160, v160, v192\n"
      "v_add_f32 v164, v164, v196\n"
      "v_add_f32 v168, v168, v200\n"
      "v_add_f32 v172, v172, v204\n"
      "v_add_f32 v176, v176, v208\n"
      "v_add_f32 v180, v180, v212\n"
      "v_add_f32 v184, v184, v216\n"
      "v_add_f32 v188, v188, v220\n"
      "v_permlane16_swap_b32 v160, v176\n"
      "v_permlane16_swap_b32 v164, v180\n"
      "s_add_u32 m0, s77, 52224\n"
      "s_add_u32 s70, s74, 417792\n"
      "buffer_load_dwordx4 v248, s[16:19], s70 offen offset:0 lds\n"
      "v_permlane16_swap_b32 v168, v184\n"
      "v_permlane16_swap_b32 v172, v188\n"
      "v_add_f32 v160, v160, v176\n"
      "v_add_f32 v164, v164, v180\n"
      "v_add_f32 v168, v168, v184\n"
      "v_add_f32 v172, v172, v188\n"
      "v_add_f32_dpp v162, v162, v162 row_ror:8 row_mask:0xf bank_mask:0xf\n"
      "v_add_f32_dpp v166, v166, v166 row_ror:8 row_mask:0xf bank_mask:0xf\n"
      "v_add_f32_dpp v170, v170, v170 row_ror:8 row_mask:0xf bank_mask:0xf\n"
      "v_add_f32_dpp v174, v174, v174 row_ror:8 row_mask:0xf bank_mask:0xf\n"
      "v_add_f32_dpp v160, v160, v160 row_ror:8 row_mask:0xf bank_mask:0xf\n"
      "v_add_f32_dpp v164, v164, v164 row_ror:8 row_mask:0xf bank_mask:0xf\n"
      "buffer_load_dwordx4 v248, s[16:19], s70 offen offset:1024 lds\n"
      "v_add_f32_dpp v168, v168, v168 row_ror:8 row_mask:0xf bank_mask:0xf\n"
      "v_add_f32_dpp v172, v172, v172 row_ror:8 row_mask:0xf bank_mask:0xf\n"
      "v_add_f32_dpp v162, v162, v162 row_ror:4 row_mask:0xf bank_mask:0xf\n"
      "v_add_f32_dpp v166, v166, v166 row_ror:4 row_mask:0xf bank_mask:0xf\n"
      "v_add_f32_dpp v170, v170, v170 row_ror:4 row_mask:0xf bank_mask:0xf\n"
      "Lhs0_0_%=:\n"
      "s_cbranch_execz Lhr0_%=\n"
      "s_nop 0\n"
      "v_add_f32_dpp v174, v174, v174 row_ror:4 row_mask:0xf bank_mask:0xf\n"
      "v_add_f32_dpp v160, v160, v160 row_ror:4 row_mask:0xf bank_mask:0xf\n"
      "v_add_f32_dpp v164, v164, v164 row_ror:4 row_mask:0xf bank_mask:0xf\n"
      "v_add_f32_dpp v168, v168, v168 row_ror:4 row_mask:0xf bank_mask:0xf\n"
      "v_add_f32_dpp v172, v172, v172 row_ror:4 row_mask:0xf bank_mask:0xf\n"
      "v_add_f32_dpp v162, v162, v162 row_ror:2 row_mask:0xf bank_mask:0xf\n"
      "v_add_f32_dpp v166, v166, v166 row_ror:2 row_mask:0xf bank_mask:0xf\n"
      "buffer_load_dwordx4 v248, s[16:19], s70 offen offset:2048 lds\n"
      "v_add_f32_dpp v170, v170, v170 row_ror:2 row_mask:0xf bank_mask:0xf\n"
      "v_add_f32_dpp v174, v174, v174 row_ror:2 row_mask:0xf bank_mask:0xf\n"
      "v_add_f32_dpp v160, v160, v160 row_ror:2 row_mask:0xf bank_mask:0xf\n"
      "v_add_f32_dpp v164, v164, v164 row_ror:2 row_mask:0xf bank_mask:0xf\n"
      "v_add_f32_dpp v168, v168, v168 row_ror:2 row_mask:0xf bank_mask:0xf\n"
      "v_add_f32_dpp v172, v172, v172 row_ror:2 row_mask:0xf bank_mask:0xf\n"
      "v_add_f32_dpp v162, v162, v162 row_ror:1 row_mask:0xf bank_mask:0xf\n"
      "v_add_f32_dpp v166, v166, v166 row_ror:1 row_mask:0xf bank_mask:0xf\n"
      "v_add_f32_dpp v170, v170, v170 row_ror:1 row_mask:0xf bank_mask:0xf\n"
      "v_add_f32_dpp v174, v174, v174 row_ror:1 row_mask:0xf bank_mask:0xf\n"
      "v_add_f32_dpp v160, v160, v160 row_ror:1 row_mask:0xf bank_mask:0xf\n"
      "v_add_f32_dpp v164, v164, v164 row_ror:1 row_mask:0xf bank_mask:0xf\n"
      "s_add_u32 m0, s77, 73728\n"
      "s_add_u32 s70, s74, 589824\n"
      "buffer_load_dwordx4 v3, s[16:19], s70 offen offset:0 lds\n"
      "v_add_f32_dpp v168, v168, v168 row_ror:1 row_mask:0xf bank_mask:0xf\n"
      "v_add_f32_dpp v172, v172, v172 row_ror:1 row_mask:0xf bank_mask:0xf\n"
      "v_mul_f32 v162, 0x3b000000, v162\n"
      "v_mul_f32 v160, 0x3b000000, v160\n"
      "v_mul_f32 v166, 0x3b000000, v166\n"
      "v_mul_f32 v164, 0x3b000000, v164\n"
      "v_mul_f32 v170, 0x3b000000, v170\n"
      "v_mul_f32 v168, 0x3b000000, v168\n"
      "v_mul_f32 v174, 0x3b000000, v174\n"
      "v_mul_f32 v172, 0x3b000000, v172\n"
      "v_fma_f32 v160, -v162, v162, v160\n"
      "v_fma_f32 v164, -v166, v166, v164\n"
      "buffer_load_dwordx4 v3, s[16:19], s70 offen offset:1024 lds\n"
      "v_fma_f32 v168, -v170, v170, v168\n"
      "v_fma_f32 v172, -v174, v174, v172\n"
      "v_max_f32 v160, 0, v160\n"
      "v_max_f32 v164, 0, v164\n"
      "v_max_f32 v168, 0, v168\n"
      "v_max_f32 v172, 0, v172\n"
      "v_add_f32 v160, 0x3727c5ac, v160\n"
      "v_add_f32 v164, 0x3727c5ac, v164\n"
      "v_add_f32 v168, 0x3727c5ac, v168\n"
      "v_add_f32 v172, 0x3727c5ac, v172\n"
      "v_rsq_f32 v160, v160\n"
      "v_rsq_f32 v164, v164\n"
      "buffer_load_dwordx4 v3, s[16:19], s70 offen offset:2048 lds\n"
      "v_rsq_f32 v168, v168\n"
      "v_rsq_f32 v172, v172\n"
      "v_mul_f32 v162, -v162, v160\n"
      "v_mul_f32 v166, -v166, v164\n"
      "v_mul_f32 v170, -v170, v168\n"
      "v_mul_f32 v174, -v174, v172\n"
      "v_lshlrev_b32 v20, 5, v255\n"
      "s_mov_b32 s63, 53248\n"
      "s_nop 0\n"
      "v_readlane_b32 s80, v162, 0\n"
      "v_readlane_b32 s81, v160, 0\n"
      "v_readlane_b32 s82, v166, 0\n"
      "v_readlane_b32 s83, v164, 0\n"
      "s_add_u32 m0, s77, 76800\n"
      "s_add_u32 s70, s74, 614400\n"
      "buffer_load_dwordx4 v248, s[16:19], s70 offen offset:0 lds\n"
      "v_readlane_b32 s84, v170, 0\n"
      "v_readlane_b32 s85, v168, 0\n"
      "v_readlane_b32 s86, v174, 0\n"
      "v_readlane_b32 s87, v172, 0\n"
      "v_pk_fma_f32 v[32:33], v[32:33], s[80:81], s[80:81] op_sel:[0,1,0] op_sel_hi:[1,1,0]\n"
      "v_pk_fma_f32 v[34:35], v[34:35], s[80:81], s[80:81] op_sel:[0,1,0] op_sel_hi:[1,1,0]\n"
      "v_pk_fma_f32 v[36:37], v[36:37], s[80:81], s[80:81] op_sel:[0,1,0] op_sel_hi:[1,1,0]\n"
      "v_pk_fma_f32 v[38:39], v[38:39], s[80:81], s[80:81] op_sel:[0,1,0] op_sel_hi:[1,1,0]\n"
      "v_pk_fma_f32 v[40:41], v[40:41], s[82:83], s[82:83] op_sel:[0,1,0] op_sel_hi:[1,1,0]\n"
      "v_pk_fma_f32 v[42:43], v[42:43], s[82:83], s[82:83] op_sel:[0,1,0] op_sel_hi:[1,1,0]\n"
      "v_pk_fma_f32 v[44:45], v[44:45], s[82:83], s[82:83] op_sel:[0,1,0] op_sel_hi:[1,1,0]\n"
      "v_pk_fma_f32 v[46:47], v[46:47], s[82:83], s[82:83] op_sel:[0,1,0] op_sel_hi:[1,1,0]\n"
      "buffer_load_dwordx4 v248, s[16:19], s70 offen offset:1024 lds\n"
      "v_pk_fma_f32 v[48:49], v[48:49], s[84:85], s[84:85] op_sel:[0,1,0] op_sel_hi:[1,1,0]\n"
      "v_pk_fma_f32 v[50:51], v[50:51], s[84:85], s[84:85] op_sel:[0,1,0] op_sel_hi:[1,1,0]\n"
      "v_pk_fma_f32 v[52:53], v[52:53], s[84:85], s[84:85] op_sel:[0,1,0] op_sel_hi:[1,1,0]\n"
      "v_pk_fma_f32 v[54:55], v[54:55], s[84:85], s[84:85] op_sel:[0,1,0] op_sel_hi:[1,1,0]\n"
      "v_pk_fma_f32 v[56:57], v[56:57], s[86:87], s[86:87] op_sel:[0,1,0] op_sel_hi:[1,1,0]\n"
      "v_pk_fma_f32 v[58:59], v[58:59], s[86:87], s[86:87] op_sel:[0,1,0] op_sel_hi:[1,1,0]\n"
      "v_pk_fma_f32 v[60:61], v[60:61], s[86:87], s[86:87] op_sel:[0,1,0] op_sel_hi:[1,1,0]\n"
      "v_pk_fma_f32 v[62:63], v[62:63], s[86:87], s[86:87] op_sel:[0,1,0] op_sel_hi:[1,1,0]\n"
      "v_readlane_b32 s80, v162, 16\n"
      "v_readlane_b32 s81, v160, 16\n"
      "v_readlane_b32 s82, v166, 16\n"
      "v_readlane_b32 s83, v164, 16\n"
      "buffer_load_dwordx4 v248, s[16:19], s70 offen offset:2048 lds\n"
      "v_readlane_b32 s84, v170, 16\n"
      "v_readlane_b32 s85, v168, 16\n"
      "v_readlane_b32 s86, v174, 16\n"
      "v_readlane_b32 s87, v172, 16\n"
      "v_pk_fma_f32 v[64:65], v[64:65], s[80:81], s[80:81] op_sel:[0,1,0] op_sel_hi:[1,1,0]\n"
      "v_pk_fma_f32 v[66:67], v[66:67], s[80:81], s[80:81] op_sel:[0,1,0] op_sel_hi:[1,1,0]\n"
      "v_pk_fma_f32 v[68:69], v[68:69], s[80:81], s[80:81] op_sel:[0,1,0] op_sel_hi:[1,1,0]\n"
      "v_pk_fma_f32 v[70:71], v[70:71], s[80:81], s[80:81] op_sel:[0,1,0] op_sel_hi:[1,1,0]\n"
      "v_pk_fma_f32 v[72:73], v[72:73], s[82:83], s[82:83] op_sel:[0,1,0] op_sel_hi:[1,1,0]\n"
      "v_pk_fma_f32 v[74:75], v[74:75], s[82:83], s[82:83] op_sel:[0,1,0] op_sel_hi:[1,1,0]\n"
      "v_pk_fma_f32 v[76:77], v[76:77], s[82:83], s[82:83] op_sel:[0,1,0] op_sel_hi:[1,1,0]\n"
      "v_pk_fma_f32 v[78:79], v[78:79], s[82:83], s[82:83] op_sel:[0,1,0] op_sel_hi:[1,1,0]\n"
      "v_pk_fma_f32 v[80:81], v[80:81], s[84:85], s[84:85] op_sel:[0,1,0] op_sel_hi:[1,1,0]\n"
      "v_pk_fma_f32 v[82:83], v[82:83], s[84:85], s[84:85] op_sel:[0,1,0] op_sel_hi:[1,1,0]\n"
      "v_pk_fma_f32 v[84:85], v[84:85], s[84:85], s[84:85] op_sel:[0,1,0] op_sel_hi:[1,1,0]\n"
      "v_pk_fma_f32 v[86:87], v[86:87], s[84:85], s[84:85] op_sel:[0,1,0] op_sel_hi:[1,1,0]\n"
      "v_pk_fma_f32 v[88:89], v[88:89], s[86:87], s[86:87] op_sel:[0,1,0] op_sel_hi:[1,1,0]\n"
      "v_pk_fma_f32 v[90:91], v[90:91], s[86:87], s[86:87] op_sel:[0,1,0] op_sel_hi:[1,1,0]\n"
      "v_pk_fma_f32 v[92:93], v[92:93], s[86:87], s[86:87] op_sel:[0,1,0] op_sel_hi:[1,1,0]\n"
      "v_pk_fma_f32 v[94:95], v[94:95], s[86:87], s[86:87] op_sel:[0,1,0] op_sel_hi:[1,1,0]\n"
      "buffer_load_dwordx4 v[176:179], v20, s[8:11], s63 offen offset:0 sc1\n"
      "buffer_load_dwordx4 v[180:183], v20, s[8:11], s63 offen offset:16 sc1\n"
      "buffer_load_dwordx4 v[184:187], v20, s[8:11], s63 offen offset:2048 sc1\n"
      "buffer_load_dwordx4 v[188:191], v20, s[8:11], s63 offen offset:2064 sc1\n"
      "v_readlane_b32 s80, v162, 32\n"
      "v_readlane_b32 s81, v160, 32\n"
      "v_readlane_b32 s82, v166, 32\n"
      "v_readlane_b32 s83, v164, 32\n"
      "v_readlane_b32 s84, v170, 32\n"
      "v_readlane_b32 s85, v168, 32\n"
      "v_readlane_b32 s86, v174, 32\n"
      "v_readlane_b32 s87, v172, 32\n"
      "v_pk_fma_f32 v[96:97], v[96:97], s[80:81], s[80:81] op_sel:[0,1,0] op_sel_hi:[1,1,0]\n"
      "v_pk_fma_f32 v[98:99], v[98:99], s[80:81], s[80:81] op_sel:[0,1,0] op_sel_hi:[1,1,0]\n"
      "v_pk_fma_f32 v[100:101], v[100:101], s[80:81], s[80:81] op_sel:[0,1,0] op_sel_hi:[1,1,0]\n"
      "v_pk_fma_f32 v[102:103], v[102:103], s[80:81], s[80:81] op_sel:[0,1,0] op_sel_hi:[1,1,0]\n"
      "v_pk_fma_f32 v[104:105], v[104:105], s[82:83], s[82:83] op_sel:[0,1,0] op_sel_hi:[1,1,0]\n"
      "v_pk_fma_f32 v[106:107], v[106:107], s[82:83], s[82:83] op_sel:[0,1,0] op_sel_hi:[1,1,0]\n"
      "v_pk_fma_f32 v[108:109], v[108:109], s[82:83], s[82:83] op_sel:[0,1,0] op_sel_hi:[1,1,0]\n"
      "v_pk_fma_f32 v[110:111], v[110:111], s[82:83], s[82:83] op_sel:[0,1,0] op_sel_hi:[1,1,0]\n"
      "v_pk_fma_f32 v[112:113], v[112:113], s[84:85], s[84:85] op_sel:[0,1,0] op_sel_hi:[1,1,0]\n"
      "v_pk_fma_f32 v[114:115], v[114:115], s[84:85], s[84:85] op_sel:[0,1,0] op_sel_hi:[1,1,0]\n"
      "v_pk_fma_f32 v[116:117], v[116:117], s[84:85], s[84:85] op_sel:[0,1,0] op_sel_hi:[1,1,0]\n"
      "v_pk_fma_f32 v[118:119], v[118:119], s[84:85], s[84:85] op_sel:[0,1,0] op_sel_hi:[1,1,0]\n"
      "v_pk_fma_f32 v[120:121], v[120:121], s[86:87], s[86:87] op_sel:[0,1,0] op_sel_hi:[1,1,0]\n"
      "v_pk_fma_f32 v[122:123], v[122:123], s[86:87], s[86:87] op_sel:[0,1,0] op_sel_hi:[1,1,0]\n"
      "v_pk_fma_f32 v[124:125], v[124:125], s[86:87], s[86:87] op_sel:[0,1,0] op_sel_hi:[1,1,0]\n"
      "v_pk_fma_f32 v[126:127], v[126:127], s[86:87], s[86:87] op_sel:[0,1,0] op_sel_hi:[1,1,0]\n"
      "v_readlane_b32 s80, v162, 48\n"
      "v_readlane_b32 s81, v160, 48\n"
      "v_readlane_b32 s82, v166, 48\n"
      "v_readlane_b32 s83, v164, 48\n"
      "v_readlane_b32 s84, v170, 48\n"
      "v_readlane_b32 s85, v168, 48\n"
      "v_readlane_b32 s86, v174, 48\n"
      "v_readlane_b32 s87, v172, 48\n"
      "v_pk_fma_f32 v[128:129], v[128:129], s[80:81], s[80:81] op_sel:[0,1,0] op_sel_hi:[1,1,0]\n"
      "v_pk_fma_f32 v[130:131], v[130:131], s[80:81], s[80:81] op_sel:[0,1,0] op_sel_hi:[1,1,0]\n"
      "v_pk_fma_f32 v[132:133], v[132:133], s[80:81], s[80:81] op_sel:[0,1,0] op_sel_hi:[1,1,0]\n"
      "v_pk_fma_f32 v[134:135], v[134:135], s[80:81], s[80:81] op_sel:[0,1,0] op_sel_hi:[1,1,0]\n"
      "v_pk_fma_f32 v[136:137], v[136:137], s[82:83], s[82:83] op_sel:[0,1,0] op_sel_hi:[1,1,0]\n"
      "v_pk_fma_f32 v[138:139], v[138:139], s[82:83], s[82:83] op_sel:[0,1,0] op_sel_hi:[1,1,0]\n"
      "v_pk_fma_f32 v[140:141], v[140:141], s[82:83], s[82:83] op_sel:[0,1,0] op_sel_hi:[1,1,0]\n"
      "v_pk_fma_f32 v[142:143], v[142:143], s[82:83], s[82:83] op_sel:[0,1,0] op_sel_hi:[1,1,0]\n"
      "v_pk_fma_f32 v[144:145], v[144:145], s[84:85], s[84:85] op_sel:[0,1,0] op_sel_hi:[1,1,0]\n"
      "v_pk_fma_f32 v[146:147], v[146:147], s[84:85], s[84:85] op_sel:[0,1,0] op_sel_hi:[1,1,0]\n"
      "v_pk_fma_f32 v[148:149], v[148:149], s[84:85], s[84:85] op_sel:[0,1,0] op_sel_hi:[1,1,0]\n"
      "v_pk_fma_f32 v[150:151], v[150:151], s[84:85], s[84:85] op_sel:[0,1,0] op_sel_hi:[1,1,0]\n"
      "v_pk_fma_f32 v[152:153], v[152:153], s[86:87], s[86:87] op_sel:[0,1,0] op_sel_hi:[1,1,0]\n"
      "v_pk_fma_f32 v[154:155], v[154:155], s[86:87], s[86:87] op_sel:[0,1,0] op_sel_hi:[1,1,0]\n"
      "v_pk_fma_f32 v[156:157], v[156:157], s[86:87], s[86:87] op_sel:[0,1,0] op_sel_hi:[1,1,0]\n"
      "v_pk_fma_f32 v[158:159], v[158:159], s[86:87], s[86:87] op_sel:[0,1,0] op_sel_hi:[1,1,0]\n"
      "Lp7_%=:\n"
      "s_waitcnt vmcnt(0)\n"
      "v_xor_b32 v22, s56, v177\n"
      "v_bitop3_b32 v22, s56, v181, v22 bitop3:0xbe\n"
      "v_bitop3_b32 v22, s56, v185, v22 bitop3:0xbe\n"
      "v_bitop3_b32 v22, s56, v189, v22 bitop3:0xbe\n"
      "v_bitop3_b32 v22, s57, v179, v22 bitop3:0xbe\n"
      "v_bitop3_b32 v22, s57, v183, v22 bitop3:0xbe\n"
      "v_bitop3_b32 v22, s57, v187, v22 bitop3:0xbe\n"
      "v_bitop3_b32 v22, s57, v191, v22 bitop3:0xbe\n"
      "v_cmp_ne_u32 vcc, 0, v22\n"
      "s_nop 0\n"
      "s_cmp_eq_u64 vcc, 0\n"
      "s_cbranch_scc1 Lpd8_%=\n"
      "s_cmp_eq_u32 s62, 0\n"
      "s_cbranch_scc1 Lpd8_%=\n"
      "s_sub_u32 s62, s62, 1\n"
      "s_sleep 1\n"
      "buffer_load_dwordx4 v[176:179], v20, s[8:11], s63 offen offset:0 sc1\n"
      "buffer_load_dwordx4 v[180:183], v20, s[8:11], s63 offen offset:16 sc1\n"
      "buffer_load_dwordx4 v[184:187], v20, s[8:11], s63 offen offset:2048 sc1\n"
      "buffer_load_dwordx4 v[188:191], v20, s[8:11], s63 offen offset:2064 sc1\n"
      "s_branch Lp7_%=\n"
      "Lpd8_%=:\n"
      "v_lshlrev_b32 v160, 16, v176\n"
      "v_and_b32 v161, 0xffff0000, v176\n"
      "v_lshlrev_b32 v168, 16, v184\n"
      "v_and_b32 v169, 0xffff0000, v184\n"
      "v_lshlrev_b32 v162, 16, v178\n"
      "v_and_b32 v163, 0xffff0000, v178\n"
      "v_lshlrev_b32 v170, 16, v186\n"
      "v_and_b32 v171, 0xffff0000, v186\n"
      "v_lshlrev_b32 v164, 16, v180\n"
      "v_and_b32 v165, 0xffff0000, v180\n"
      "v_lshlrev_b32 v172, 16, v188\n"
      "v_and_b32 v173, 0xffff0000, v188\n"
      "v_lshlrev_b32 v166, 16, v182\n"
      "v_and_b32 v167, 0xffff0000, v182\n"
      "v_lshlrev_b32_e64 v174, 16, v190\n"
      "v_and_b32 v175, 0xffff0000, v190\n"
      "v_pk_fma_f32 v[32:33], v[32:33], v[168:169], v[160:161]\n"
      "v_pk_fma_f32 v[34:35], v[34:35], v[170:171], v[162:163]\n"
      "v_pk_fma_f32 v[36:37], v[36:37], v[172:173], v[164:165]\n"
      "v_pk_fma_f32 v[38:39], v[38:39], v[174:175], v[166:167]\n"
      "v_cvt_pk_bf16_f32 v192, v32, v33\n"
      "v_cvt_pk_bf16_f32 v193, v34, v35\n"
      "v_cvt_pk_bf16_f32 v194, v36, v37\n"
      "v_cvt_pk_bf16_f32 v195, v38, v39\n"
      "ds_write_b128 v8, v[192:195]\n"
      "buffer_load_dword v250, v4, s[16:19], s99 offen\n"
      "v_pk_fma_f32 v[40:41], v[40:41], v[168:169], v[160:161]\n"
      "v_pk_fma_f32 v[42:43], v[42:43], v[170:171], v[162:163]\n"
      "v_pk_fma_f32 v[44:45], v[44:45], v[172:173], v[164:165]\n"
      "v_pk_fma_f32 v[46:47], v[46:47], v[174:175], v[166:167]\n"
      "v_cvt_pk_bf16_f32 v196, v40, v41\n"
      "v_cvt_pk_bf16_f32 v197, v42, v43\n"
      "v_cvt_pk_bf16_f32 v198, v44, v45\n"
      "v_cvt_pk_bf16_f32 v199, v46, v47\n"
      "ds_write_b128 v9, v[196:199]\n"
      "v_pk_fma_f32 v[48:49], v[48:49], v[168:169], v[160:161]\n"
      "v_pk_fma_f32 v[50:51], v[50:51], v[170:171], v[162:163]\n"
      "v_pk_fma_f32 v[52:53], v[52:53], v[172:173], v[164:165]\n"
      "v_pk_fma_f32 v[54:55], v[54:55], v[174:175], v[166:167]\n"
      "v_cvt_pk_bf16_f32 v200, v48, v49\n"
      "v_cvt_pk_bf16_f32 v201, v50, v51\n"
      "v_cvt_pk_bf16_f32 v202, v52, v53\n"
      "v_cvt_pk_bf16_f32 v203, v54, v55\n"
      "ds_write_b128 v10, v[200:203]\n"
      "buffer_load_dword v250, v23, s[36:39], s97 offen offset:2048\n"
      "v_pk_fma_f32 v[56:57], v[56:57], v[168:169], v[160:161]\n"
      "v_pk_fma_f32 v[58:59], v[58:59], v[170:171], v[162:163]\n"
      "v_pk_fma_f32 v[60:61], v[60:61], v[172:173], v[164:165]\n"
      "v_pk_fma_f32 v[62:63], v[62:63], v[174:175], v[166:167]\n"
      "v_cvt_pk_bf16_f32 v204, v56, v57\n"
      "v_cvt_pk_bf16_f32 v205, v58, v59\n"
      "v_cvt_pk_bf16_f32 v206, v60, v61\n"
      "v_cvt_pk_bf16_f32 v207, v62, v63\n"
      "ds_write_b128 v11, v[204:207]\n"
      "v_pk_fma_f32 v[64:65], v[64:65], v[168:169], v[160:161]\n"
      "v_pk_fma_f32 v[66:67], v[66:67], v[170:171], v[162:163]\n"
      "v_pk_fma_f32 v[68:69], v[68:69], v[172:173], v[164:165]\n"
      "v_pk_fma_f32 v[70:71], v[70:71], v[174:175], v[166:167]\n"
      "v_cvt_pk_bf16_f32 v208, v64, v65\n"
      "v_cvt_pk_bf16_f32 v209, v66, v67\n"
      "v_cvt_pk_bf16_f32 v210, v68, v69\n"
      "v_cvt_pk_bf16_f32 v211, v70, v71\n"
      "ds_write_b128 v12, v[208:211]\n"
      "buffer_load_dwordx4 v[232:235], v224, s[24:27], s92 offen\n"
      "v_pk_fma_f32 v[72:73], v[72:73], v[168:169], v[160:161]\n"
      "v_pk_fma_f32 v[74:75], v[74:75], v[170:171], v[162:163]\n"
      "v_pk_fma_f32 v[76:77], v[76:77], v[172:173], v[164:165]\n"
      "v_pk_fma_f32 v[78:79], v[78:79], v[174:175], v[166:167]\n"
      "v_cvt_pk_bf16_f32 v212, v72, v73\n"
      "v_cvt_pk_bf16_f32 v213, v74, v75\n"
      "v_cvt_pk_bf16_f32 v214, v76, v77\n"
      "v_cvt_pk_bf16_f32 v215, v78, v79\n"
      "ds_write_b128 v13, v[212:215]\n"
      "v_pk_fma_f32 v[80:81], v[80:81], v[168:169], v[160:161]\n"
      "v_pk_fma_f32 v[82:83], v[82:83], v[170:171], v[162:163]\n"
      "v_pk_fma_f32 v[84:85], v[84:85], v[172:173], v[164:165]\n"
      "v_pk_fma_f32 v[86:87], v[86:87], v[174:175], v[166:167]\n"
      "v_cvt_pk_bf16_f32 v216, v80, v81\n"
      "v_cvt_pk_bf16_f32 v217, v82, v83\n"
      "v_cvt_pk_bf16_f32 v218, v84, v85\n"
      "v_cvt_pk_bf16_f32 v219, v86, v87\n"
      "ds_write_b128 v14, v[216:219]\n"
      "buffer_load_dwordx4 v[236:239], v225, s[24:27], s93 offen\n"
      "v_pk_fma_f32 v[88:89], v[88:89], v[168:169], v[160:161]\n"
      "v_pk_fma_f32 v[90:91], v[90:91], v[170:171], v[162:163]\n"
      "v_pk_fma_f32 v[92:93], v[92:93], v[172:173], v[164:165]\n"
      "v_pk_fma_f32 v[94:95], v[94:95], v[174:175], v[166:167]\n"
      "v_cvt_pk_bf16_f32 v220, v88, v89\n"
      "v_cvt_pk_bf16_f32 v221, v90, v91\n"
      "v_cvt_pk_bf16_f32 v222, v92, v93\n"
      "v_cvt_pk_bf16_f32 v223, v94, v95\n"
      "ds_write_b128 v15, v[220:223]\n"
      "v_pk_fma_f32 v[96:97], v[96:97], v[168:169], v[160:161]\n"
      "v_pk_fma_f32 v[98:99], v[98:99], v[170:171], v[162:163]\n"
      "v_pk_fma_f32 v[100:101], v[100:101], v[172:173], v[164:165]\n"
      "v_pk_fma_f32 v[102:103], v[102:103], v[174:175], v[166:167]\n"
      "v_cvt_pk_bf16_f32 v192, v96, v97\n"
      "v_cvt_pk_bf16_f32 v193, v98, v99\n"
      "v_cvt_pk_bf16_f32 v194, v100, v101\n"
      "v_cvt_pk_bf16_f32 v195, v102, v103\n"
      "v_xor_b32 v8, 128, v8\n"
      "ds_write_b128 v8, v[192:195]\n"
      "v_pk_fma_f32 v[104:105], v[104:105], v[168:169], v[160:161]\n"
      "v_pk_fma_f32 v[106:107], v[106:107], v[170:171], v[162:163]\n"
      "v_pk_fma_f32 v[108:109], v[108:109], v[172:173], v[164:165]\n"
      "v_pk_fma_f32 v[110:111], v[110:111], v[174:175], v[166:167]\n"
      "v_cvt_pk_bf16_f32 v196, v104, v105\n"
      "v_cvt_pk_bf16_f32 v197, v106, v107\n"
      "v_cvt_pk_bf16_f32 v198, v108, v109\n"
      "v_cvt_pk_bf16_f32 v199, v110, v111\n"
      "v_xor_b32 v9, 128, v9\n"
      "ds_write_b128 v9, v[196:199]\n"
      "v_pk_fma_f32 v[112:113], v[112:113], v[168:169], v[160:161]\n"
      "v_pk_fma_f32 v[114:115], v[114:115], v[170:171], v[162:163]\n"
      "v_pk_fma_f32 v[116:117], v[116:117], v[172:173], v[164:165]\n"
      "v_pk_fma_f32 v[118:119], v[118:119], v[174:175], v[166:167]\n"
      "v_cvt_pk_bf16_f32 v200, v112, v113\n"
      "v_cvt_pk_bf16_f32 v201, v114, v115\n"
      "v_cvt_pk_bf16_f32 v202, v116, v117\n"
      "v_cvt_pk_bf16_f32 v203, v118, v119\n"
      "v_xor_b32 v10, 128, v10\n"
      "ds_write_b128 v10, v[200:203]\n"
      "v_pk_fma_f32 v[120:121], v[120:121], v[168:169], v[160:161]\n"
      "v_pk_fma_f32 v[122:123], v[122:123], v[170:171], v[162:163]\n"
      "v_pk_fma_f32 v[124:125], v[124:125], v[172:173], v[164:165]\n"
      "v_pk_fma_f32 v[126:127], v[126:127], v[174:175], v[166:167]\n"
      "v_cvt_pk_bf16_f32 v204, v120, v121\n"
      "v_cvt_pk_bf16_f32 v205, v122, v123\n"
      "v_cvt_pk_bf16_f32 v206, v124, v125\n"
      "v_cvt_pk_bf16_f32 v207, v126, v127\n"
      "v_xor_b32 v11, 128, v11\n"
      "ds_write_b128 v11, v[204:207]\n"
      "v_pk_fma_f32 v[128:129], v[128:129], v[168:169], v[160:161]\n"
      "v_pk_fma_f32 v[130:131], v[130:131], v[170:171], v[162:163]\n"
      "v_pk_fma_f32 v[132:133], v[132:133], v[172:173], v[164:165]\n"
      "v_pk_fma_f32 v[134:135], v[134:135], v[174:175], v[166:167]\n"
      "v_cvt_pk_bf16_f32 v208, v128, v129\n"
      "v_cvt_pk_bf16_f32 v209, v130, v131\n"
      "v_cvt_pk_bf16_f32 v210, v132, v133\n"
      "v_cvt_pk_bf16_f32 v211, v134, v135\n"
      "v_xor_b32 v12, 128, v12\n"
      "ds_write_b128 v12, v[208:211]\n"
      "v_pk_fma_f32 v[136:137], v[136:137], v[168:169], v[160:161]\n"
      "v_pk_fma_f32 v[138:139], v[138:139], v[170:171], v[162:163]\n"
      "v_pk_fma_f32 v[140:141], v[140:141], v[172:173], v[164:165]\n"
      "v_pk_fma_f32 v[142:143], v[142:143], v[174:175], v[166:167]\n"
      "v_cvt_pk_bf16_f32 v212, v136, v137\n"
      "v_cvt_pk_bf16_f32 v213, v138, v139\n"
      "v_cvt_pk_bf16_f32 v214, v140, v141\n"
      "v_cvt_pk_bf16_f32 v215, v142, v143\n"
      "v_xor_b32 v13, 128, v13\n"
      "ds_write_b128 v13, v[212:215]\n"
      "v_pk_fma_f32 v[144:145], v[144:145], v[168:169], v[160:161]\n"
      "v_pk_fma_f32 v[146:147], v[146:147], v[170:171], v[162:163]\n"
      "v_pk_fma_f32 v[148:149], v[148:149], v[172:173], v[164:165]\n"
      "v_pk_fma_f32 v[150:151], v[150:151], v[174:175], v[166:167]\n"
      "v_cvt_pk_bf16_f32 v216, v144, v145\n"
      "v_cvt_pk_bf16_f32 v217, v146, v147\n"
      "v_cvt_pk_bf16_f32 v218, v148, v149\n"
      "v_cvt_pk_bf16_f32 v219, v150, v151\n"
      "v_xor_b32 v14, 128, v14\n"
      "ds_write_b128 v14, v[216:219]\n"
      "v_pk_fma_f32 v[152:153], v[152:153], v[168:169], v[160:161]\n"
      "v_pk_fma_f32 v[154:155], v[154:155], v[170:171], v[162:163]\n"
      "v_pk_fma_f32 v[156:157], v[156:157], v[172:173], v[164:165]\n"
      "v_pk_fma_f32 v[158:159], v[158:159], v[174:175], v[166:167]\n"
      "v_cvt_pk_bf16_f32 v220, v152, v153\n"
      "v_cvt_pk_bf16_f32 v221, v154, v155\n"
      "v_cvt_pk_bf16_f32 v222, v156, v157\n"
      "v_cvt_pk_bf16_f32 v223, v158, v159\n"
      "v_xor_b32 v15, 128, v15\n"
      "ds_write_b128 v15, v[220:223]\n"
      "s_waitcnt lgkmcnt(0)\n"
      "s_barrier\n"
      "ds_read_b128 v[112:115], v16 offset:0\n"
      "ds_read_b128 v[116:119], v16 offset:16384\n"
      "ds_read_b64_tr_b16 v[80:81], v6 offset:0\n"
      "ds_read_b64_tr_b16 v[82:83], v6 offset:512\n"
      "ds_read_b64_tr_b16 v[84:85], v7 offset:0\n"
      "ds_read_b64_tr_b16 v[86:87], v7 offset:512\n"
      "ds_read_b64_tr_b16 v[88:89], v6 offset:1024\n"
      "ds_read_b64_tr_b16 v[90:91], v6 offset:1536\n"
      "ds_read_b64_tr_b16 v[92:93], v7 offset:1024\n"
      "ds_read_b64_tr_b16 v[94:95], v7 offset:1536\n"
      "ds_read_b64_tr_b16 v[96:97], v6 offset:2048\n"
      "ds_read_b64_tr_b16 v[98:99], v6 offset:2560\n"
      "s_waitcnt lgkmcnt(8)\n"
      "v_mfma_f32_16x16x32_bf16 v[32:35], v[80:83], v[112:115], 0\n"
      "ds_read_b64_tr_b16 v[100:101], v7 offset:2048\n"
      "ds_read_b128 v[120:123], v17 offset:1024\n"
      "v_mfma_f32_16x16x32_bf16 v[36:39], v[80:83], v[116:119], 0\n"
      "s_waitcnt lgkmcnt(8)\n"
      "ds_read_b64_tr_b16 v[102:103], v7 offset:2560\n"
      "ds_read_b128 v[124:127], v17 offset:17408\n"
      "v_mfma_f32_16x16x32_bf16 v[40:43], v[84:87], v[112:115], 0\n"
      "ds_read_b64_tr_b16 v[104:105], v6 offset:12288\n"
      "v_lshrrev_b32 v8, 4, v255\n"
      "v_mfma_f32_16x16x32_bf16 v[44:47], v[84:87], v[116:119], 0\n"
      "s_waitcnt lgkmcnt(9)\n"
      "ds_read_b64_tr_b16 v[106:107], v6 offset:12800\n"
      "v_bfe_u32 v9, v255, 2, 2\n"
      "v_mfma_f32_16x16x32_bf16 v[48:51], v[88:91], v[112:115], 0\n"
      "ds_read_b64_tr_b16 v[108:109], v7 offset:12288\n"
      "v_lshl_add_u32 v9, v8, 3, v9\n"
      "v_mfma_f32_16x16x32_bf16 v[52:55], v[88:91], v[116:119], 0\n"
      "s_waitcnt lgkmcnt(9)\n"
      "ds_read_b64_tr_b16 v[110:111], v7 offset:12800\n"
      "v_lshlrev_b32 v9, 5, v9\n"
      "v_mfma_f32_16x16x32_bf16 v[56:59], v[92:95], v[112:115], 0\n"
      "ds_read_b64_tr_b16 v[80:81], v6 offset:13312\n"
      "v_and_b32 v10, 3, v255\n"
      "v_mfma_f32_16x16x32_bf16 v[60:63], v[92:95], v[116:119], 0\n"
      "s_waitcnt lgkmcnt(9)\n"
      "ds_read_b64_tr_b16 v[82:83], v6 offset:13824\n"
      "v_lshl_add_u32 v9, v10, 3, v9\n"
      "v_mfma_f32_16x16x32_bf16 v[64:67], v[112:115], v[96:99], 0\n"
      "ds_read_b64_tr_b16 v[84:85], v7 offset:13312\n"
      "v_add_u32 v9, s77, v9\n"
      "v_mfma_f32_16x16x32_bf16 v[68:71], v[116:119], v[96:99], 0\n"
      "s_waitcnt lgkmcnt(8)\n"
      "ds_read_b64_tr_b16 v[86:87], v7 offset:13824\n"
      "v_lshl_add_u32 v8, v8, 4, s77\n"
      "v_mfma_f32_16x16x32_bf16 v[72:75], v[112:115], v[100:103], 0\n"
      "ds_read_b64_tr_b16 v[88:89], v6 offset:14336\n"
      "v_add_u32 v10, s77, v2\n"
      "v_mfma_f32_16x16x32_bf16 v[76:79], v[116:119], v[100:103], 0\n"
      "s_waitcnt lgkmcnt(7)\n"
      "ds_read_b64_tr_b16 v[90:91], v6 offset:14848\n"
      "s_lshl_b32 s63, s53, 6\n"
      "v_mfma_f32_16x16x32_bf16 v[32:35], v[104:107], v[120:123], v[32:35]\n"
      "ds_read_b64_tr_b16 v[92:93], v7 offset:14336\n"
      "ds_read_b128 v[128:131], v16 offset:2048\n"
      "v_mfma_f32_16x16x32_bf16 v[36:39], v[104:107], v[124:127], v[36:39]\n"
      "s_waitcnt lgkmcnt(8)\n"
      "ds_read_b64_tr_b16 v[94:95], v7 offset:14848\n"
      "ds_read_b128 v[132:135], v16 offset:18432\n"
      "v_mfma_f32_16x16x32_bf16 v[40:43], v[108:111], v[120:123], v[40:43]\n"
      "ds_read_b64_tr_b16 v[96:97], v6 offset:24576\n"
      "s_add_u32 s63, s63, 133120\n"
      "v_mfma_f32_16x16x32_bf16 v[44:47], v[108:111], v[124:127], v[44:47]\n"
      "s_waitcnt lgkmcnt(9)\n"
      "ds_read_b64_tr_b16 v[98:99], v6 offset:25088\n"
      "v_and_b32 v11, 15, v255\n"
      "v_mfma_f32_16x16x32_bf16 v[48:51], v[80:83], v[120:123], v[48:51]\n"
      "ds_read_b64_tr_b16 v[100:101], v7 offset:24576\n"
      "v_lshl_add_u32 v11, v11, 2, s63\n"
      "v_mfma_f32_16x16x32_bf16 v[52:55], v[80:83], v[124:127], v[52:55]\n"
      "s_waitcnt lgkmcnt(9)\n"
      "ds_read_b64_tr_b16 v[102:103], v7 offset:25088\n"
      "v_add_u32 v12, 133120, v254\n"
      "v_mfma_f32_16x16x32_bf16 v[56:59], v[84:87], v[120:123], v[56:59]\n"
      "ds_read_b64_tr_b16 v[104:105], v6 offset:25600\n"
      "s_lshl1_add_u32 s78, s72, s60\n"
      "v_mfma_f32_16x16x32_bf16 v[60:63], v[84:87], v[124:127], v[60:63]\n"
      "s_waitcnt lgkmcnt(9)\n"
      "ds_read_b64_tr_b16 v[106:107], v6 offset:26112\n"
      "s_lshl1_add_u32 s79, s73, s61\n"
      "v_mfma_f32_16x16x32_bf16 v[64:67], v[120:123], v[88:91], v[64:67]\n"
      "ds_read_b64_tr_b16 v[108:109], v7 offset:25600\n"
      "s_lshl_b32 s63, s79, 7\n"
      "v_mfma_f32_16x16x32_bf16 v[68:71], v[124:127], v[88:91], v[68:71]\n"
      "s_waitcnt lgkmcnt(8)\n"
      "ds_read_b64_tr_b16 v[110:111], v7 offset:26112\n"
      "s_add_u32 s63, s63, s78\n"
      "s_barrier\n"
      "v_mfma_f32_16x16x32_bf16 v[72:75], v[120:123], v[92:95], v[72:75]\n"
      "s_add_u32 m0, s77, 0\n"
      "ds_read_b64_tr_b16 v[80:81], v6 offset:26624\n"
      "s_lshl_b32 s84, s63, 10\n"
      "v_mfma_f32_16x16x32_bf16 v[76:79], v[124:127], v[92:95], v[76:79]\n"
      "s_waitcnt lgkmcnt(7)\n"
      "ds_read_b64_tr_b16 v[82:83], v6 offset:27136\n"
      "s_add_u32 s84, s84, 1048576\n"
      "v_mfma_f32_16x16x32_bf16 v[32:35], v[96:99], v[128:131], v[32:35]\n"
      "s_add_u32 s70, s74, 786432\n"
      "ds_read_b64_tr_b16 v[84:85], v7 offset:26624\n"
      "buffer_load_dwordx4 v3, s[16:19], s70 offen offset:0 lds\n"
      "v_mfma_f32_16x16x32_bf16 v[36:39], v[96:99], v[132:135], v[36:39]\n"
      "s_waitcnt lgkmcnt(7)\n"
      "ds_read_b64_tr_b16 v[86:87], v7 offset:27136\n"
      "ds_read_b128 v[136:139], v17 offset:3072\n"
      "v_mfma_f32_16x16x32_bf16 v[40:43], v[100:103], v[128:131], v[40:43]\n"
      "ds_read_b64_tr_b16 v[88:89], v6 offset:36864\n"
      "ds_read_b128 v[140:143], v17 offset:19456\n"
      "v_mfma_f32_16x16x32_bf16 v[44:47], v[100:103], v[132:135], v[44:47]\n"
      "s_waitcnt lgkmcnt(9)\n"
      "ds_read_b64_tr_b16 v[90:91], v6 offset:37376\n"
      "buffer_load_dwordx4 v3, s[16:19], s70 offen offset:1024 lds\n"
      "v_mfma_f32_16x16x32_bf16 v[48:51], v[104:107], v[128:131], v[48:51]\n"
      "ds_read_b64_tr_b16 v[92:93], v7 offset:36864\n"
      "s_add_u32 s85, s84, 65536\n"
      "v_mfma_f32_16x16x32_bf16 v[52:55], v[104:107], v[132:135], v[52:55]\n"
      "s_waitcnt lgkmcnt(9)\n"
      "ds_read_b64_tr_b16 v[94:95], v7 offset:37376\n"
      "s_lshl_b32 s82, s63, 11\n"
      "v_mfma_f32_16x16x32_bf16 v[56:59], v[108:111], v[128:131], v[56:59]\n"
      "ds_read_b64_tr_b16 v[96:97], v6 offset:37888\n"
      "buffer_load_dwordx4 v3, s[16:19], s70 offen offset:2048 lds\n"
      "v_mfma_f32_16x16x32_bf16 v[60:63], v[108:111], v[132:135], v[60:63]\n"
      "s_waitcnt lgkmcnt(9)\n"
      "ds_read_b64_tr_b16 v[98:99], v6 offset:38400\n"
      "s_add_u32 s82, s82, 3145728\n"
      "v_mfma_f32_16x16x32_bf16 v[64:67], v[128:131], v[80:83], v[64:67]\n"
      "s_add_u32 m0, s77, 3072\n"
      "ds_read_b64_tr_b16 v[100:101], v7 offset:37888\n"
      "s_add_u32 s83, s82, 131072\n"
      "v_mfma_f32_16x16x32_bf16 v[68:71], v[132:135], v[80:83], v[68:71]\n"
      "s_waitcnt lgkmcnt(9)\n"
      "ds_read_b64_tr_b16 v[102:103], v7 offset:38400\n"
      "v_and_b32 v20, 0x230, v254\n"
      "v_mfma_f32_16x16x32_bf16 v[72:75], v[128:131], v[84:87], v[72:75]\n"
      "s_add_u32 s70, s74, 811008\n"
      "ds_read_b64_tr_b16 v[104:105], v6 offset:38912\n"
      "buffer_load_dwordx4 v248, s[16:19], s70 offen offset:0 lds\n"
      "v_mfma_f32_16x16x32_bf16 v[76:79], v[132:135], v[84:87], v[76:79]\n"
      "s_waitcnt lgkmcnt(7)\n"
      "ds_read_b64_tr_b16 v[106:107], v6 offset:39424\n"
      "v_and_b32 v21, 0xc0, v254\n"
      "v_mfma_f32_16x16x32_bf16 v[32:35], v[88:91], v[136:139], v[32:35]\n"
      "ds_read_b64_tr_b16 v[108:109], v7 offset:38912\n"
      "ds_read_b128 v[144:147], v16 offset:4096\n"
      "v_mfma_f32_16x16x32_bf16 v[36:39], v[88:91], v[140:143], v[36:39]\n"
      "s_waitcnt lgkmcnt(8)\n"
      "ds_read_b64_tr_b16 v[110:111], v7 offset:39424\n"
      "buffer_load_dwordx4 v248, s[16:19], s70 offen offset:1024 lds\n"
      "v_mfma_f32_16x16x32_bf16 v[40:43], v[92:95], v[136:139], v[40:43]\n"
      "ds_read_b64_tr_b16 v[80:81], v18 offset:0\n"
      "ds_read_b128 v[148:151], v16 offset:20480\n"
      "v_mfma_f32_16x16x32_bf16 v[44:47], v[92:95], v[140:143], v[44:47]\n"
      "s_waitcnt lgkmcnt(9)\n"
      "ds_read_b64_tr_b16 v[82:83], v18 offset:512\n"
      "v_lshl_or_b32 v20, v21, 1, v20\n"
      "v_mfma_f32_16x16x32_bf16 v[48:51], v[96:99], v[136:139], v[48:51]\n"
      "ds_read_b64_tr_b16 v[84:85], v19 offset:0\n"
      "buffer_load_dwordx4 v248, s[16:19], s70 offen offset:2048 lds\n"
      "v_mfma_f32_16x16x32_bf16 v[52:55], v[96:99], v[140:143], v[52:55]\n"
      "s_waitcnt lgkmcnt(9)\n"
      "ds_read_b64_tr_b16 v[86:87], v19 offset:512\n"
      "v_bfe_u32 v21, v255, 4, 1\n"
      "v_mfma_f32_16x16x32_bf16 v[56:59], v[100:103], v[136:139], v[56:59]\n"
      "ds_read_b64_tr_b16 v[88:89], v18 offset:1024\n"
      "v_lshl_or_b32 v20, v21, 6, v20\n"
      "v_mfma_f32_16x16x32_bf16 v[60:63], v[100:103], v[140:143], v[60:63]\n"
      "s_waitcnt lgkmcnt(9)\n"
      "ds_read_b64_tr_b16 v[90:91], v18 offset:1536\n"
      "s_mov_b32 s80, 0x3eb8aa3b\n"
      "v_mfma_f32_16x16x32_bf16 v[64:67], v[136:139], v[104:107], v[64:67]\n"
      "ds_read_b64_tr_b16 v[92:93], v19 offset:1024\n"
      "s_and_b32 s63, s72, 3\n"
      "v_mfma_f32_16x16x32_bf16 v[68:71], v[140:143], v[104:107], v[68:71]\n"
      "s_waitcnt lgkmcnt(8)\n"
      "ds_read_b64_tr_b16 v[94:95], v19 offset:1536\n"
      "s_lshl2_add_u32 s63, s73, s63\n"
      "s_barrier\n"
      "v_mfma_f32_16x16x32_bf16 v[72:75], v[136:139], v[108:111], v[72:75]\n"
      "s_add_u32 m0, s77, 24576\n"
      "ds_read_b64_tr_b16 v[96:97], v18 offset:2048\n"
      "s_lshr_b32 s64, s72, 2\n"
      "v_mfma_f32_16x16x32_bf16 v[76:79], v[140:143], v[108:111], v[76:79]\n"
      "s_waitcnt lgkmcnt(7)\n"
      "ds_read_b64_tr_b16 v[98:99], v18 offset:2560\n"
      "s_mul_i32 s90, s63, 256\n"
      "v_mfma_f32_16x16x32_bf16 v[32:35], v[80:83], v[144:147], v[32:35]\n"
      "Lhs0_1_%=:\n"
      "s_cbranch_execz Lhr0_%=\n"
      "s_add_u32 s70, s74, 983040\n"
      "ds_read_b64_tr_b16 v[100:101], v19 offset:2048\n"
      "buffer_load_dwordx4 v3, s[16:19], s70 offen offset:0 lds\n"
      "v_mfma_f32_16x16x32_bf16 v[36:39], v[80:83], v[148:151], v[36:39]\n"
      "s_waitcnt lgkmcnt(7)\n"
      "ds_read_b64_tr_b16 v[102:103], v19 offset:2560\n"
      "ds_read_b128 v[112:115], v17 offset:5120\n"
      "v_mfma_f32_16x16x32_bf16 v[40:43], v[84:87], v[144:147], v[40:43]\n"
      "ds_read_b64_tr_b16 v[104:105], v18 offset:12288\n"
      "ds_read_b128 v[116:119], v17 offset:21504\n"
      "v_mfma_f32_16x16x32_bf16 v[44:47], v[84:87], v[148:151], v[44:47]\n"
      "s_waitcnt lgkmcnt(9)\n"
      "ds_read_b64_tr_b16 v[106:107], v18 offset:12800\n"
      "buffer_load_dwordx4 v3, s[16:19], s70 offen offset:1024 lds\n"
      "v_mfma_f32_16x16x32_bf16 v[48:51], v[88:91], v[144:147], v[48:51]\n"
      "ds_read_b64_tr_b16 v[108:109], v19 offset:12288\n"
      "s_lshl3_add_u32 s90, s64, s90\n"
      "v_mfma_f32_16x16x32_bf16 v[52:55], v[88:91], v[148:151], v[52:55]\n"
      "s_waitcnt lgkmcnt(9)\n"
      "ds_read_b64_tr_b16 v[110:111], v19 offset:12800\n"
      "s_add_u32 s90, s90, 21561344\n"
      "v_mfma_f32_16x16x32_bf16 v[56:59], v[92:95], v[144:147], v[56:59]\n"
      "ds_read_b64_tr_b16 v[80:81], v18 offset:13312\n"
      "buffer_load_dwordx4 v3, s[16:19], s70 offen offset:2048 lds\n"
      "v_mfma_f32_16x16x32_bf16 v[60:63], v[92:95], v[148:151], v[60:63]\n"
      "s_waitcnt lgkmcnt(9)\n"
      "ds_read_b64_tr_b16 v[82:83], v18 offset:13824\n"
      "v_mfma_f32_16x16x32_bf16 v[64:67], v[144:147], v[96:99], v[64:67]\n"
      "s_add_u32 m0, s77, 27648\n"
      "ds_read_b64_tr_b16 v[84:85], v19 offset:13312\n"
      "v_mfma_f32_16x16x32_bf16 v[68:71], v[148:151], v[96:99], v[68:71]\n"
      "s_waitcnt lgkmcnt(9)\n"
      "ds_read_b64_tr_b16 v[86:87], v19 offset:13824\n"
      "v_mfma_f32_16x16x32_bf16 v[72:75], v[144:147], v[100:103], v[72:75]\n"
      "s_add_u32 s70, s74, 1007616\n"
      "ds_read_b64_tr_b16 v[88:89], v18 offset:14336\n"
      "buffer_load_dwordx4 v248, s[16:19], s70 offen offset:0 lds\n"
      "v_mfma_f32_16x16x32_bf16 v[76:79], v[148:151], v[100:103], v[76:79]\n"
      "s_waitcnt lgkmcnt(7)\n"
      "ds_read_b64_tr_b16 v[90:91], v18 offset:14848\n"
      "v_mfma_f32_16x16x32_bf16 v[32:35], v[104:107], v[112:115], v[32:35]\n"
      "ds_read_b64_tr_b16 v[92:93], v19 offset:14336\n"
      "ds_read_b128 v[120:123], v16 offset:6144\n"
      "v_mfma_f32_16x16x32_bf16 v[36:39], v[104:107], v[116:119], v[36:39]\n"
      "s_waitcnt lgkmcnt(8)\n"
      "ds_read_b64_tr_b16 v[94:95], v19 offset:14848\n"
      "buffer_load_dwordx4 v248, s[16:19], s70 offen offset:1024 lds\n"
      "v_mfma_f32_16x16x32_bf16 v[40:43], v[108:111], v[112:115], v[40:43]\n"
      "ds_read_b64_tr_b16 v[96:97], v18 offset:24576\n"
      "ds_read_b128 v[124:127], v16 offset:22528\n"
      "v_mfma_f32_16x16x32_bf16 v[44:47], v[108:111], v[116:119], v[44:47]\n"
      "s_waitcnt lgkmcnt(9)\n"
      "ds_read_b64_tr_b16 v[98:99], v18 offset:25088\n"
      "v_mfma_f32_16x16x32_bf16 v[48:51], v[80:83], v[112:115], v[48:51]\n"
      "ds_read_b64_tr_b16 v[100:101], v19 offset:24576\n"
      "buffer_load_dwordx4 v248, s[16:19], s70 offen offset:2048 lds\n"
      "v_mfma_f32_16x16x32_bf16 v[52:55], v[80:83], v[116:119], v[52:55]\n"
      "s_waitcnt lgkmcnt(9)\n"
      "ds_read_b64_tr_b16 v[102:103], v19 offset:25088\n"
      "v_mfma_f32_16x16x32_bf16 v[56:59], v[84:87], v[112:115], v[56:59]\n"
      "ds_read_b64_tr_b16 v[104:105], v18 offset:25600\n"
      "v_mfma_f32_16x16x32_bf16 v[60:63], v[84:87], v[116:119], v[60:63]\n"
      "s_waitcnt lgkmcnt(9)\n"
      "ds_read_b64_tr_b16 v[106:107], v18 offset:26112\n"
      "v_mfma_f32_16x16x32_bf16 v[64:67], v[112:115], v[88:91], v[64:67]\n"
      "ds_read_b64_tr_b16 v[108:109], v19 offset:25600\n"
      "v_mfma_f32_16x16x32_bf16 v[68:71], v[116:119], v[88:91], v[68:71]\n"
      "s_waitcnt lgkmcnt(8)\n"
      "ds_read_b64_tr_b16 v[110:111], v19 offset:26112\n"
      "s_barrier\n"
      "v_mfma_f32_16x16x32_bf16 v[72:75], v[112:115], v[92:95], v[72:75]\n"
      "s_add_u32 m0, s77, 49152\n"
      "ds_read_b64_tr_b16 v[80:81], v18 offset:26624\n"
      "v_mfma_f32_16x16x32_bf16 v[76:79], v[116:119], v[92:95], v[76:79]\n"
      "s_waitcnt lgkmcnt(7)\n"
      "ds_read_b64_tr_b16 v[82:83], v18 offset:27136\n"
      "v_mfma_f32_16x16x32_bf16 v[32:35], v[96:99], v[120:123], v[32:35]\n"
      "s_add_u32 s70, s74, 1179648\n"
      "ds_read_b64_tr_b16 v[84:85], v19 offset:26624\n"
      "buffer_load_dwordx4 v3, s[16:19], s70 offen offset:0 lds\n"
      "v_mfma_f32_16x16x32_bf16 v[36:39], v[96:99], v[124:127], v[36:39]\n"
      "s_waitcnt lgkmcnt(7)\n"
      "ds_read_b64_tr_b16 v[86:87], v19 offset:27136\n"
      "ds_read_b128 v[128:131], v17 offset:7168\n"
      "v_mfma_f32_16x16x32_bf16 v[40:43], v[100:103], v[120:123], v[40:43]\n"
      "ds_read_b64_tr_b16 v[88:89], v18 offset:36864\n"
      "ds_read_b128 v[132:135], v17 offset:23552\n"
      "v_mfma_f32_16x16x32_bf16 v[44:47], v[100:103], v[124:127], v[44:47]\n"
      "s_waitcnt lgkmcnt(9)\n"
      "ds_read_b64_tr_b16 v[90:91], v18 offset:37376\n"
      "buffer_load_dwordx4 v3, s[16:19], s70 offen offset:1024 lds\n"
      "v_mfma_f32_16x16x32_bf16 v[48:51], v[104:107], v[120:123], v[48:51]\n"
      "ds_read_b64_tr_b16 v[92:93], v19 offset:36864\n"
      "v_mfma_f32_16x16x32_bf16 v[52:55], v[104:107], v[124:127], v[52:55]\n"
      "s_waitcnt lgkmcnt(9)\n"
      "ds_read_b64_tr_b16 v[94:95], v19 offset:37376\n"
      "v_mfma_f32_16x16x32_bf16 v[56:59], v[108:111], v[120:123], v[56:59]\n"
      "ds_read_b64_tr_b16 v[96:97], v18 offset:37888\n"
      "buffer_load_dwordx4 v3, s[16:19], s70 offen offset:2048 lds\n"
      "v_mfma_f32_16x16x32_bf16 v[60:63], v[108:111], v[124:127], v[60:63]\n"
      "s_waitcnt lgkmcnt(9)\n"
      "ds_read_b64_tr_b16 v[98:99], v18 offset:38400\n"
      "v_mfma_f32_16x16x32_bf16 v[64:67], v[120:123], v[80:83], v[64:67]\n"
      "s_add_u32 m0, s77, 52224\n"
      "ds_read_b64_tr_b16 v[100:101], v19 offset:37888\n"
      "v_mfma_f32_16x16x32_bf16 v[68:71], v[124:127], v[80:83], v[68:71]\n"
      "s_waitcnt lgkmcnt(9)\n"
      "ds_read_b64_tr_b16 v[102:103], v19 offset:38400\n"
      "v_mfma_f32_16x16x32_bf16 v[72:75], v[120:123], v[84:87], v[72:75]\n"
      "s_add_u32 s70, s74, 1204224\n"
      "ds_read_b64_tr_b16 v[104:105], v18 offset:38912\n"
      "buffer_load_dwordx4 v248, s[16:19], s70 offen offset:0 lds\n"
      "v_mfma_f32_16x16x32_bf16 v[76:79], v[124:127], v[84:87], v[76:79]\n"
      "s_waitcnt lgkmcnt(7)\n"
      "ds_read_b64_tr_b16 v[106:107], v18 offset:39424\n"
      "v_mfma_f32_16x16x32_bf16 v[32:35], v[88:91], v[128:131], v[32:35]\n"
      "ds_read_b64_tr_b16 v[108:109], v19 offset:38912\n"
      "ds_read_b128 v[136:139], v16 offset:8192\n"
      "v_mfma_f32_16x16x32_bf16 v[36:39], v[88:91], v[132:135], v[36:39]\n"
      "s_waitcnt lgkmcnt(8)\n"
      "ds_read_b64_tr_b16 v[110:111], v19 offset:39424\n"
      "buffer_load_dwordx4 v248, s[16:19], s70 offen offset:1024 lds\n"
      "s_waitcnt vmcnt(11)\n"
      "s_barrier\n"
      "v_mfma_f32_16x16x32_bf16 v[40:43], v[92:95], v[128:131], v[40:43]\n"
      "ds_read_b64_tr_b16 v[80:81], v6 offset:0\n"
      "ds_read_b128 v[140:143], v16 offset:24576\n"
      "v_mfma_f32_16x16x32_bf16 v[44:47], v[92:95], v[132:135], v[44:47]\n"
      "s_waitcnt lgkmcnt(9)\n"
      "ds_read_b64_tr_b16 v[82:83], v6 offset:512\n"
      "v_mfma_f32_16x16x32_bf16 v[48:51], v[96:99], v[128:131], v[48:51]\n"
      "ds_read_b64_tr_b16 v[84:85], v7 offset:0\n"
      "buffer_load_dwordx4 v248, s[16:19], s70 offen offset:2048 lds\n"
      "v_mfma_f32_16x16x32_bf16 v[52:55], v[96:99], v[132:135], v[52:55]\n"
      "s_waitcnt lgkmcnt(9)\n"
      "ds_read_b64_tr_b16 v[86:87], v7 offset:512\n"
      "v_mfma_f32_16x16x32_bf16 v[56:59], v[100:103], v[128:131], v[56:59]\n"
      "ds_read_b64_tr_b16 v[88:89], v6 offset:1024\n"
      "v_mfma_f32_16x16x32_bf16 v[60:63], v[100:103], v[132:135], v[60:63]\n"
      "s_waitcnt lgkmcnt(9)\n"
      "ds_read_b64_tr_b16 v[90:91], v6 offset:1536\n"
      "v_mfma_f32_16x16x32_bf16 v[64:67], v[128:131], v[104:107], v[64:67]\n"
      "ds_read_b64_tr_b16 v[92:93], v7 offset:1024\n"
      "v_mfma_f32_16x16x32_bf16 v[68:71], v[132:135], v[104:107], v[68:71]\n"
      "s_waitcnt lgkmcnt(8)\n"
      "ds_read_b64_tr_b16 v[94:95], v7 offset:1536\n"
      "s_barrier\n"
      "v_mfma_f32_16x16x32_bf16 v[72:75], v[128:131], v[108:111], v[72:75]\n"
      "s_add_u32 m0, s77, 73728\n"
      "ds_read_b64_tr_b16 v[96:97], v6 offset:2048\n"
      "v_mfma_f32_16x16x32_bf16 v[76:79], v[132:135], v[108:111], v[76:79]\n"
      "s_waitcnt lgkmcnt(7)\n"
      "ds_read_b64_tr_b16 v[98:99], v6 offset:2560\n"
      "v_mfma_f32_16x16x32_bf16 v[32:35], v[80:83], v[136:139], v[32:35]\n"
      "s_add_u32 s70, s74, 1376256\n"
      "ds_read_b64_tr_b16 v[100:101], v7 offset:2048\n"
      "buffer_load_dwordx4 v3, s[16:19], s70 offen offset:0 lds\n"
      "v_mfma_f32_16x16x32_bf16 v[36:39], v[80:83], v[140:143], v[36:39]\n"
      "s_waitcnt lgkmcnt(7)\n"
      "ds_read_b64_tr_b16 v[102:103], v7 offset:2560\n"
      "ds_read_b128 v[144:147], v17 offset:9216\n"
      "v_mfma_f32_16x16x32_bf16 v[40:43], v[84:87], v[136:139], v[40:43]\n"
      "ds_read_b64_tr_b16 v[104:105], v6 offset:12288\n"
      "ds_read_b128 v[148:151], v17 offset:25600\n"
      "v_mfma_f32_16x16x32_bf16 v[44:47], v[84:87], v[140:143], v[44:47]\n"
      "s_waitcnt lgkmcnt(9)\n"
      "ds_read_b64_tr_b16 v[106:107], v6 offset:12800\n"
      "buffer_load_dwordx4 v3, s[16:19], s70 offen offset:1024 lds\n"
      "v_mfma_f32_16x16x32_bf16 v[48:51], v[88:91], v[136:139], v[48:51]\n"
      "ds_read_b64_tr_b16 v[108:109], v7 offset:12288\n"
      "v_mfma_f32_16x16x32_bf16 v[52:55], v[88:91], v[140:143], v[52:55]\n"
      "s_waitcnt lgkmcnt(9)\n"
      "ds_read_b64_tr_b16 v[110:111], v7 offset:12800\n"
      "v_mfma_f32_16x16x32_bf16 v[56:59], v[92:95], v[136:139], v[56:59]\n"
      "ds_read_b64_tr_b16 v[80:81], v6 offset:13312\n"
      "buffer_load_dwordx4 v3, s[16:19], s70 offen offset:2048 lds\n"
      "v_mfma_f32_16x16x32_bf16 v[60:63], v[92:95], v[140:143], v[60:63]\n"
      "s_waitcnt lgkmcnt(9)\n"
      "ds_read_b64_tr_b16 v[82:83], v6 offset:13824\n"
      "v_mfma_f32_16x16x32_bf16 v[64:67], v[136:139], v[96:99], v[64:67]\n"
      "s_add_u32 m0, s77, 76800\n"
      "ds_read_b64_tr_b16 v[84:85], v7 offset:13312\n"
      "v_mfma_f32_16x16x32_bf16 v[68:71], v[140:143], v[96:99], v[68:71]\n"
      "s_waitcnt lgkmcnt(9)\n"
      "ds_read_b64_tr_b16 v[86:87], v7 offset:13824\n"
      "v_mfma_f32_16x16x32_bf16 v[72:75], v[136:139], v[100:103], v[72:75]\n"
      "s_add_u32 s70, s74, 1400832\n"
      "ds_read_b64_tr_b16 v[88:89], v6 offset:14336\n"
      "buffer_load_dwordx4 v248, s[16:19], s70 offen offset:0 lds\n"
      "v_mfma_f32_16x16x32_bf16 v[76:79], v[140:143], v[100:103], v[76:79]\n"
      "s_waitcnt lgkmcnt(7)\n"
      "ds_read_b64_tr_b16 v[90:91], v6 offset:14848\n"
      "v_mfma_f32_16x16x32_bf16 v[32:35], v[104:107], v[144:147], v[32:35]\n"
      "ds_read_b64_tr_b16 v[92:93], v7 offset:14336\n"
      "ds_read_b128 v[112:115], v16 offset:10240\n"
      "v_mfma_f32_16x16x32_bf16 v[36:39], v[104:107], v[148:151], v[36:39]\n"
      "s_waitcnt lgkmcnt(8)\n"
      "ds_read_b64_tr_b16 v[94:95], v7 offset:14848\n"
      "buffer_load_dwordx4 v248, s[16:19], s70 offen offset:1024 lds\n"
      "s_waitcnt vmcnt(11)\n"
      "s_barrier\n"
      "v_mfma_f32_16x16x32_bf16 v[40:43], v[108:111], v[144:147], v[40:43]\n"
      "ds_read_b64_tr_b16 v[96:97], v6 offset:24576\n"
      "ds_read_b128 v[116:119], v16 offset:26624\n"
      "v_mfma_f32_16x16x32_bf16 v[44:47], v[108:111], v[148:151], v[44:47]\n"
      "s_waitcnt lgkmcnt(9)\n"
      "ds_read_b64_tr_b16 v[98:99], v6 offset:25088\n"
      "v_mfma_f32_16x16x32_bf16 v[48:51], v[80:83], v[144:147], v[48:51]\n"
      "ds_read_b64_tr_b16 v[100:101], v7 offset:24576\n"
      "buffer_load_dwordx4 v248, s[16:19], s70 offen offset:2048 lds\n"
      "v_mfma_f32_16x16x32_bf16 v[52:55], v[80:83], v[148:151], v[52:55]\n"
      "s_waitcnt lgkmcnt(9)\n"
      "ds_read_b64_tr_b16 v[102:103], v7 offset:25088\n"
      "v_mfma_f32_16x16x32_bf16 v[56:59], v[84:87], v[144:147], v[56:59]\n"
      "ds_read_b64_tr_b16 v[104:105], v6 offset:25600\n"
      "v_mfma_f32_16x16x32_bf16 v[60:63], v[84:87], v[148:151], v[60:63]\n"
      "s_waitcnt lgkmcnt(9)\n"
      "ds_read_b64_tr_b16 v[106:107], v6 offset:26112\n"
      "v_mfma_f32_16x16x32_bf16 v[64:67], v[144:147], v[88:91], v[64:67]\n"
      "ds_read_b64_tr_b16 v[108:109], v7 offset:25600\n"
      "v_mfma_f32_16x16x32_bf16 v[68:71], v[148:151], v[88:91], v[68:71]\n"
      "s_waitcnt lgkmcnt(8)\n"
      "ds_read_b64_tr_b16 v[110:111], v7 offset:26112\n"
      "s_barrier\n"
      "v_mfma_f32_16x16x32_bf16 v[72:75], v[144:147], v[92:95], v[72:75]\n"
      "s_add_u32 m0, s77, 2048\n"
      "ds_read_b64_tr_b16 v[80:81], v6 offset:26624\n"
      "buffer_load_dwordx4 v31, s[36:39], s98 offen lds\n"
      "v_mfma_f32_16x16x32_bf16 v[76:79], v[148:151], v[92:95], v[76:79]\n"
      "s_waitcnt lgkmcnt(7)\n"
      "ds_read_b64_tr_b16 v[82:83], v6 offset:27136\n"
      "v_mfma_f32_16x16x32_bf16 v[32:35], v[96:99], v[112:115], v[32:35]\n"
      "s_add_u32 m0, s77, 3072\n"
      "ds_read_b64_tr_b16 v[84:85], v7 offset:26624\n"
      "ds_read_b128 v[120:123], v17 offset:11264\n"
      "v_mfma_f32_16x16x32_bf16 v[36:39], v[96:99], v[116:119], v[36:39]\n"
      "s_waitcnt lgkmcnt(8)\n"
      "ds_read_b64_tr_b16 v[86:87], v7 offset:27136\n"
      "ds_read_b128 v[124:127], v17 offset:27648\n"
      "v_mfma_f32_16x16x32_bf16 v[40:43], v[100:103], v[112:115], v[40:43]\n"
      "s_add_u32 s98, s98, 196608\n"
      "ds_read_b64_tr_b16 v[88:89], v6 offset:36864\n"
      "buffer_load_dwordx4 v31, s[36:39], s98 offen lds\n"
      "v_mfma_f32_16x16x32_bf16 v[44:47], v[100:103], v[116:119], v[44:47]\n"
      "s_waitcnt lgkmcnt(9)\n"
      "ds_read_b64_tr_b16 v[90:91], v6 offset:37376\n"
      "v_mfma_f32_16x16x32_bf16 v[48:51], v[104:107], v[112:115], v[48:51]\n"
      "s_add_u32 m0, s77, 4096\n"
      "ds_read_b64_tr_b16 v[92:93], v7 offset:36864\n"
      "v_mfma_f32_16x16x32_bf16 v[52:55], v[104:107], v[116:119], v[52:55]\n"
      "s_waitcnt lgkmcnt(9)\n"
      "ds_read_b64_tr_b16 v[94:95], v7 offset:37376\n"
      "v_mfma_f32_16x16x32_bf16 v[56:59], v[108:111], v[112:115], v[56:59]\n"
      "s_add_u32 s98, s98, 196608\n"
      "ds_read_b64_tr_b16 v[96:97], v6 offset:37888\n"
      "buffer_load_dwordx4 v31, s[36:39], s98 offen lds\n"
      "v_mfma_f32_16x16x32_bf16 v[60:63], v[108:111], v[116:119], v[60:63]\n"
      "s_waitcnt lgkmcnt(9)\n"
      "ds_read_b64_tr_b16 v[98:99], v6 offset:38400\n"
      "v_mfma_f32_16x16x32_bf16 v[64:67], v[112:115], v[80:83], v[64:67]\n"
      "s_add_u32 m0, s77, 5120\n"
      "ds_read_b64_tr_b16 v[100:101], v7 offset:37888\n"
      "v_mfma_f32_16x16x32_bf16 v[68:71], v[116:119], v[80:83], v[68:71]\n"
      "s_waitcnt lgkmcnt(8)\n"
      "ds_read_b64_tr_b16 v[102:103], v7 offset:38400\n"
      "v_mfma_f32_16x16x32_bf16 v[72:75], v[112:115], v[84:87], v[72:75]\n"
      "s_add_u32 s98, s98, 196608\n"
      "ds_read_b64_tr_b16 v[104:105], v6 offset:38912\n"
      "buffer_load_dwordx4 v31, s[36:39], s98 offen lds\n"
      "v_mfma_f32_16x16x32_bf16 v[76:79], v[116:119], v[84:87], v[76:79]\n"
      "s_waitcnt lgkmcnt(7)\n"
      "ds_read_b64_tr_b16 v[106:107], v6 offset:39424\n"
      "s_waitcnt vmcnt(28)\n"
      "v_add_u32 v224, s77, v254\n"
      "ds_write_b128 v224, v[232:235]\n"
      "v_mfma_f32_16x16x32_bf16 v[32:35], v[88:91], v[120:123], v[32:35]\n"
      "ds_read_b64_tr_b16 v[108:109], v7 offset:38912\n"
      "ds_read_b128 v[128:131], v16 offset:12288\n"
      "v_mfma_f32_16x16x32_bf16 v[36:39], v[88:91], v[124:127], v[36:39]\n"
      "s_waitcnt lgkmcnt(9)\n"
      "ds_read_b64_tr_b16 v[110:111], v7 offset:39424\n"
      "ds_read_b128 v[132:135], v16 offset:28672\n"
      "s_waitcnt vmcnt(10)\n"
      "s_barrier\n"
      "v_mfma_f32_16x16x32_bf16 v[40:43], v[92:95], v[120:123], v[40:43]\n"
      "ds_read_b64_tr_b16 v[80:81], v18 offset:0\n"
      "ds_read_b128 v[136:139], v17 offset:13312\n"
      "v_mfma_f32_16x16x32_bf16 v[44:47], v[92:95], v[124:127], v[44:47]\n"
      "s_waitcnt lgkmcnt(11)\n"
      "ds_read_b64_tr_b16 v[82:83], v18 offset:512\n"
      "ds_read_b128 v[140:143], v17 offset:29696\n"
      "v_mfma_f32_16x16x32_bf16 v[48:51], v[96:99], v[120:123], v[48:51]\n"
      "ds_read_b64_tr_b16 v[84:85], v18 offset:12288\n"
      "ds_read_b128 v[144:147], v16 offset:14336\n"
      "v_mfma_f32_16x16x32_bf16 v[52:55], v[96:99], v[124:127], v[52:55]\n"
      "s_waitcnt lgkmcnt(13)\n"
      "ds_read_b64_tr_b16 v[86:87], v18 offset:12800\n"
      "ds_read_b128 v[148:151], v16 offset:30720\n"
      "s_waitcnt vmcnt(4)\n"
      "s_barrier\n"
      "v_mfma_f32_16x16x32_bf16 v[56:59], v[100:103], v[120:123], v[56:59]\n"
      "ds_read_b64_tr_b16 v[88:89], v18 offset:24576\n"
      "ds_read_b128 v[112:115], v17 offset:15360\n"
      "v_mfma_f32_16x16x32_bf16 v[60:63], v[100:103], v[124:127], v[60:63]\n"
      "s_waitcnt lgkmcnt(15)\n"
      "ds_read_b64_tr_b16 v[90:91], v18 offset:25088\n"
      "ds_read_b128 v[116:119], v17 offset:31744\n"
      "v_mfma_f32_16x16x32_bf16 v[64:67], v[120:123], v[104:107], v[64:67]\n"
      "ds_read_b64_tr_b16 v[92:93], v18 offset:36864\n"
      "ds_write_b128 v224, v[236:239] offset:1024\n"
      "v_mfma_f32_16x16x32_bf16 v[68:71], v[124:127], v[104:107], v[68:71]\n"
      "s_waitcnt lgkmcnt(15)\n"
      "ds_read_b64_tr_b16 v[94:95], v18 offset:37376\n"
      "ds_read_b64_tr_b16 v[240:241], v226 offset:0\n"
      "v_mfma_f32_16x16x32_bf16 v[72:75], v[120:123], v[108:111], v[72:75]\n"
      "ds_read_b64_tr_b16 v[96:97], v19 offset:0\n"
      "ds_read_b64_tr_b16 v[242:243], v226 offset:512\n"
      "v_mfma_f32_16x16x32_bf16 v[76:79], v[124:127], v[108:111], v[76:79]\n"
      "s_waitcnt lgkmcnt(15)\n"
      "ds_read_b64_tr_b16 v[98:99], v19 offset:512\n"
      "ds_read_b64_tr_b16 v[244:245], v226 offset:1024\n"
      "v_mfma_f32_16x16x32_bf16 v[32:35], v[80:83], v[128:131], v[32:35]\n"
      "ds_read_b64_tr_b16 v[100:101], v19 offset:12288\n"
      "ds_read_b64_tr_b16 v[246:247], v226 offset:1536\n"
      "v_mfma_f32_16x16x32_bf16 v[36:39], v[80:83], v[132:135], v[36:39]\n"
      "s_waitcnt lgkmcnt(15)\n"
      "ds_read_b64_tr_b16 v[102:103], v19 offset:12800\n"
      "v_mfma_f32_16x16x32_bf16 v[32:35], v[84:87], v[136:139], v[32:35]\n"
      "ds_read_b64_tr_b16 v[104:105], v19 offset:24576\n"
      "v_mfma_f32_16x16x32_bf16 v[36:39], v[84:87], v[140:143], v[36:39]\n"
      "s_waitcnt lgkmcnt(13)\n"
      "ds_read_b64_tr_b16 v[106:107], v19 offset:25088\n"
      "s_waitcnt lgkmcnt(3)\n"
      "buffer_store_dwordx4 v[240:243], v227, s[8:11], s94 offen sc1\n"
      "v_mfma_f32_16x16x32_bf16 v[32:35], v[88:91], v[144:147], v[32:35]\n"
      "ds_read_b64_tr_b16 v[108:109], v19 offset:36864\n"
      "v_mfma_f32_16x16x32_bf16 v[36:39], v[88:91], v[148:151], v[36:39]\n"
      "s_waitcnt lgkmcnt(11)\n"
      "ds_read_b64_tr_b16 v[110:111], v19 offset:37376\n"
      "v_mfma_f32_16x16x32_bf16 v[32:35], v[92:95], v[112:115], v[32:35]\n"
      "ds_read_b64_tr_b16 v[80:81], v18 offset:1024\n"
      "s_waitcnt lgkmcnt(6)\n"
      "buffer_store_dwordx4 v[244:247], v227, s[8:11], s95 offen sc1\n"
      "v_mfma_f32_16x16x32_bf16 v[36:39], v[92:95], v[116:119], v[36:39]\n"
      "s_waitcnt lgkmcnt(9)\n"
      "ds_read_b64_tr_b16 v[82:83], v18 offset:1536\n"
      "v_mfma_f32_16x16x32_bf16 v[40:43], v[96:99], v[128:131], v[40:43]\n"
      "ds_read_b64_tr_b16 v[84:85], v18 offset:13312\n"
      "v_mfma_f32_16x16x32_bf16 v[44:47], v[96:99], v[132:135], v[44:47]\n"
      "s_waitcnt lgkmcnt(7)\n"
      "ds_read_b64_tr_b16 v[86:87], v18 offset:13824\n"
      "v_mfma_f32_16x16x32_bf16 v[40:43], v[100:103], v[136:139], v[40:43]\n"
      "ds_read_b64_tr_b16 v[88:89], v18 offset:25600\n"
      "v_mfma_f32_16x16x32_bf16 v[44:47], v[100:103], v[140:143], v[44:47]\n"
      "s_waitcnt lgkmcnt(7)\n"
      "ds_read_b64_tr_b16 v[90:91], v18 offset:26112\n"
      "v_mul_f32 v32, s80, v32\n"
      "v_mul_f32 v33, s80, v33\n"
      "v_mfma_f32_16x16x32_bf16 v[40:43], v[104:107], v[144:147], v[40:43]\n"
      "ds_read_b64_tr_b16 v[92:93], v18 offset:37888\n"
      "v_mul_f32 v34, s80, v34\n"
      "v_mul_f32 v35, s80, v35\n"
      "v_mfma_f32_16x16x32_bf16 v[44:47], v[104:107], v[148:151], v[44:47]\n"
      "s_waitcnt lgkmcnt(7)\n"
      "ds_read_b64_tr_b16 v[94:95], v18 offset:38400\n"
      "v_mul_f32 v36, s80, v36\n"
      "v_mul_f32 v37, s80, v37\n"
      "v_mfma_f32_16x16x32_bf16 v[40:43], v[108:111], v[112:115], v[40:43]\n"
      "ds_read_b64_tr_b16 v[96:97], v19 offset:1024\n"
      "v_mul_f32 v38, s80, v38\n"
      "v_mul_f32 v39, s80, v39\n"
      "v_mfma_f32_16x16x32_bf16 v[44:47], v[108:111], v[116:119], v[44:47]\n"
      "s_waitcnt lgkmcnt(7)\n"
      "ds_read_b64_tr_b16 v[98:99], v19 offset:1536\n"
      "v_cvt_pk_bf16_f32 v160, v32, v33\n"
      "v_cvt_pk_bf16_f32 v161, v34, v35\n"
      "v_mfma_f32_16x16x32_bf16 v[48:51], v[80:83], v[128:131], v[48:51]\n"
      "ds_read_b64_tr_b16 v[100:101], v19 offset:13312\n"
      "v_cvt_pk_bf16_f32 v162, v36, v37\n"
      "v_cvt_pk_bf16_f32 v163, v38, v39\n"
      "v_mfma_f32_16x16x32_bf16 v[52:55], v[80:83], v[132:135], v[52:55]\n"
      "s_waitcnt lgkmcnt(7)\n"
      "ds_read_b64_tr_b16 v[102:103], v19 offset:13824\n"
      "v_permlane16_swap_b32 v160, v162\n"
      "v_permlane16_swap_b32 v161, v163\n"
      "v_mfma_f32_16x16x32_bf16 v[48:51], v[84:87], v[136:139], v[48:51]\n"
      "ds_read_b64_tr_b16 v[104:105], v19 offset:25600\n"
      "buffer_store_dwordx4 v[160:163], v254, s[8:11], s84 offen offset:0 sc1\n"
      "v_mfma_f32_16x16x32_bf16 v[52:55], v[84:87], v[140:143], v[52:55]\n"
      "s_waitcnt lgkmcnt(7)\n"
      "ds_read_b64_tr_b16 v[106:107], v19 offset:26112\n"
      "v_mul_f32 v40, s80, v40\n"
      "v_mfma_f32_16x16x32_bf16 v[48:51], v[88:91], v[144:147], v[48:51]\n"
      "ds_read_b64_tr_b16 v[108:109], v19 offset:37888\n"
      "v_mul_f32 v41, s80, v41\n"
      "v_mul_f32 v42, s80, v42\n"
      "v_mfma_f32_16x16x32_bf16 v[52:55], v[88:91], v[148:151], v[52:55]\n"
      "s_waitcnt lgkmcnt(7)\n"
      "ds_read_b64_tr_b16 v[110:111], v19 offset:38400\n"
      "v_mul_f32 v43, s80, v43\n"
      "v_mul_f32 v44, s80, v44\n"
      "v_mfma_f32_16x16x32_bf16 v[48:51], v[92:95], v[112:115], v[48:51]\n"
      "ds_read_b64_tr_b16 v[80:81], v18 offset:2048\n"
      "v_mul_f32 v45, s80, v45\n"
      "v_mul_f32 v46, s80, v46\n"
      "v_mfma_f32_16x16x32_bf16 v[52:55], v[92:95], v[116:119], v[52:55]\n"
      "s_waitcnt lgkmcnt(7)\n"
      "ds_read_b64_tr_b16 v[82:83], v18 offset:2560\n"
      "v_mul_f32 v47, s80, v47\n"
      "v_cvt_pk_bf16_f32 v164, v40, v41\n"
      "v_mfma_f32_16x16x32_bf16 v[56:59], v[96:99], v[128:131], v[56:59]\n"
      "ds_read_b64_tr_b16 v[84:85], v18 offset:14336\n"
      "v_cvt_pk_bf16_f32 v165, v42, v43\n"
      "v_cvt_pk_bf16_f32 v166, v44, v45\n"
      "v_mfma_f32_16x16x32_bf16 v[60:63], v[96:99], v[132:135], v[60:63]\n"
      "s_waitcnt lgkmcnt(7)\n"
      "ds_read_b64_tr_b16 v[86:87], v18 offset:14848\n"
      "v_cvt_pk_bf16_f32 v167, v46, v47\n"
      "v_permlane16_swap_b32 v164, v166\n"
      "v_mfma_f32_16x16x32_bf16 v[56:59], v[100:103], v[136:139], v[56:59]\n"
      "ds_read_b64_tr_b16 v[88:89], v18 offset:26624\n"
      "v_permlane16_swap_b32 v165, v167\n"
      "buffer_store_dwordx4 v[164:167], v254, s[8:11], s85 offen offset:0 sc1\n"
      "v_mfma_f32_16x16x32_bf16 v[60:63], v[100:103], v[140:143], v[60:63]\n"
      "s_waitcnt lgkmcnt(7)\n"
      "ds_read_b64_tr_b16 v[90:91], v18 offset:27136\n"
      "v_mfma_f32_16x16x32_bf16 v[56:59], v[104:107], v[144:147], v[56:59]\n"
      "ds_read_b64_tr_b16 v[92:93], v18 offset:38912\n"
      "v_cvt_pk_bf16_f32 v168, v48, v49\n"
      "v_cvt_pk_bf16_f32 v169, v50, v51\n"
      "v_mfma_f32_16x16x32_bf16 v[60:63], v[104:107], v[148:151], v[60:63]\n"
      "s_waitcnt lgkmcnt(7)\n"
      "ds_read_b64_tr_b16 v[94:95], v18 offset:39424\n"
      "v_cvt_pk_bf16_f32 v170, v52, v53\n"
      "v_cvt_pk_bf16_f32 v171, v54, v55\n"
      "v_mfma_f32_16x16x32_bf16 v[56:59], v[108:111], v[112:115], v[56:59]\n"
      "ds_read_b64_tr_b16 v[96:97], v19 offset:2048\n"
      "v_permlane16_swap_b32 v168, v170\n"
      "v_permlane16_swap_b32 v169, v171\n"
      "v_mfma_f32_16x16x32_bf16 v[60:63], v[108:111], v[116:119], v[60:63]\n"
      "s_waitcnt lgkmcnt(7)\n"
      "ds_read_b64_tr_b16 v[98:99], v19 offset:2560\n"
      "buffer_store_dwordx4 v[168:171], v20, s[8:11], s82 offen offset:0 sc1\n"
      "v_mfma_f32_16x16x32_bf16 v[64:67], v[128:131], v[80:83], v[64:67]\n"
      "ds_read_b64_tr_b16 v[100:101], v19 offset:14336\n"
      "v_mfma_f32_16x16x32_bf16 v[68:71], v[132:135], v[80:83], v[68:71]\n"
      "s_waitcnt lgkmcnt(7)\n"
      "ds_read_b64_tr_b16 v[102:103], v19 offset:14848\n"
      "v_mfma_f32_16x16x32_bf16 v[64:67], v[136:139], v[84:87], v[64:67]\n"
      "ds_read_b64_tr_b16 v[104:105], v19 offset:26624\n"
      "v_mfma_f32_16x16x32_bf16 v[68:71], v[140:143], v[84:87], v[68:71]\n"
      "s_waitcnt lgkmcnt(7)\n"
      "ds_read_b64_tr_b16 v[106:107], v19 offset:27136\n"
      "v_cvt_pk_bf16_f32 v172, v56, v57\n"
      "v_cvt_pk_bf16_f32 v173, v58, v59\n"
      "v_mfma_f32_16x16x32_bf16 v[64:67], v[144:147], v[88:91], v[64:67]\n"
      "ds_read_b64_tr_b16 v[108:109], v19 offset:38912\n"
      "v_cvt_pk_bf16_f32 v174, v60, v61\n"
      "v_cvt_pk_bf16_f32 v175, v62, v63\n"
      "v_mfma_f32_16x16x32_bf16 v[68:71], v[148:151], v[88:91], v[68:71]\n"
      "s_waitcnt lgkmcnt(7)\n"
      "ds_read_b64_tr_b16 v[110:111], v19 offset:39424\n"
      "v_permlane16_swap_b32 v172, v174\n"
      "v_permlane16_swap_b32 v173, v175\n"
      "v_mfma_f32_16x16x32_bf16 v[64:67], v[112:115], v[92:95], v[64:67]\n"
      "buffer_store_dwordx4 v[172:175], v20, s[8:11], s83 offen offset:0 sc1\n"
      "v_mfma_f32_16x16x32_bf16 v[68:71], v[116:119], v[92:95], v[68:71]\n"
      "s_waitcnt lgkmcnt(6)\n"
      "v_mfma_f32_16x16x32_bf16 v[72:75], v[128:131], v[96:99], v[72:75]\n"
      "v_mfma_f32_16x16x32_bf16 v[76:79], v[132:135], v[96:99], v[76:79]\n"
      "s_waitcnt lgkmcnt(4)\n"
      "v_mfma_f32_16x16x32_bf16 v[72:75], v[136:139], v[100:103], v[72:75]\n"
      "v_mfma_f32_16x16x32_bf16 v[76:79], v[140:143], v[100:103], v[76:79]\n"
      "s_waitcnt lgkmcnt(2)\n"
      "v_cvt_pk_bf16_f32 v176, v64, v65\n"
      "v_cvt_pk_bf16_f32 v177, v66, v67\n"
      "v_mfma_f32_16x16x32_bf16 v[72:75], v[144:147], v[104:107], v[72:75]\n"
      "v_cvt_pk_bf16_f32 v178, v68, v69\n"
      "v_cvt_pk_bf16_f32 v179, v70, v71\n"
      "v_mfma_f32_16x16x32_bf16 v[76:79], v[148:151], v[104:107], v[76:79]\n"
      "s_waitcnt lgkmcnt(0)\n"
      "buffer_store_dwordx4 v[176:179], v254, s[8:11], s82 offen offset:1024 sc1\n"
      "s_barrier\n"
      "v_mfma_f32_16x16x32_bf16 v[72:75], v[112:115], v[108:111], v[72:75]\n"
      "v_mfma_f32_16x16x32_bf16 v[76:79], v[116:119], v[108:111], v[76:79]\n"
      "s_nop 6\n"
      "v_cvt_pk_bf16_f32 v180, v72, v73\n"
      "v_cvt_pk_bf16_f32 v181, v74, v75\n"
      "v_cvt_pk_bf16_f32 v182, v76, v77\n"
      "v_cvt_pk_bf16_f32 v183, v78, v79\n"
      "buffer_store_dwordx4 v[180:183], v254, s[8:11], s83 offen offset:1024 sc1\n"
      "ds_write_b32 v10, v5 offset:24576\n"
      "ds_read_b128 v[32:35], v8 offset:24576\n"
      "ds_read_b128 v[36:39], v8 offset:24640\n"
      "ds_read_b128 v[40:43], v8 offset:24704\n"
      "ds_read_b128 v[44:47], v8 offset:24768\n"
      "s_waitcnt vmcnt(8)\n"
      "ds_read_b64_tr_b16 v[48:49], v9 offset:2048\n"
      "ds_read_b64_tr_b16 v[50:51], v9 offset:2176\n"
      "ds_read_b64_tr_b16 v[52:53], v9 offset:3072\n"
      "ds_read_b64_tr_b16 v[54:55], v9 offset:3200\n"
      "ds_read_b64_tr_b16 v[56:57], v9 offset:4096\n"
      "ds_read_b64_tr_b16 v[58:59], v9 offset:4224\n"
      "ds_read_b64_tr_b16 v[60:61], v9 offset:5120\n"
      "ds_read_b64_tr_b16 v[62:63], v9 offset:5248\n"
      "s_waitcnt lgkmcnt(6)\n"
      "v_mfma_f32_16x16x32_bf16 v[28:31], v[32:35], v[48:51], 0\n"
      "s_waitcnt lgkmcnt(4)\n"
      "v_mfma_f32_16x16x32_bf16 v[28:31], v[36:39], v[52:55], v[28:31]\n"
      "s_waitcnt lgkmcnt(2)\n"
      "v_mfma_f32_16x16x32_bf16 v[28:31], v[40:43], v[56:59], v[28:31]\n"
      "s_waitcnt lgkmcnt(0)\n"
      "v_mfma_f32_16x16x32_bf16 v[28:31], v[44:47], v[60:63], v[28:31]\n"
      "s_nop 7\n"
      "ds_write_b32 v11, v28\n"
      "s_lshr_b32 s68, s55, 3\n"
      "s_lshl2_add_u32 s68, s54, s68\n"
      "s_lshl_b32 s69, s68, 17\n"
      "s_lshl_b32 s63, s53, 12\n"
      "s_add_u32 s69, s69, s63\n"
      "s_add_u32 s69, s69, 3145728\n"
      "s_mul_i32 s70, s53, 2080\n"
      "s_lshl_b32 s72, s53, 14\n"
      "s_lshl_b32 s63, s59, 7\n"
      "s_add_u32 s72, s72, s63\n"
      "s_lshl_b32 s73, s53, 11\n"
      "s_lshl_b32 s63, s59, 16\n"
      "s_add_u32 s72, s72, s63\n"
      "s_lshl2_add_u32 s77, s54, s53\n"
      "s_mul_i32 s77, s77, 256\n"
      "s_add_u32 s77, s77, 21561344\n"
      "s_lshl_b32 s71, s68, 6\n"
      "s_lshl3_add_u32 s71, s91, s71\n"
      "s_lshl1_add_u32 s71, s53, s71\n"
      "s_lshl_b32 s71, s71, 10\n"
      "s_add_u32 s71, s71, 1048576\n"
      "s_lshl_b32 s80, s53, 14\n"
      "s_lshl1_add_u32 s82, s58, s60\n"
      "s_lshl_b32 s82, s82, 15\n"
      "s_lshl1_add_u32 s63, s59, s61\n"
      "s_lshl_b32 s63, s63, 6\n"
      "s_add_u32 s82, s82, s63\n"
      "s_lshl_b32 s83, s54, 20\n"
      "s_mul_i32 s63, s59, 229376\n"
      "s_add_u32 s63, s63, 19660800\n"
      "s_min_u32 s65, s58, 13\n"
      "s_lshl_b32 s65, s65, 10\n"
      "s_add_u32 s65, s65, s63\n"
      "s_lshl_b32 s64, s53, 1\n"
      "s_xor_b32 s64, s64, s59\n"
      "s_mul_i32 s85, s64, 28672\n"
      "s_add_u32 s85, s85, s65\n"
      "s_xor_b32 s64, s64, 1\n"
      "s_mul_i32 s86, s64, 28672\n"
      "s_add_u32 s86, s86, s65\n"
      "s_lshl_b32 s84, s53, 9\n"
      "s_add_u32 s84, s84, 59392\n"
      "s_and_b32 s64, s58, 7\n"
      "s_lshl2_add_u32 s64, s64, s53\n"
      "s_mul_i32 s64, s64, 6272\n"
      "s_add_u32 s95, s64, s63\n"
      "s_add_u32 s95, s95, 28672\n"
      "s_lshl2_add_u32 s89, s59, s53\n"
      "s_mul_i32 s89, s89, 256\n"
      "s_add_u32 s89, s89, 21561344\n"
      "s_lshl_b32 s96, s58, 18\n"
      "s_lshl_b32 s63, s53, 10\n"
      "s_add_u32 s96, s96, s63\n"
      "s_lshl_b32 s63, s60, 17\n"
      "s_xor_b32 s64, s63, 0x20000\n"
      "s_add_u32 s98, s96, s64\n"
      "s_add_u32 s96, s96, s63\n"
      "s_add_u32 s97, s96, 16384\n"
      "s_add_u32 s99, s98, 16384\n"
      "s_lshl1_add_u32 s87, s59, s61\n"
      "s_lshl_b32 s87, s87, 7\n"
      "s_add_u32 s87, s87, 57344\n"
      "s_mul_i32 s92, s59, 229376\n"
      "s_mul_i32 s63, s60, 14336\n"
      "s_add_u32 s92, s92, s63\n"
      "s_add_u32 s92, s92, 19660800\n"
      "s_mul_i32 s63, s61, 3072\n"
      "s_add_u32 s93, s92, s63\n"
      "s_add_u32 s93, s93, 8192\n"
      "s_lshl_b32 s63, s61, 12\n"
      "s_add_u32 s92, s92, s63\n"
      "s_lshl_b32 s40, s53, 12\n"
      "s_mul_i32 s41, s53, 3072\n"
      "v_and_b32 v150, 31, v255\n"
      "v_lshrrev_b32 v155, 5, v255\n"
      "v_lshlrev_b32 v151, 8, v155\n"
      "v_lshl_add_u32 v151, v150, 4, v151\n"
      "v_add_u32 v151, 1024, v151\n"
      "v_add_u32 v152, 512, v151\n"
      "v_mov_b32 v153, 2064\n"
      "v_mov_b32 v154, 2048\n"
      "v_cmp_eq_u32 vcc, 16, v150\n"
      "s_nop 1\n"
      "Lhs0_2_%=:\n"
      "s_cbranch_execz Lhr0_%=\n"
      "v_cndmask_b32 v153, v153, v154, vcc\n"
      "v_cmp_gt_u32 vcc, 16, v150\n"
      "s_nop 1\n"
      "v_cndmask_b32 v134, v153, v151, vcc\n"
      "v_cndmask_b32 v136, v153, v152, vcc\n"
      "v_mov_b32_e64 v132, v254\n"
      "v_add_u32 v133, 65536, v132\n"
      "v_add_u32 v135, 65536, v134\n"
      "v_add_u32 v137, 65536, v136\n"
      "v_mov_b32 v138, 0xdddddddd\n"
      "v_add_u32 v147, 7168, v249\n"
      "v_bfe_u32 v145, v255, 3, 2\n"
      "v_lshlrev_b32 v145, 11, v145\n"
      "v_and_b32 v151, 0x70, v254\n"
      "v_add_u32 v145, v145, v151\n"
      "s_lshl2_add_u32 s63, s55, s53\n"
      "s_lshl_b32 s63, s63, 7\n"
      "v_lshlrev_b32 v139, 14, v255\n"
      "v_add_u32 v139, s63, v139\n"
      "v_lshlrev_b32 v141, 10, v150\n"
      "v_lshl_add_u32 v141, v155, 3, v141\n"
      "s_lshl2_add_u32 s63, s55, s53\n"
      "s_mul_i32 s63, s63, 56\n"
      "s_and_b32 s64, s59, 4\n"
      "s_add_u32 s63, s63, s64\n"
      "v_lshrrev_b32 v151, 2, v255\n"
      "v_and_b32 v140, 3, v255\n"
      "v_lshl_add_u32 v140, v151, 3, v140\n"
      "v_add_u32 v140, s63, v140\n"
      "v_lshlrev_b32 v140, 7, v140\n"
      "v_mov_b32 v152, 0x40000000\n"
      "v_cmp_lt_u32 vcc, 6, v151\n"
      "s_nop 1\n"
      "v_cndmask_b32 v140, v140, v152, vcc\n"
      "v_and_b32 v151, 1, v255\n"
      "v_xor_b32 v150, 1, v155\n"
      "v_lshlrev_b32 v150, 11, v150\n"
      "v_and_b32 v152, 0x1f0, v254\n"
      "v_add_u32 v228, v150, v152\n"
      "s_cmp_lt_u32 s58, 14\n"
      "s_cselect_b32 s63, 0, 0x40000000\n"
      "v_or_b32 v229, s63, v254\n"
      "v_lshlrev_b32 v230, 7, v255\n"
      "v_mov_b32 v150, 0x40000000\n"
      "v_cmp_le_u32 vcc, 25, v255\n"
      "s_nop 1\n"
      "v_cndmask_b32 v230, v230, v150, vcc\n"
      "v_add_u32 v231, 3200, v230\n"
      "v_lshrrev_b32 v150, 3, v255\n"
      "v_xor_b32 v151, v150, v255\n"
      "v_and_b32 v151, 7, v151\n"
      "v_lshl_add_u32 v150, v155, 2, v150\n"
      "v_lshlrev_b32 v150, 12, v150\n"
      "v_lshl_add_u32 v142, v151, 4, v150\n"
      "v_add_u32 v148, 64512, v142\n"
      "v_lshrrev_b32 v150, 4, v255\n"
      "v_xor_b32 v150, v150, v255\n"
      "v_and_b32 v150, 7, v150\n"
      "v_and_b32 v151, 15, v255\n"
      "v_lshlrev_b32 v151, 7, v151\n"
      "v_lshl_add_u32 v150, v150, 4, v151\n"
      "s_lshl_b32 s63, s53, 14\n"
      "v_add_u32 v143, s63, v150\n"
      "v_xor_b32 v144, 64, v143\n"
      "s_lshl2_add_u32 s63, s52, s53\n"
      "s_lshl_b32 s63, s63, 10\n"
      "v_lshlrev_b32 v149, 7, v255\n"
      "v_add_u32 v149, s63, v149\n"
      "v_mov_b32 v150, 0x40000000\n"
      "v_cmp_lt_u32 vcc, 7, v255\n"
      "s_nop 1\n"
      "v_cndmask_b32 v149, v149, v150, vcc\n"
      "v_and_b32 v150, 31, v255\n"
      "v_mul_u32_u24 v150, 2080, v150\n"
      "v_add_u32 v146, 65536, v150\n"
      "v_and_b32 v248, 7, v255\n"
      "v_lshlrev_b32 v248, 3, v248\n"
      "s_cmp_lg_u32 s53, 0\n"
      "s_waitcnt vmcnt(0) lgkmcnt(0)\n"
      "s_barrier\n"
      "s_cbranch_scc1 Lf9_%=\n"
      "s_mov_b64 exec, 1\n"
      "buffer_store_dwordx2 v[252:253], off, s[8:11], s90 sc1\n"
      "s_mov_b64 exec, -1\n"
      "Lf9_%=:\n"
      "s_barrier\n"
      "s_mov_b64 exec, 0\n"
      "s_cmp_eq_u32 s53, 0\n"
      "s_cbranch_scc1 Lhs1_3_%=\n"
      "s_cmp_eq_u32 s53, 1\n"
      "s_cbranch_scc1 Lhs1_0_%=\n"
      "s_cmp_eq_u32 s53, 2\n"
      "s_cbranch_scc1 Lhs1_1_%=\n"
      "s_branch Lhs1_2_%=\n"
      "Lhr1_%=:\n"
      "s_mov_b64 exec, -1\n"
      "s_sleep 2\n"
      "buffer_load_dwordx2 v[126:127], v248, s[8:11], s77 offen sc1\n"
      "s_cmp_lg_u32 s53, 3\n"
      "s_cbranch_scc1 Lm10_%=\n"
      "s_sub_u32 s63, s55, 8\n"
      "s_cmp_ge_u32 s63, 16\n"
      "s_cbranch_scc1 Lm10_%=\n"
      "ds_read_b128 v[64:67], v12 offset:0\n"
      "ds_read_b128 v[68:71], v12 offset:64\n"
      "ds_read_b128 v[72:75], v12 offset:128\n"
      "ds_read_b128 v[76:79], v12 offset:192\n"
      "s_lshl_b32 s63, s75, 6\n"
      "s_add_u32 s63, s63, 53248\n"
      "s_waitcnt lgkmcnt(0)\n"
      "v_pk_add_f32 v[64:65], v[64:65], v[68:69]\n"
      "v_pk_add_f32 v[66:67], v[66:67], v[70:71]\n"
      "v_pk_add_f32 v[72:73], v[72:73], v[76:77]\n"
      "v_pk_add_f32 v[74:75], v[74:75], v[78:79]\n"
      "v_pk_add_f32 v[64:65], v[64:65], v[72:73]\n"
      "v_pk_add_f32 v[66:67], v[66:67], v[74:75]\n"
      "v_cvt_pk_bf16_f32 v68, v64, v65\n"
      "v_cvt_pk_bf16_f32 v69, v66, v67\n"
      "v_lshlrev_b32 v64, 16, v68\n"
      "v_and_b32 v65, 0xffff0000, v68\n"
      "v_lshlrev_b32 v66, 16, v69\n"
      "v_and_b32 v67, 0xffff0000, v69\n"
      "v_add_f32 v64, s76, v64\n"
      "v_add_f32 v65, s76, v65\n"
      "v_add_f32 v66, s76, v66\n"
      "v_add_f32 v67, s76, v67\n"
      "v_cvt_pk_bf16_f32 v24, v64, v65\n"
      "v_mov_b32 v25, s56\n"
      "v_cvt_pk_bf16_f32 v26, v66, v67\n"
      "v_mov_b32 v27, s57\n"
      "s_mov_b64 exec, 15\n"
      "buffer_store_dwordx4 v[24:27], v254, s[8:11], s63 offen sc1\n"
      "s_mov_b64 exec, -1\n"
      "Lm10_%=:\n"
      "v_mov_b32 v116, 0x3f803f80\n"
      "v_mov_b32 v117, 0x3f803f80\n"
      "v_mov_b32 v118, 0x3f803f80\n"
      "v_mov_b32 v119, 0x3f803f80\n"
      "v_mov_b32 v32, 0\n"
      "v_mov_b32 v33, 0\n"
      "v_mov_b32 v34, 0\n"
      "v_mov_b32 v35, 0\n"
      "v_mov_b32 v36, 0\n"
      "v_mov_b32 v37, 0\n"
      "v_mov_b32 v38, 0\n"
      "v_mov_b32 v39, 0\n"
      "v_mov_b32 v40, 0\n"
      "v_mov_b32 v41, 0\n"
      "v_mov_b32 v42, 0\n"
      "v_mov_b32 v43, 0\n"
      "v_mov_b32 v44, 0\n"
      "v_mov_b32 v45, 0\n"
      "v_mov_b32 v46, 0\n"
      "v_mov_b32 v47, 0\n"
      "v_mov_b32 v48, 0\n"
      "v_mov_b32 v49, 0\n"
      "v_mov_b32 v50, 0\n"
      "v_mov_b32 v51, 0\n"
      "v_mov_b32 v52, 0\n"
      "v_mov_b32 v53, 0\n"
      "v_mov_b32 v54, 0\n"
      "v_mov_b32 v55, 0\n"
      "v_mov_b32 v56, 0\n"
      "v_mov_b32 v57, 0\n"
      "v_mov_b32 v58, 0\n"
      "v_mov_b32 v59, 0\n"
      "v_mov_b32 v60, 0\n"
      "v_mov_b32 v61, 0\n"
      "v_mov_b32 v62, 0\n"
      "v_mov_b32 v63, 0\n"
      "ds_write_b128 v146, v[116:119] offset:2048\n"
      "ds_write_b128 v146, v[32:35] offset:2064\n"
      "Lp12_%=:\n"
      "s_waitcnt vmcnt(0)\n"
      "v_cmp_ne_u64 vcc, s[56:57], v[126:127]\n"
      "s_nop 0\n"
      "s_cmp_eq_u64 vcc, 0\n"
      "s_cbranch_scc1 Lpd13_%=\n"
      "s_cmp_eq_u32 s62, 0\n"
      "s_cbranch_scc1 Lpd13_%=\n"
      "s_sub_u32 s62, s62, 1\n"
      "s_sleep 1\n"
      "buffer_load_dwordx2 v[126:127], v248, s[8:11], s77 offen sc1\n"
      "s_branch Lp12_%=\n"
      "Lpd13_%=:\n"
      "buffer_load_dwordx4 v[96:99], v254, s[8:11], s71 offen sc1\n"
      "buffer_load_dwordx4 v[100:103], v254, s[8:11], s71 offen offset:1024 sc1\n"
      "s_add_u32 m0, s70, 65536\n"
      "s_xor_b32 s67, s69, 0\n"
      "buffer_load_dwordx4 v254, s[8:11], s67 offen sc1 lds\n"
      "buffer_load_dwordx4 v254, s[8:11], s67 offen offset:1024 sc1 lds\n"
      "s_add_u32 m0, s70, 73856\n"
      "s_xor_b32 s67, s69, 2048\n"
      "buffer_load_dwordx4 v254, s[8:11], s67 offen sc1 lds\n"
      "buffer_load_dwordx4 v254, s[8:11], s67 offen offset:1024 sc1 lds\n"
      "s_waitcnt lgkmcnt(0)\n"
      "s_waitcnt vmcnt(2)\n"
      "s_barrier\n"
      "ds_read_b128 v[104:107], v133 offset:0\n"
      "ds_read_b128 v[108:111], v135 offset:0\n"
      "ds_read_b128 v[112:115], v137 offset:0\n"
      "ds_read_b128 v[208:211], v133 offset:2080\n"
      "ds_read_b128 v[212:215], v135 offset:2080\n"
      "s_add_u32 m0, s70, 82176\n"
      "s_xor_b32 s67, s69, 16384\n"
      "buffer_load_dwordx4 v254, s[8:11], s67 offen sc1 lds\n"
      "buffer_load_dwordx4 v254, s[8:11], s67 offen offset:1024 sc1 lds\n"
      "s_waitcnt lgkmcnt(4)\n"
      "v_mfma_f32_32x32x16_bf16 v[0:15], v[104:107], v[96:99], 0\n"
      "v_mfma_f32_32x32x16_bf16 v[16:31], v[104:107], v[100:103], 0\n"
      "s_add_u32 m0, s70, 90496\n"
      "s_xor_b32 s67, s69, 18432\n"
      "buffer_load_dwordx4 v254, s[8:11], s67 offen sc1 lds\n"
      "buffer_load_dwordx4 v254, s[8:11], s67 offen offset:1024 sc1 lds\n"
      "s_nop 6\n"
      "v_max3_f32 v64, v0, v1, v2\n"
      "v_max3_f32 v64, v64, v3, v4\n"
      "v_max3_f32 v64, v64, v5, v6\n"
      "v_max3_f32 v64, v64, v7, v8\n"
      "v_max3_f32 v64, v64, v9, v10\n"
      "v_max3_f32 v64, v64, v11, v12\n"
      "v_max3_f32 v64, v64, v13, v14\n"
      "v_max_f32 v64, v64, v15\n"
      "v_mov_b32 v65, v64\n"
      "s_nop 1\n"
      "v_permlane32_swap_b32 v64, v65\n"
      "v_max_f32 v64, v64, v65\n"
      "v_mul_f32 v64, -1.0, v64\n"
      "v_mov_b32 v65, v64\n"
      "v_mov_b64 v[66:67], v[64:65]\n"
      "v_mov_b64 v[68:69], v[64:65]\n"
      "v_mov_b64 v[70:71], v[64:65]\n"
      "v_mov_b64 v[72:73], v[64:65]\n"
      "v_mov_b64 v[74:75], v[64:65]\n"
      "v_mov_b64 v[76:77], v[64:65]\n"
      "v_mov_b64 v[78:79], v[64:65]\n"
      "s_nop 1\n"
      "v_mfma_f32_32x32x16_bf16 v[0:15], v[104:107], v[96:99], v[64:79]\n"
      "s_nop 0\n"
      "v_max3_f32 v80, v16, v17, v18\n"
      "v_max3_f32 v80, v80, v19, v20\n"
      "v_max3_f32 v80, v80, v21, v22\n"
      "v_max3_f32 v80, v80, v23, v24\n"
      "v_max3_f32 v80, v80, v25, v26\n"
      "v_max3_f32 v80, v80, v27, v28\n"
      "v_max3_f32 v80, v80, v29, v30\n"
      "v_max_f32 v80, v80, v31\n"
      "v_mov_b32 v81, v80\n"
      "s_nop 1\n"
      "v_permlane32_swap_b32 v80, v81\n"
      "v_max_f32 v80, v80, v81\n"
      "v_mul_f32 v80, -1.0, v80\n"
      "v_mov_b32 v81, v80\n"
      "v_mov_b64 v[82:83], v[80:81]\n"
      "v_mov_b64 v[84:85], v[80:81]\n"
      "v_mov_b64 v[86:87], v[80:81]\n"
      "v_mov_b64 v[88:89], v[80:81]\n"
      "v_mov_b64 v[90:91], v[80:81]\n"
      "v_mov_b64 v[92:93], v[80:81]\n"
      "v_mov_b64 v[94:95], v[80:81]\n"
      "v_exp_f32 v0, v0\n"
      "v_exp_f32 v1, v1\n"
      "ds_read_b128 v[216:219], v137 offset:2080\n"
      "v_exp_f32 v2, v2\n"
      "v_exp_f32 v3, v3\n"
      "v_exp_f32 v4, v4\n"
      "v_mfma_f32_32x32x16_bf16 v[16:31], v[104:107], v[100:103], v[80:95]\n"
      "s_waitcnt lgkmcnt(2)\n"
      "ds_read_b128 v[104:107], v133 offset:4160\n"
      "v_exp_f32 v5, v5\n"
      "v_exp_f32 v6, v6\n"
      "v_exp_f32 v7, v7\n"
      "v_exp_f32 v8, v8\n"
      "v_exp_f32 v9, v9\n"
      "v_exp_f32 v10, v10\n"
      "v_smfmac_f32_32x32x32_bf16 v[32:47], v[108:111], v[0:7], v138\n"
      "s_add_u32 m0, s70, 98816\n"
      "s_xor_b32 s67, s69, 32768\n"
      "buffer_load_dwordx4 v254, s[8:11], s67 offen sc1 lds\n"
      "v_exp_f32 v11, v11\n"
      "v_exp_f32 v12, v12\n"
      "v_exp_f32 v13, v13\n"
      "v_exp_f32 v14, v14\n"
      "v_exp_f32 v15, v15\n"
      "v_exp_f32 v16, v16\n"
      "v_exp_f32 v17, v17\n"
      "v_smfmac_f32_32x32x32_bf16 v[32:47], v[112:115], v[8:15], v138\n"
      "buffer_load_dwordx4 v254, s[8:11], s67 offen offset:1024 sc1 lds\n"
      "v_exp_f32 v18, v18\n"
      "v_exp_f32 v19, v19\n"
      "v_exp_f32 v20, v20\n"
      "v_mfma_f32_32x32x16_bf16 v[0:15], v[208:211], v[96:99], v[64:79]\n"
      "v_exp_f32 v21, v21\n"
      "v_exp_f32 v22, v22\n"
      "v_exp_f32 v23, v23\n"
      "v_exp_f32 v24, v24\n"
      "v_exp_f32 v25, v25\n"
      "v_exp_f32 v26, v26\n"
      "v_smfmac_f32_32x32x32_bf16 v[48:63], v[108:111], v[16:23], v138\n"
      "ds_read_b128 v[108:111], v135 offset:4160\n"
      "v_exp_f32 v27, v27\n"
      "v_exp_f32 v28, v28\n"
      "v_exp_f32 v29, v29\n"
      "v_exp_f32 v30, v30\n"
      "v_exp_f32 v31, v31\n"
      "v_exp_f32 v0, v0\n"
      "v_exp_f32 v1, v1\n"
      "v_smfmac_f32_32x32x32_bf16 v[48:63], v[112:115], v[24:31], v138\n"
      "ds_read_b128 v[112:115], v137 offset:4160\n"
      "v_exp_f32 v2, v2\n"
      "v_exp_f32 v3, v3\n"
      "v_exp_f32 v4, v4\n"
      "v_mfma_f32_32x32x16_bf16 v[16:31], v[208:211], v[100:103], v[80:95]\n"
      "s_waitcnt lgkmcnt(2)\n"
      "ds_read_b128 v[208:211], v133 offset:6240\n"
      "v_exp_f32 v5, v5\n"
      "v_exp_f32 v6, v6\n"
      "v_exp_f32 v7, v7\n"
      "v_exp_f32 v8, v8\n"
      "v_exp_f32 v9, v9\n"
      "v_exp_f32 v10, v10\n"
      "v_smfmac_f32_32x32x32_bf16 v[32:47], v[212:215], v[0:7], v138\n"
      "s_add_u32 m0, s70, 107136\n"
      "s_xor_b32 s67, s69, 34816\n"
      "buffer_load_dwordx4 v254, s[8:11], s67 offen sc1 lds\n"
      "v_exp_f32 v11, v11\n"
      "v_exp_f32 v12, v12\n"
      "v_exp_f32 v13, v13\n"
      "v_exp_f32 v14, v14\n"
      "v_exp_f32 v15, v15\n"
      "v_exp_f32 v16, v16\n"
      "v_exp_f32 v17, v17\n"
      "v_smfmac_f32_32x32x32_bf16 v[32:47], v[216:219], v[8:15], v138\n"
      "buffer_load_dwordx4 v254, s[8:11], s67 offen offset:1024 sc1 lds\n"
      "v_exp_f32 v18, v18\n"
      "v_exp_f32 v19, v19\n"
      "v_exp_f32 v20, v20\n"
      "v_mfma_f32_32x32x16_bf16 v[0:15], v[104:107], v[96:99], v[64:79]\n"
      "s_waitcnt vmcnt(8)\n"
      "s_barrier\n"
      "v_exp_f32 v21, v21\n"
      "v_exp_f32 v22, v22\n"
      "v_exp_f32 v23, v23\n"
      "v_exp_f32 v24, v24\n"
      "v_exp_f32 v25, v25\n"
      "v_exp_f32 v26, v26\n"
      "v_smfmac_f32_32x32x32_bf16 v[48:63], v[212:215], v[16:23], v138\n"
      "s_add_u32 m0, s70, 115456\n"
      "s_xor_b32 s67, s69, 49152\n"
      "ds_read_b128 v[212:215], v135 offset:6240\n"
      "buffer_load_dwordx4 v254, s[8:11], s67 offen sc1 lds\n"
      "v_exp_f32 v27, v27\n"
      "v_exp_f32 v28, v28\n"
      "v_exp_f32 v29, v29\n"
      "v_exp_f32 v30, v30\n"
      "v_exp_f32 v31, v31\n"
      "v_exp_f32 v0, v0\n"
      "v_exp_f32 v1, v1\n"
      "v_smfmac_f32_32x32x32_bf16 v[48:63], v[216:219], v[24:31], v138\n"
      "ds_read_b128 v[216:219], v137 offset:6240\n"
      "buffer_load_dwordx4 v254, s[8:11], s67 offen offset:1024 sc1 lds\n"
      "v_exp_f32 v2, v2\n"
      "v_exp_f32 v3, v3\n"
      "v_exp_f32 v4, v4\n"
      "v_mfma_f32_32x32x16_bf16 v[16:31], v[104:107], v[100:103], v[80:95]\n"
      "s_waitcnt lgkmcnt(2)\n"
      "ds_read_b128 v[104:107], v133 offset:8320\n"
      "v_exp_f32 v5, v5\n"
      "v_exp_f32 v6, v6\n"
      "v_exp_f32 v7, v7\n"
      "v_exp_f32 v8, v8\n"
      "v_exp_f32 v9, v9\n"
      "v_exp_f32 v10, v10\n"
      "v_smfmac_f32_32x32x32_bf16 v[32:47], v[108:111], v[0:7], v138\n"
      "s_add_u32 m0, s70, 123776\n"
      "s_xor_b32 s67, s69, 51200\n"
      "buffer_load_dwordx4 v254, s[8:11], s67 offen sc1 lds\n"
      "v_exp_f32 v11, v11\n"
      "v_exp_f32 v12, v12\n"
      "v_exp_f32 v13, v13\n"
      "v_exp_f32 v14, v14\n"
      "v_exp_f32 v15, v15\n"
      "v_exp_f32 v16, v16\n"
      "v_exp_f32 v17, v17\n"
      "v_smfmac_f32_32x32x32_bf16 v[32:47], v[112:115], v[8:15], v138\n"
      "buffer_load_dwordx4 v254, s[8:11], s67 offen offset:1024 sc1 lds\n"
      "v_exp_f32 v18, v18\n"
      "v_exp_f32 v19, v19\n"
      "v_exp_f32 v20, v20\n"
      "v_mfma_f32_32x32x16_bf16 v[0:15], v[208:211], v[96:99], v[64:79]\n"
      "v_exp_f32 v21, v21\n"
      "v_exp_f32 v22, v22\n"
      "v_exp_f32 v23, v23\n"
      "v_exp_f32 v24, v24\n"
      "v_exp_f32 v25, v25\n"
      "v_exp_f32 v26, v26\n"
      "v_smfmac_f32_32x32x32_bf16 v[48:63], v[108:111], v[16:23], v138\n"
      "ds_read_b128 v[108:111], v135 offset:8320\n"
      "v_exp_f32 v27, v27\n"
      "v_exp_f32 v28, v28\n"
      "v_exp_f32 v29, v29\n"
      "v_exp_f32 v30, v30\n"
      "v_exp_f32 v31, v31\n"
      "v_exp_f32 v0, v0\n"
      "v_exp_f32 v1, v1\n"
      "v_smfmac_f32_32x32x32_bf16 v[48:63], v[112:115], v[24:31], v138\n"
      "ds_read_b128 v[112:115], v137 offset:8320\n"
      "v_exp_f32 v2, v2\n"
      "v_exp_f32 v3, v3\n"
      "v_exp_f32 v4, v4\n"
      "v_mfma_f32_32x32x16_bf16 v[16:31], v[208:211], v[100:103], v[80:95]\n"
      "s_waitcnt lgkmcnt(2)\n"
      "ds_read_b128 v[208:211], v133 offset:10400\n"
      "v_exp_f32 v5, v5\n"
      "v_exp_f32 v6, v6\n"
      "v_exp_f32 v7, v7\n"
      "v_exp_f32 v8, v8\n"
      "v_exp_f32 v9, v9\n"
      "v_exp_f32 v10, v10\n"
      "v_smfmac_f32_32x32x32_bf16 v[32:47], v[212:215], v[0:7], v138\n"
      "s_add_u32 m0, s80, 0\n"
      "s_xor_b32 s66, s96, 0\n"
      "buffer_load_dwordx4 v142, s[28:31], s66 offen offset:0 lds\n"
      "v_exp_f32 v11, v11\n"
      "v_exp_f32 v12, v12\n"
      "v_exp_f32 v13, v13\n"
      "v_exp_f32 v14, v14\n"
      "v_exp_f32 v15, v15\n"
      "v_exp_f32 v16, v16\n"
      "v_exp_f32 v17, v17\n"
      "v_smfmac_f32_32x32x32_bf16 v[32:47], v[216:219], v[8:15], v138\n"
      "buffer_load_dwordx4 v148, s[28:31], s66 offen offset:1024 lds\n"
      "v_exp_f32 v18, v18\n"
      "v_exp_f32 v19, v19\n"
      "v_exp_f32 v20, v20\n"
      "v_mfma_f32_32x32x16_bf16 v[0:15], v[104:107], v[96:99], v[64:79]\n"
      "v_exp_f32 v21, v21\n"
      "v_exp_f32 v22, v22\n"
      "v_exp_f32 v23, v23\n"
      "v_exp_f32 v24, v24\n"
      "v_exp_f32 v25, v25\n"
      "v_exp_f32 v26, v26\n"
      "v_smfmac_f32_32x32x32_bf16 v[48:63], v[212:215], v[16:23], v138\n"
      "ds_read_b128 v[212:215], v135 offset:10400\n"
      "v_exp_f32 v27, v27\n"
      "v_exp_f32 v28, v28\n"
      "v_exp_f32 v29, v29\n"
      "v_exp_f32 v30, v30\n"
      "v_exp_f32 v31, v31\n"
      "v_exp_f32 v0, v0\n"
      "v_exp_f32 v1, v1\n"
      "v_smfmac_f32_32x32x32_bf16 v[48:63], v[216:219], v[24:31], v138\n"
      "ds_read_b128 v[216:219], v137 offset:10400\n"
      "v_exp_f32 v2, v2\n"
      "v_exp_f32 v3, v3\n"
      "v_exp_f32 v4, v4\n"
      "v_mfma_f32_32x32x16_bf16 v[16:31], v[104:107], v[100:103], v[80:95]\n"
      "s_waitcnt lgkmcnt(2)\n"
      "ds_read_b128 v[104:107], v133 offset:12480\n"
      "v_exp_f32 v5, v5\n"
      "v_exp_f32 v6, v6\n"
      "v_exp_f32 v7, v7\n"
      "v_exp_f32 v8, v8\n"
      "v_exp_f32 v9, v9\n"
      "v_exp_f32 v10, v10\n"
      "v_smfmac_f32_32x32x32_bf16 v[32:47], v[108:111], v[0:7], v138\n"
      "s_add_u32 m0, s80, 2048\n"
      "s_xor_b32 s66, s96, 128\n"
      "buffer_load_dwordx4 v142, s[28:31], s66 offen offset:0 lds\n"
      "v_exp_f32 v11, v11\n"
      "v_exp_f32 v12, v12\n"
      "v_exp_f32 v13, v13\n"
      "v_exp_f32 v14, v14\n"
      "v_exp_f32 v15, v15\n"
      "v_exp_f32 v16, v16\n"
      "v_exp_f32 v17, v17\n"
      "v_smfmac_f32_32x32x32_bf16 v[32:47], v[112:115], v[8:15], v138\n"
      "buffer_load_dwordx4 v148, s[28:31], s66 offen offset:1024 lds\n"
      "v_exp_f32 v18, v18\n"
      "v_exp_f32 v19, v19\n"
      "v_exp_f32 v20, v20\n"
      "v_mfma_f32_32x32x16_bf16 v[0:15], v[208:211], v[96:99], v[64:79]\n"
      "v_exp_f32 v21, v21\n"
      "v_exp_f32 v22, v22\n"
      "v_exp_f32 v23, v23\n"
      "v_exp_f32 v24, v24\n"
      "v_exp_f32 v25, v25\n"
      "v_exp_f32 v26, v26\n"
      "v_smfmac_f32_32x32x32_bf16 v[48:63], v[108:111], v[16:23], v138\n"
      "ds_read_b128 v[108:111], v135 offset:12480\n"
      "v_exp_f32 v27, v27\n"
      "v_exp_f32 v28, v28\n"
      "v_exp_f32 v29, v29\n"
      "v_exp_f32 v30, v30\n"
      "v_exp_f32 v31, v31\n"
      "v_exp_f32 v0, v0\n"
      "v_exp_f32 v1, v1\n"
      "v_smfmac_f32_32x32x32_bf16 v[48:63], v[112:115], v[24:31], v138\n"
      "ds_read_b128 v[112:115], v137 offset:12480\n"
      "v_exp_f32 v2, v2\n"
      "v_exp_f32 v3, v3\n"
      "v_exp_f32 v4, v4\n"
      "v_mfma_f32_32x32x16_bf16 v[16:31], v[208:211], v[100:103], v[80:95]\n"
      "s_waitcnt lgkmcnt(2)\n"
      "ds_read_b128 v[208:211], v133 offset:14560\n"
      "v_exp_f32 v5, v5\n"
      "v_exp_f32 v6, v6\n"
      "v_exp_f32 v7, v7\n"
      "v_exp_f32 v8, v8\n"
      "v_exp_f32 v9, v9\n"
      "v_exp_f32 v10, v10\n"
      "v_smfmac_f32_32x32x32_bf16 v[32:47], v[212:215], v[0:7], v138\n"
      "s_add_u32 m0, s80, 4096\n"
      "s_xor_b32 s66, s96, 256\n"
      "buffer_load_dwordx4 v142, s[28:31], s66 offen offset:0 lds\n"
      "v_exp_f32 v11, v11\n"
      "v_exp_f32 v12, v12\n"
      "v_exp_f32 v13, v13\n"
      "v_exp_f32 v14, v14\n"
      "v_exp_f32 v15, v15\n"
      "v_exp_f32 v16, v16\n"
      "v_exp_f32 v17, v17\n"
      "v_smfmac_f32_32x32x32_bf16 v[32:47], v[216:219], v[8:15], v138\n"
      "buffer_load_dwordx4 v148, s[28:31], s66 offen offset:1024 lds\n"
      "v_exp_f32 v18, v18\n"
      "v_exp_f32 v19, v19\n"
      "v_exp_f32 v20, v20\n"
      "v_mfma_f32_32x32x16_bf16 v[0:15], v[104:107], v[96:99], v[64:79]\n"
      "s_waitcnt vmcnt(6)\n"
      "s_barrier\n"
      "v_exp_f32 v21, v21\n"
      "v_exp_f32 v22, v22\n"
      "v_exp_f32 v23, v23\n"
      "v_exp_f32 v24, v24\n"
      "v_exp_f32 v25, v25\n"
      "v_exp_f32 v26, v26\n"
      "v_smfmac_f32_32x32x32_bf16 v[48:63], v[212:215], v[16:23], v138\n"
      "ds_read_b128 v[212:215], v135 offset:14560\n"
      "v_exp_f32 v27, v27\n"
      "v_exp_f32 v28, v28\n"
      "v_exp_f32 v29, v29\n"
      "v_exp_f32 v30, v30\n"
      "v_exp_f32 v31, v31\n"
      "v_exp_f32 v0, v0\n"
      "v_exp_f32 v1, v1\n"
      "v_smfmac_f32_32x32x32_bf16 v[48:63], v[216:219], v[24:31], v138\n"
      "ds_read_b128 v[216:219], v137 offset:14560\n"
      "v_exp_f32 v2, v2\n"
      "v_exp_f32 v3, v3\n"
      "v_exp_f32 v4, v4\n"
      "v_mfma_f32_32x32x16_bf16 v[16:31], v[104:107], v[100:103], v[80:95]\n"
      "s_waitcnt lgkmcnt(2)\n"
      "ds_read_b128 v[104:107], v133 offset:16640\n"
      "v_exp_f32 v5, v5\n"
      "v_exp_f32 v6, v6\n"
      "v_exp_f32 v7, v7\n"
      "v_exp_f32 v8, v8\n"
      "v_exp_f32 v9, v9\n"
      "v_exp_f32 v10, v10\n"
      "v_smfmac_f32_32x32x32_bf16 v[32:47], v[108:111], v[0:7], v138\n"
      "s_add_u32 m0, s80, 6144\n"
      "s_xor_b32 s66, s96, 384\n"
      "buffer_load_dwordx4 v142, s[28:31], s66 offen offset:0 lds\n"
      "v_exp_f32 v11, v11\n"
      "v_exp_f32 v12, v12\n"
      "v_exp_f32 v13, v13\n"
      "v_exp_f32 v14, v14\n"
      "v_exp_f32 v15, v15\n"
      "v_exp_f32 v16, v16\n"
      "v_exp_f32 v17, v17\n"
      "v_smfmac_f32_32x32x32_bf16 v[32:47], v[112:115], v[8:15], v138\n"
      "buffer_load_dwordx4 v148, s[28:31], s66 offen offset:1024 lds\n"
      "v_exp_f32 v18, v18\n"
      "v_exp_f32 v19, v19\n"
      "v_exp_f32 v20, v20\n"
      "v_mfma_f32_32x32x16_bf16 v[0:15], v[208:211], v[96:99], v[64:79]\n"
      "v_exp_f32 v21, v21\n"
      "v_exp_f32 v22, v22\n"
      "v_exp_f32 v23, v23\n"
      "v_exp_f32 v24, v24\n"
      "v_exp_f32 v25, v25\n"
      "v_exp_f32 v26, v26\n"
      "v_smfmac_f32_32x32x32_bf16 v[48:63], v[108:111], v[16:23], v138\n"
      "ds_read_b128 v[108:111], v135 offset:16640\n"
      "v_exp_f32 v27, v27\n"
      "v_exp_f32 v28, v28\n"
      "v_exp_f32 v29, v29\n"
      "v_exp_f32 v30, v30\n"
      "v_exp_f32 v31, v31\n"
      "v_exp_f32 v0, v0\n"
      "v_exp_f32 v1, v1\n"
      "v_smfmac_f32_32x32x32_bf16 v[48:63], v[112:115], v[24:31], v138\n"
      "ds_read_b128 v[112:115], v137 offset:16640\n"
      "v_exp_f32 v2, v2\n"
      "v_exp_f32 v3, v3\n"
      "v_exp_f32 v4, v4\n"
      "v_mfma_f32_32x32x16_bf16 v[16:31], v[208:211], v[100:103], v[80:95]\n"
      "s_waitcnt lgkmcnt(2)\n"
      "ds_read_b128 v[208:211], v133 offset:18720\n"
      "v_exp_f32 v5, v5\n"
      "v_exp_f32 v6, v6\n"
      "v_exp_f32 v7, v7\n"
      "v_exp_f32 v8, v8\n"
      "v_exp_f32 v9, v9\n"
      "v_exp_f32 v10, v10\n"
      "v_smfmac_f32_32x32x32_bf16 v[32:47], v[212:215], v[0:7], v138\n"
      "s_add_u32 m0, s80, 8192\n"
      "s_xor_b32 s66, s96, 512\n"
      "buffer_load_dwordx4 v142, s[28:31], s66 offen offset:0 lds\n"
      "v_exp_f32 v11, v11\n"
      "v_exp_f32 v12, v12\n"
      "v_exp_f32 v13, v13\n"
      "v_exp_f32 v14, v14\n"
      "v_exp_f32 v15, v15\n"
      "v_exp_f32 v16, v16\n"
      "v_exp_f32 v17, v17\n"
      "v_smfmac_f32_32x32x32_bf16 v[32:47], v[216:219], v[8:15], v138\n"
      "buffer_load_dwordx4 v148, s[28:31], s66 offen offset:1024 lds\n"
      "v_exp_f32 v18, v18\n"
      "v_exp_f32 v19, v19\n"
      "v_exp_f32 v20, v20\n"
      "v_mfma_f32_32x32x16_bf16 v[0:15], v[104:107], v[96:99], v[64:79]\n"
      "v_exp_f32 v21, v21\n"
      "v_exp_f32 v22, v22\n"
      "v_exp_f32 v23, v23\n"
      "v_exp_f32 v24, v24\n"
      "v_exp_f32 v25, v25\n"
      "v_exp_f32 v26, v26\n"
      "v_smfmac_f32_32x32x32_bf16 v[48:63], v[212:215], v[16:23], v138\n"
      "ds_read_b128 v[212:215], v135 offset:18720\n"
      "v_exp_f32 v27, v27\n"
      "v_exp_f32 v28, v28\n"
      "v_exp_f32 v29, v29\n"
      "v_exp_f32 v30, v30\n"
      "v_exp_f32 v31, v31\n"
      "v_exp_f32 v0, v0\n"
      "v_exp_f32 v1, v1\n"
      "v_smfmac_f32_32x32x32_bf16 v[48:63], v[216:219], v[24:31], v138\n"
      "ds_read_b128 v[216:219], v137 offset:18720\n"
      "v_exp_f32 v2, v2\n"
      "v_exp_f32 v3, v3\n"
      "v_exp_f32 v4, v4\n"
      "v_mfma_f32_32x32x16_bf16 v[16:31], v[104:107], v[100:103], v[80:95]\n"
      "s_waitcnt lgkmcnt(2)\n"
      "ds_read_b128 v[104:107], v133 offset:20800\n"
      "v_exp_f32 v5, v5\n"
      "v_exp_f32 v6, v6\n"
      "v_exp_f32 v7, v7\n"
      "v_exp_f32 v8, v8\n"
      "v_exp_f32 v9, v9\n"
      "v_exp_f32 v10, v10\n"
      "v_smfmac_f32_32x32x32_bf16 v[32:47], v[108:111], v[0:7], v138\n"
      "s_add_u32 m0, s80, 10240\n"
      "s_xor_b32 s66, s96, 640\n"
      "buffer_load_dwordx4 v142, s[28:31], s66 offen offset:0 lds\n"
      "v_exp_f32 v11, v11\n"
      "v_exp_f32 v12, v12\n"
      "v_exp_f32 v13, v13\n"
      "v_exp_f32 v14, v14\n"
      "v_exp_f32 v15, v15\n"
      "v_exp_f32 v16, v16\n"
      "v_exp_f32 v17, v17\n"
      "v_smfmac_f32_32x32x32_bf16 v[32:47], v[112:115], v[8:15], v138\n"
      "buffer_load_dwordx4 v148, s[28:31], s66 offen offset:1024 lds\n"
      "v_exp_f32 v18, v18\n"
      "v_exp_f32 v19, v19\n"
      "v_exp_f32 v20, v20\n"
      "v_mfma_f32_32x32x16_bf16 v[0:15], v[208:211], v[96:99], v[64:79]\n"
      "v_exp_f32 v21, v21\n"
      "v_exp_f32 v22, v22\n"
      "v_exp_f32 v23, v23\n"
      "v_exp_f32 v24, v24\n"
      "v_exp_f32 v25, v25\n"
      "v_exp_f32 v26, v26\n"
      "v_smfmac_f32_32x32x32_bf16 v[48:63], v[108:111], v[16:23], v138\n"
      "ds_read_b128 v[108:111], v135 offset:20800\n"
      "v_exp_f32 v27, v27\n"
      "v_exp_f32 v28, v28\n"
      "v_exp_f32 v29, v29\n"
      "v_exp_f32 v30, v30\n"
      "v_exp_f32 v31, v31\n"
      "v_exp_f32 v0, v0\n"
      "v_exp_f32 v1, v1\n"
      "v_smfmac_f32_32x32x32_bf16 v[48:63], v[112:115], v[24:31], v138\n"
      "ds_read_b128 v[112:115], v137 offset:20800\n"
      "v_exp_f32 v2, v2\n"
      "v_exp_f32 v3, v3\n"
      "v_exp_f32 v4, v4\n"
      "v_mfma_f32_32x32x16_bf16 v[16:31], v[208:211], v[100:103], v[80:95]\n"
      "s_waitcnt lgkmcnt(2)\n"
      "ds_read_b128 v[208:211], v133 offset:22880\n"
      "v_exp_f32 v5, v5\n"
      "v_exp_f32 v6, v6\n"
      "v_exp_f32 v7, v7\n"
      "v_exp_f32 v8, v8\n"
      "v_exp_f32 v9, v9\n"
      "v_exp_f32 v10, v10\n"
      "v_smfmac_f32_32x32x32_bf16 v[32:47], v[212:215], v[0:7], v138\n"
      "s_add_u32 m0, s80, 12288\n"
      "s_xor_b32 s66, s96, 768\n"
      "buffer_load_dwordx4 v142, s[28:31], s66 offen offset:0 lds\n"
      "v_exp_f32 v11, v11\n"
      "v_exp_f32 v12, v12\n"
      "v_exp_f32 v13, v13\n"
      "v_exp_f32 v14, v14\n"
      "v_exp_f32 v15, v15\n"
      "v_exp_f32 v16, v16\n"
      "v_exp_f32 v17, v17\n"
      "v_smfmac_f32_32x32x32_bf16 v[32:47], v[216:219], v[8:15], v138\n"
      "buffer_load_dwordx4 v148, s[28:31], s66 offen offset:1024 lds\n"
      "v_exp_f32 v18, v18\n"
      "v_exp_f32 v19, v19\n"
      "v_exp_f32 v20, v20\n"
      "v_mfma_f32_32x32x16_bf16 v[0:15], v[104:107], v[96:99], v[64:79]\n"
      "v_exp_f32 v21, v21\n"
      "v_exp_f32 v22, v22\n"
      "v_exp_f32 v23, v23\n"
      "v_exp_f32 v24, v24\n"
      "v_exp_f32 v25, v25\n"
      "v_exp_f32 v26, v26\n"
      "v_smfmac_f32_32x32x32_bf16 v[48:63], v[212:215], v[16:23], v138\n"
      "ds_read_b128 v[212:215], v135 offset:22880\n"
      "v_exp_f32 v27, v27\n"
      "v_exp_f32 v28, v28\n"
      "v_exp_f32 v29, v29\n"
      "v_exp_f32 v30, v30\n"
      "v_exp_f32 v31, v31\n"
      "v_exp_f32 v0, v0\n"
      "v_exp_f32 v1, v1\n"
      "v_smfmac_f32_32x32x32_bf16 v[48:63], v[216:219], v[24:31], v138\n"
      "ds_read_b128 v[216:219], v137 offset:22880\n"
      "v_exp_f32 v2, v2\n"
      "v_exp_f32 v3, v3\n"
      "v_exp_f32 v4, v4\n"
      "v_mfma_f32_32x32x16_bf16 v[16:31], v[104:107], v[100:103], v[80:95]\n"
      "s_waitcnt lgkmcnt(2)\n"
      "ds_read_b128 v[104:107], v133 offset:24960\n"
      "v_exp_f32 v5, v5\n"
      "v_exp_f32 v6, v6\n"
      "v_exp_f32 v7, v7\n"
      "v_exp_f32 v8, v8\n"
      "v_exp_f32 v9, v9\n"
      "v_exp_f32 v10, v10\n"
      "v_smfmac_f32_32x32x32_bf16 v[32:47], v[108:111], v[0:7], v138\n"
      "s_add_u32 m0, s80, 14336\n"
      "s_xor_b32 s66, s96, 896\n"
      "buffer_load_dwordx4 v142, s[28:31], s66 offen offset:0 lds\n"
      "v_exp_f32 v11, v11\n"
      "v_exp_f32 v12, v12\n"
      "v_exp_f32 v13, v13\n"
      "v_exp_f32 v14, v14\n"
      "v_exp_f32 v15, v15\n"
      "v_exp_f32 v16, v16\n"
      "v_exp_f32 v17, v17\n"
      "v_smfmac_f32_32x32x32_bf16 v[32:47], v[112:115], v[8:15], v138\n"
      "buffer_load_dwordx4 v148, s[28:31], s66 offen offset:1024 lds\n"
      "v_exp_f32 v18, v18\n"
      "v_exp_f32 v19, v19\n"
      "v_exp_f32 v20, v20\n"
      "v_mfma_f32_32x32x16_bf16 v[0:15], v[208:211], v[96:99], v[64:79]\n"
      "s_waitcnt vmcnt(14)\n"
      "ds_read_b128 a[0:3], v143 offset:0\n"
      "ds_read_b128 a[16:19], v144 offset:0\n"
      "v_exp_f32 v21, v21\n"
      "v_exp_f32 v22, v22\n"
      "v_exp_f32 v23, v23\n"
      "v_exp_f32 v24, v24\n"
      "v_exp_f32 v25, v25\n"
      "v_exp_f32 v26, v26\n"
      "v_smfmac_f32_32x32x32_bf16 v[48:63], v[108:111], v[16:23], v138\n"
      "ds_read_b128 v[108:111], v135 offset:24960\n"
      "v_exp_f32 v27, v27\n"
      "v_exp_f32 v28, v28\n"
      "v_exp_f32 v29, v29\n"
      "v_exp_f32 v30, v30\n"
      "v_exp_f32 v31, v31\n"
      "v_exp_f32 v0, v0\n"
      "v_exp_f32 v1, v1\n"
      "v_smfmac_f32_32x32x32_bf16 v[48:63], v[112:115], v[24:31], v138\n"
      "ds_read_b128 v[112:115], v137 offset:24960\n"
      "v_exp_f32 v2, v2\n"
      "v_exp_f32 v3, v3\n"
      "v_exp_f32 v4, v4\n"
      "v_mfma_f32_32x32x16_bf16 v[16:31], v[208:211], v[100:103], v[80:95]\n"
      "s_waitcnt lgkmcnt(2)\n"
      "ds_read_b128 v[208:211], v133 offset:27040\n"
      "v_exp_f32 v5, v5\n"
      "v_exp_f32 v6, v6\n"
      "v_exp_f32 v7, v7\n"
      "v_exp_f32 v8, v8\n"
      "v_exp_f32 v9, v9\n"
      "v_exp_f32 v10, v10\n"
      "v_smfmac_f32_32x32x32_bf16 v[32:47], v[212:215], v[0:7], v138\n"
      "Lhs1_0_%=:\n"
      "s_cbranch_execz Lhs1_4_%=\n"
      "s_add_u32 m0, s80, 0\n"
      "s_xor_b32 s66, s97, 0\n"
      "buffer_load_dwordx4 v142, s[28:31], s66 offen offset:0 lds\n"
      "v_exp_f32 v11, v11\n"
      "v_exp_f32 v12, v12\n"
      "v_exp_f32 v13, v13\n"
      "v_exp_f32 v14, v14\n"
      "v_exp_f32 v15, v15\n"
      "v_exp_f32 v16, v16\n"
      "v_exp_f32 v17, v17\n"
      "v_smfmac_f32_32x32x32_bf16 v[32:47], v[216:219], v[8:15], v138\n"
      "buffer_load_dwordx4 v148, s[28:31], s66 offen offset:1024 lds\n"
      "v_exp_f32 v18, v18\n"
      "v_exp_f32 v19, v19\n"
      "v_exp_f32 v20, v20\n"
      "v_mfma_f32_32x32x16_bf16 v[0:15], v[104:107], v[96:99], v[64:79]\n"
      "s_waitcnt vmcnt(14)\n"
      "ds_read_b128 a[32:35], v143 offset:2048\n"
      "ds_read_b128 a[48:51], v144 offset:2048\n"
      "v_exp_f32 v21, v21\n"
      "v_exp_f32 v22, v22\n"
      "v_exp_f32 v23, v23\n"
      "v_exp_f32 v24, v24\n"
      "v_exp_f32 v25, v25\n"
      "v_exp_f32 v26, v26\n"
      "v_smfmac_f32_32x32x32_bf16 v[48:63], v[212:215], v[16:23], v138\n"
      "ds_read_b128 v[212:215], v135 offset:27040\n"
      "v_exp_f32 v27, v27\n"
      "v_exp_f32 v28, v28\n"
      "v_exp_f32 v29, v29\n"
      "v_exp_f32 v30, v30\n"
      "v_exp_f32 v31, v31\n"
      "v_exp_f32 v0, v0\n"
      "v_exp_f32 v1, v1\n"
      "v_smfmac_f32_32x32x32_bf16 v[48:63], v[216:219], v[24:31], v138\n"
      "ds_read_b128 v[216:219], v137 offset:27040\n"
      "v_exp_f32 v2, v2\n"
      "v_exp_f32 v3, v3\n"
      "v_exp_f32 v4, v4\n"
      "v_mfma_f32_32x32x16_bf16 v[16:31], v[104:107], v[100:103], v[80:95]\n"
      "s_waitcnt lgkmcnt(2)\n"
      "ds_read_b128 v[104:107], v133 offset:29120\n"
      "v_exp_f32 v5, v5\n"
      "v_exp_f32 v6, v6\n"
      "v_exp_f32 v7, v7\n"
      "v_exp_f32 v8, v8\n"
      "v_exp_f32 v9, v9\n"
      "v_exp_f32 v10, v10\n"
      "v_smfmac_f32_32x32x32_bf16 v[32:47], v[108:111], v[0:7], v138\n"
      "s_add_u32 m0, s80, 2048\n"
      "s_xor_b32 s66, s97, 128\n"
      "buffer_load_dwordx4 v142, s[28:31], s66 offen offset:0 lds\n"
      "v_exp_f32 v11, v11\n"
      "v_exp_f32 v12, v12\n"
      "v_exp_f32 v13, v13\n"
      "v_exp_f32 v14, v14\n"
      "v_exp_f32 v15, v15\n"
      "v_exp_f32 v16, v16\n"
      "v_exp_f32 v17, v17\n"
      "v_smfmac_f32_32x32x32_bf16 v[32:47], v[112:115], v[8:15], v138\n"
      "buffer_load_dwordx4 v148, s[28:31], s66 offen offset:1024 lds\n"
      "v_exp_f32 v18, v18\n"
      "v_exp_f32 v19, v19\n"
      "v_exp_f32 v20, v20\n"
      "v_mfma_f32_32x32x16_bf16 v[0:15], v[208:211], v[96:99], v[64:79]\n"
      "s_waitcnt vmcnt(14)\n"
      "ds_read_b128 a[64:67], v143 offset:4096\n"
      "ds_read_b128 a[80:83], v144 offset:4096\n"
      "v_exp_f32 v21, v21\n"
      "v_exp_f32 v22, v22\n"
      "v_exp_f32 v23, v23\n"
      "v_exp_f32 v24, v24\n"
      "v_exp_f32 v25, v25\n"
      "v_exp_f32 v26, v26\n"
      "v_smfmac_f32_32x32x32_bf16 v[48:63], v[108:111], v[16:23], v138\n"
      "ds_read_b128 v[108:111], v135 offset:29120\n"
      "v_exp_f32 v27, v27\n"
      "v_exp_f32 v28, v28\n"
      "v_exp_f32 v29, v29\n"
      "v_exp_f32 v30, v30\n"
      "v_exp_f32 v31, v31\n"
      "v_exp_f32 v0, v0\n"
      "v_exp_f32 v1, v1\n"
      "v_smfmac_f32_32x32x32_bf16 v[48:63], v[112:115], v[24:31], v138\n"
      "ds_read_b128 v[112:115], v137 offset:29120\n"
      "v_exp_f32 v2, v2\n"
      "v_exp_f32 v3, v3\n"
      "v_exp_f32 v4, v4\n"
      "v_mfma_f32_32x32x16_bf16 v[16:31], v[208:211], v[100:103], v[80:95]\n"
      "s_waitcnt lgkmcnt(2)\n"
      "ds_read_b128 v[208:211], v133 offset:31200\n"
      "v_exp_f32 v5, v5\n"
      "v_exp_f32 v6, v6\n"
      "v_exp_f32 v7, v7\n"
      "v_exp_f32 v8, v8\n"
      "v_exp_f32 v9, v9\n"
      "v_exp_f32 v10, v10\n"
      "v_smfmac_f32_32x32x32_bf16 v[32:47], v[212:215], v[0:7], v138\n"
      "s_add_u32 m0, s80, 4096\n"
      "s_xor_b32 s66, s97, 256\n"
      "buffer_load_dwordx4 v142, s[28:31], s66 offen offset:0 lds\n"
      "v_exp_f32 v11, v11\n"
      "v_exp_f32 v12, v12\n"
      "v_exp_f32 v13, v13\n"
      "v_exp_f32 v14, v14\n"
      "v_exp_f32 v15, v15\n"
      "v_exp_f32 v16, v16\n"
      "v_exp_f32 v17, v17\n"
      "v_smfmac_f32_32x32x32_bf16 v[32:47], v[216:219], v[8:15], v138\n"
      "buffer_load_dwordx4 v148, s[28:31], s66 offen offset:1024 lds\n"
      "v_exp_f32 v18, v18\n"
      "v_exp_f32 v19, v19\n"
      "v_exp_f32 v20, v20\n"
      "v_mfma_f32_32x32x16_bf16 v[0:15], v[104:107], v[96:99], v[64:79]\n"
      "s_waitcnt vmcnt(14)\n"
      "ds_read_b128 a[96:99], v143 offset:6144\n"
      "ds_read_b128 a[112:115], v144 offset:6144\n"
      "v_exp_f32 v21, v21\n"
      "v_exp_f32 v22, v22\n"
      "v_exp_f32 v23, v23\n"
      "v_exp_f32 v24, v24\n"
      "v_exp_f32 v25, v25\n"
      "v_exp_f32 v26, v26\n"
      "v_smfmac_f32_32x32x32_bf16 v[48:63], v[212:215], v[16:23], v138\n"
      "ds_read_b128 v[212:215], v135 offset:31200\n"
      "v_exp_f32 v27, v27\n"
      "v_exp_f32 v28, v28\n"
      "v_exp_f32 v29, v29\n"
      "v_exp_f32 v30, v30\n"
      "v_exp_f32 v31, v31\n"
      "v_exp_f32 v0, v0\n"
      "v_exp_f32 v1, v1\n"
      "v_smfmac_f32_32x32x32_bf16 v[48:63], v[216:219], v[24:31], v138\n"
      "ds_read_b128 v[216:219], v137 offset:31200\n"
      "v_exp_f32 v2, v2\n"
      "v_exp_f32 v3, v3\n"
      "v_exp_f32 v4, v4\n"
      "v_mfma_f32_32x32x16_bf16 v[16:31], v[104:107], v[100:103], v[80:95]\n"
      "s_waitcnt lgkmcnt(2)\n"
      "ds_read_b128 v[104:107], v133 offset:33280\n"
      "v_exp_f32 v5, v5\n"
      "v_exp_f32 v6, v6\n"
      "v_exp_f32 v7, v7\n"
      "v_exp_f32 v8, v8\n"
      "v_exp_f32 v9, v9\n"
      "v_exp_f32 v10, v10\n"
      "v_smfmac_f32_32x32x32_bf16 v[32:47], v[108:111], v[0:7], v138\n"
      "s_add_u32 m0, s80, 6144\n"
      "s_xor_b32 s66, s97, 384\n"
      "buffer_load_dwordx4 v142, s[28:31], s66 offen offset:0 lds\n"
      "v_exp_f32 v11, v11\n"
      "v_exp_f32 v12, v12\n"
      "v_exp_f32 v13, v13\n"
      "v_exp_f32 v14, v14\n"
      "v_exp_f32 v15, v15\n"
      "v_exp_f32 v16, v16\n"
      "v_exp_f32 v17, v17\n"
      "v_smfmac_f32_32x32x32_bf16 v[32:47], v[112:115], v[8:15], v138\n"
      "buffer_load_dwordx4 v148, s[28:31], s66 offen offset:1024 lds\n"
      "v_exp_f32 v18, v18\n"
      "v_exp_f32 v19, v19\n"
      "v_exp_f32 v20, v20\n"
      "v_mfma_f32_32x32x16_bf16 v[0:15], v[208:211], v[96:99], v[64:79]\n"
      "s_waitcnt vmcnt(14)\n"
      "ds_read_b128 a[128:131], v143 offset:8192\n"
      "ds_read_b128 a[144:147], v144 offset:8192\n"
      "v_exp_f32 v21, v21\n"
      "v_exp_f32 v22, v22\n"
      "v_exp_f32 v23, v23\n"
      "v_exp_f32 v24, v24\n"
      "v_exp_f32 v25, v25\n"
      "v_exp_f32 v26, v26\n"
      "v_smfmac_f32_32x32x32_bf16 v[48:63], v[108:111], v[16:23], v138\n"
      "ds_read_b128 v[108:111], v135 offset:33280\n"
      "v_exp_f32 v27, v27\n"
      "v_exp_f32 v28, v28\n"
      "v_exp_f32 v29, v29\n"
      "v_exp_f32 v30, v30\n"
      "v_exp_f32 v31, v31\n"
      "v_exp_f32 v0, v0\n"
      "v_exp_f32 v1, v1\n"
      "v_smfmac_f32_32x32x32_bf16 v[48:63], v[112:115], v[24:31], v138\n"
      "ds_read_b128 v[112:115], v137 offset:33280\n"
      "v_exp_f32 v2, v2\n"
      "v_exp_f32 v3, v3\n"
      "v_exp_f32 v4, v4\n"
      "v_mfma_f32_32x32x16_bf16 v[16:31], v[208:211], v[100:103], v[80:95]\n"
      "s_waitcnt lgkmcnt(2)\n"
      "ds_read_b128 v[208:211], v133 offset:35360\n"
      "v_exp_f32 v5, v5\n"
      "v_exp_f32 v6, v6\n"
      "v_exp_f32 v7, v7\n"
      "v_exp_f32 v8, v8\n"
      "v_exp_f32 v9, v9\n"
      "v_exp_f32 v10, v10\n"
      "v_smfmac_f32_32x32x32_bf16 v[32:47], v[212:215], v[0:7], v138\n"
      "s_add_u32 m0, s80, 8192\n"
      "s_xor_b32 s66, s97, 512\n"
      "buffer_load_dwordx4 v142, s[28:31], s66 offen offset:0 lds\n"
      "v_exp_f32 v11, v11\n"
      "v_exp_f32 v12, v12\n"
      "v_exp_f32 v13, v13\n"
      "v_exp_f32 v14, v14\n"
      "v_exp_f32 v15, v15\n"
      "v_exp_f32 v16, v16\n"
      "v_exp_f32 v17, v17\n"
      "v_smfmac_f32_32x32x32_bf16 v[32:47], v[216:219], v[8:15], v138\n"
      "buffer_load_dwordx4 v148, s[28:31], s66 offen offset:1024 lds\n"
      "v_exp_f32 v18, v18\n"
      "v_exp_f32 v19, v19\n"
      "v_exp_f32 v20, v20\n"
      "v_mfma_f32_32x32x16_bf16 v[0:15], v[104:107], v[96:99], v[64:79]\n"
      "s_barrier\n"
      "s_waitcnt vmcnt(14)\n"
      "ds_read_b128 a[160:163], v143 offset:10240\n"
      "ds_read_b128 a[176:179], v144 offset:10240\n"
      "v_exp_f32 v21, v21\n"
      "v_exp_f32 v22, v22\n"
      "v_exp_f32 v23, v23\n"
      "v_exp_f32 v24, v24\n"
      "v_exp_f32 v25, v25\n"
      "v_exp_f32 v26, v26\n"
      "v_smfmac_f32_32x32x32_bf16 v[48:63], v[212:215], v[16:23], v138\n"
      "s_add_u32 m0, s70, 65536\n"
      "s_xor_b32 s67, s69, 65536\n"
      "ds_read_b128 v[212:215], v135 offset:35360\n"
      "buffer_load_dwordx4 v254, s[8:11], s67 offen sc1 lds\n"
      "v_exp_f32 v27, v27\n"
      "v_exp_f32 v28, v28\n"
      "v_exp_f32 v29, v29\n"
      "v_exp_f32 v30, v30\n"
      "v_exp_f32 v31, v31\n"
      "v_exp_f32 v0, v0\n"
      "v_exp_f32 v1, v1\n"
      "v_smfmac_f32_32x32x32_bf16 v[48:63], v[216:219], v[24:31], v138\n"
      "ds_read_b128 v[216:219], v137 offset:35360\n"
      "buffer_load_dwordx4 v254, s[8:11], s67 offen offset:1024 sc1 lds\n"
      "v_exp_f32 v2, v2\n"
      "v_exp_f32 v3, v3\n"
      "v_exp_f32 v4, v4\n"
      "v_mfma_f32_32x32x16_bf16 v[16:31], v[104:107], v[100:103], v[80:95]\n"
      "s_waitcnt lgkmcnt(2)\n"
      "ds_read_b128 v[104:107], v133 offset:37440\n"
      "v_exp_f32 v5, v5\n"
      "v_exp_f32 v6, v6\n"
      "v_exp_f32 v7, v7\n"
      "v_exp_f32 v8, v8\n"
      "v_exp_f32 v9, v9\n"
      "v_exp_f32 v10, v10\n"
      "v_smfmac_f32_32x32x32_bf16 v[32:47], v[108:111], v[0:7], v138\n"
      "s_add_u32 m0, s80, 10240\n"
      "s_xor_b32 s66, s97, 640\n"
      "buffer_load_dwordx4 v142, s[28:31], s66 offen offset:0 lds\n"
      "v_exp_f32 v11, v11\n"
      "v_exp_f32 v12, v12\n"
      "v_exp_f32 v13, v13\n"
      "v_exp_f32 v14, v14\n"
      "v_exp_f32 v15, v15\n"
      "v_exp_f32 v16, v16\n"
      "v_exp_f32 v17, v17\n"
      "v_smfmac_f32_32x32x32_bf16 v[32:47], v[112:115], v[8:15], v138\n"
      "buffer_load_dwordx4 v148, s[28:31], s66 offen offset:1024 lds\n"
      "v_exp_f32 v18, v18\n"
      "v_exp_f32 v19, v19\n"
      "v_exp_f32 v20, v20\n"
      "v_mfma_f32_32x32x16_bf16 v[0:15], v[208:211], v[96:99], v[64:79]\n"
      "s_waitcnt vmcnt(16)\n"
      "ds_read_b128 a[192:195], v143 offset:12288\n"
      "ds_read_b128 a[208:211], v144 offset:12288\n"
      "v_exp_f32 v21, v21\n"
      "v_exp_f32 v22, v22\n"
      "v_exp_f32 v23, v23\n"
      "v_exp_f32 v24, v24\n"
      "v_exp_f32 v25, v25\n"
      "v_exp_f32 v26, v26\n"
      "v_smfmac_f32_32x32x32_bf16 v[48:63], v[108:111], v[16:23], v138\n"
      "ds_read_b128 v[108:111], v135 offset:37440\n"
      "v_exp_f32 v27, v27\n"
      "v_exp_f32 v28, v28\n"
      "v_exp_f32 v29, v29\n"
      "v_exp_f32 v30, v30\n"
      "v_exp_f32 v31, v31\n"
      "v_exp_f32 v0, v0\n"
      "v_exp_f32 v1, v1\n"
      "v_smfmac_f32_32x32x32_bf16 v[48:63], v[112:115], v[24:31], v138\n"
      "ds_read_b128 v[112:115], v137 offset:37440\n"
      "v_exp_f32 v2, v2\n"
      "v_exp_f32 v3, v3\n"
      "v_exp_f32 v4, v4\n"
      "v_mfma_f32_32x32x16_bf16 v[16:31], v[208:211], v[100:103], v[80:95]\n"
      "s_waitcnt lgkmcnt(2)\n"
      "ds_read_b128 v[208:211], v133 offset:39520\n"
      "v_exp_f32 v5, v5\n"
      "v_exp_f32 v6, v6\n"
      "v_exp_f32 v7, v7\n"
      "v_exp_f32 v8, v8\n"
      "v_exp_f32 v9, v9\n"
      "v_exp_f32 v10, v10\n"
      "v_smfmac_f32_32x32x32_bf16 v[32:47], v[212:215], v[0:7], v138\n"
      "s_add_u32 m0, s80, 12288\n"
      "s_xor_b32 s66, s97, 768\n"
      "buffer_load_dwordx4 v142, s[28:31], s66 offen offset:0 lds\n"
      "v_exp_f32 v11, v11\n"
      "v_exp_f32 v12, v12\n"
      "v_exp_f32 v13, v13\n"
      "v_exp_f32 v14, v14\n"
      "v_exp_f32 v15, v15\n"
      "v_exp_f32 v16, v16\n"
      "v_exp_f32 v17, v17\n"
      "v_smfmac_f32_32x32x32_bf16 v[32:47], v[216:219], v[8:15], v138\n"
      "buffer_load_dwordx4 v148, s[28:31], s66 offen offset:1024 lds\n"
      "v_exp_f32 v18, v18\n"
      "v_exp_f32 v19, v19\n"
      "v_exp_f32 v20, v20\n"
      "v_mfma_f32_32x32x16_bf16 v[0:15], v[104:107], v[96:99], v[64:79]\n"
      "s_waitcnt vmcnt(16)\n"
      "ds_read_b128 a[224:227], v143 offset:14336\n"
      "ds_read_b128 a[240:243], v144 offset:14336\n"
      "v_exp_f32 v21, v21\n"
      "v_exp_f32 v22, v22\n"
      "v_exp_f32 v23, v23\n"
      "v_exp_f32 v24, v24\n"
      "v_exp_f32 v25, v25\n"
      "v_exp_f32 v26, v26\n"
      "v_smfmac_f32_32x32x32_bf16 v[48:63], v[212:215], v[16:23], v138\n"
      "s_add_u32 m0, s70, 73856\n"
      "s_xor_b32 s67, s69, 67584\n"
      "ds_read_b128 v[212:215], v135 offset:39520\n"
      "buffer_load_dwordx4 v254, s[8:11], s67 offen sc1 lds\n"
      "v_exp_f32 v27, v27\n"
      "v_exp_f32 v28, v28\n"
      "v_exp_f32 v29, v29\n"
      "v_exp_f32 v30, v30\n"
      "v_exp_f32 v31, v31\n"
      "v_exp_f32 v0, v0\n"
      "v_exp_f32 v1, v1\n"
      "v_smfmac_f32_32x32x32_bf16 v[48:63], v[216:219], v[24:31], v138\n"
      "ds_read_b128 v[216:219], v137 offset:39520\n"
      "buffer_load_dwordx4 v254, s[8:11], s67 offen offset:1024 sc1 lds\n"
      "v_exp_f32 v2, v2\n"
      "v_exp_f32 v3, v3\n"
      "v_exp_f32 v4, v4\n"
      "v_mfma_f32_32x32x16_bf16 v[16:31], v[104:107], v[100:103], v[80:95]\n"
      "s_waitcnt lgkmcnt(2)\n"
      "ds_read_b128 v[104:107], v133 offset:41600\n"
      "v_exp_f32 v5, v5\n"
      "v_exp_f32 v6, v6\n"
      "v_exp_f32 v7, v7\n"
      "v_exp_f32 v8, v8\n"
      "v_exp_f32 v9, v9\n"
      "v_exp_f32 v10, v10\n"
      "v_smfmac_f32_32x32x32_bf16 v[32:47], v[108:111], v[0:7], v138\n"
      "s_add_u32 m0, s80, 14336\n"
      "s_xor_b32 s66, s97, 896\n"
      "buffer_load_dwordx4 v142, s[28:31], s66 offen offset:0 lds\n"
      "v_exp_f32 v11, v11\n"
      "v_exp_f32 v12, v12\n"
      "v_exp_f32 v13, v13\n"
      "v_exp_f32 v14, v14\n"
      "v_exp_f32 v15, v15\n"
      "v_exp_f32 v16, v16\n"
      "v_exp_f32 v17, v17\n"
      "v_smfmac_f32_32x32x32_bf16 v[32:47], v[112:115], v[8:15], v138\n"
      "buffer_load_dwordx4 v148, s[28:31], s66 offen offset:1024 lds\n"
      "v_exp_f32 v18, v18\n"
      "v_exp_f32 v19, v19\n"
      "v_exp_f32 v20, v20\n"
      "v_mfma_f32_32x32x16_bf16 v[0:15], v[208:211], v[96:99], v[64:79]\n"
      "s_waitcnt vmcnt(18)\n"
      "ds_read_b128 a[4:7], v143 offset:0\n"
      "ds_read_b128 a[20:23], v144 offset:0\n"
      "v_exp_f32 v21, v21\n"
      "v_exp_f32 v22, v22\n"
      "v_exp_f32 v23, v23\n"
      "v_exp_f32 v24, v24\n"
      "v_exp_f32 v25, v25\n"
      "v_exp_f32 v26, v26\n"
      "v_smfmac_f32_32x32x32_bf16 v[48:63], v[108:111], v[16:23], v138\n"
      "ds_read_b128 v[108:111], v135 offset:41600\n"
      "v_exp_f32 v27, v27\n"
      "v_exp_f32 v28, v28\n"
      "v_exp_f32 v29, v29\n"
      "v_exp_f32 v30, v30\n"
      "v_exp_f32 v31, v31\n"
      "v_exp_f32 v0, v0\n"
      "v_exp_f32 v1, v1\n"
      "v_smfmac_f32_32x32x32_bf16 v[48:63], v[112:115], v[24:31], v138\n"
      "ds_read_b128 v[112:115], v137 offset:41600\n"
      "v_exp_f32 v2, v2\n"
      "v_exp_f32 v3, v3\n"
      "v_exp_f32 v4, v4\n"
      "v_mfma_f32_32x32x16_bf16 v[16:31], v[208:211], v[100:103], v[80:95]\n"
      "s_waitcnt lgkmcnt(2)\n"
      "ds_read_b128 v[208:211], v133 offset:43680\n"
      "v_exp_f32 v5, v5\n"
      "v_exp_f32 v6, v6\n"
      "v_exp_f32 v7, v7\n"
      "v_exp_f32 v8, v8\n"
      "v_exp_f32 v9, v9\n"
      "v_exp_f32 v10, v10\n"
      "v_smfmac_f32_32x32x32_bf16 v[32:47], v[212:215], v[0:7], v138\n"
      "s_add_u32 m0, s80, 0\n"
      "s_xor_b32 s66, s98, 0\n"
      "buffer_load_dwordx4 v142, s[28:31], s66 offen offset:0 lds\n"
      "v_exp_f32 v11, v11\n"
      "v_exp_f32 v12, v12\n"
      "v_exp_f32 v13, v13\n"
      "v_exp_f32 v14, v14\n"
      "v_exp_f32 v15, v15\n"
      "v_exp_f32 v16, v16\n"
      "v_exp_f32 v17, v17\n"
      "v_smfmac_f32_32x32x32_bf16 v[32:47], v[216:219], v[8:15], v138\n"
      "buffer_load_dwordx4 v148, s[28:31], s66 offen offset:1024 lds\n"
      "v_exp_f32 v18, v18\n"
      "v_exp_f32 v19, v19\n"
      "v_exp_f32 v20, v20\n"
      "v_mfma_f32_32x32x16_bf16 v[0:15], v[104:107], v[96:99], v[64:79]\n"
      "s_waitcnt vmcnt(18)\n"
      "ds_read_b128 a[36:39], v143 offset:2048\n"
      "ds_read_b128 a[52:55], v144 offset:2048\n"
      "v_exp_f32 v21, v21\n"
      "v_exp_f32 v22, v22\n"
      "v_exp_f32 v23, v23\n"
      "v_exp_f32 v24, v24\n"
      "v_exp_f32 v25, v25\n"
      "v_exp_f32 v26, v26\n"
      "v_smfmac_f32_32x32x32_bf16 v[48:63], v[212:215], v[16:23], v138\n"
      "s_add_u32 m0, s70, 82176\n"
      "s_xor_b32 s67, s69, 81920\n"
      "ds_read_b128 v[212:215], v135 offset:43680\n"
      "buffer_load_dwordx4 v254, s[8:11], s67 offen sc1 lds\n"
      "v_exp_f32 v27, v27\n"
      "v_exp_f32 v28, v28\n"
      "v_exp_f32 v29, v29\n"
      "v_exp_f32 v30, v30\n"
      "v_exp_f32 v31, v31\n"
      "v_exp_f32 v0, v0\n"
      "v_exp_f32 v1, v1\n"
      "v_smfmac_f32_32x32x32_bf16 v[48:63], v[216:219], v[24:31], v138\n"
      "ds_read_b128 v[216:219], v137 offset:43680\n"
      "buffer_load_dwordx4 v254, s[8:11], s67 offen offset:1024 sc1 lds\n"
      "v_exp_f32 v2, v2\n"
      "v_exp_f32 v3, v3\n"
      "v_exp_f32 v4, v4\n"
      "v_mfma_f32_32x32x16_bf16 v[16:31], v[104:107], v[100:103], v[80:95]\n"
      "s_waitcnt lgkmcnt(2)\n"
      "ds_read_b128 v[104:107], v133 offset:45760\n"
      "v_exp_f32 v5, v5\n"
      "v_exp_f32 v6, v6\n"
      "v_exp_f32 v7, v7\n"
      "v_exp_f32 v8, v8\n"
      "v_exp_f32 v9, v9\n"
      "v_exp_f32 v10, v10\n"
      "v_smfmac_f32_32x32x32_bf16 v[32:47], v[108:111], v[0:7], v138\n"
      "s_add_u32 m0, s80, 2048\n"
      "s_xor_b32 s66, s98, 128\n"
      "buffer_load_dwordx4 v142, s[28:31], s66 offen offset:0 lds\n"
      "v_exp_f32 v11, v11\n"
      "v_exp_f32 v12, v12\n"
      "v_exp_f32 v13, v13\n"
      "v_exp_f32 v14, v14\n"
      "v_exp_f32 v15, v15\n"
      "v_exp_f32 v16, v16\n"
      "v_exp_f32 v17, v17\n"
      "v_smfmac_f32_32x32x32_bf16 v[32:47], v[112:115], v[8:15], v138\n"
      "buffer_load_dwordx4 v148, s[28:31], s66 offen offset:1024 lds\n"
      "v_exp_f32 v18, v18\n"
      "v_exp_f32 v19, v19\n"
      "v_exp_f32 v20, v20\n"
      "v_mfma_f32_32x32x16_bf16 v[0:15], v[208:211], v[96:99], v[64:79]\n"
      "s_waitcnt vmcnt(20)\n"
      "ds_read_b128 a[68:71], v143 offset:4096\n"
      "ds_read_b128 a[84:87], v144 offset:4096\n"
      "v_exp_f32 v21, v21\n"
      "v_exp_f32 v22, v22\n"
      "v_exp_f32 v23, v23\n"
      "v_exp_f32 v24, v24\n"
      "v_exp_f32 v25, v25\n"
      "v_exp_f32 v26, v26\n"
      "v_smfmac_f32_32x32x32_bf16 v[48:63], v[108:111], v[16:23], v138\n"
      "ds_read_b128 v[108:111], v135 offset:45760\n"
      "v_exp_f32 v27, v27\n"
      "v_exp_f32 v28, v28\n"
      "v_exp_f32 v29, v29\n"
      "v_exp_f32 v30, v30\n"
      "v_exp_f32 v31, v31\n"
      "v_exp_f32 v0, v0\n"
      "v_exp_f32 v1, v1\n"
      "v_smfmac_f32_32x32x32_bf16 v[48:63], v[112:115], v[24:31], v138\n"
      "ds_read_b128 v[112:115], v137 offset:45760\n"
      "v_exp_f32 v2, v2\n"
      "v_exp_f32 v3, v3\n"
      "v_exp_f32 v4, v4\n"
      "v_mfma_f32_32x32x16_bf16 v[16:31], v[208:211], v[100:103], v[80:95]\n"
      "s_waitcnt lgkmcnt(2)\n"
      "ds_read_b128 v[208:211], v133 offset:47840\n"
      "v_exp_f32 v5, v5\n"
      "v_exp_f32 v6, v6\n"
      "v_exp_f32 v7, v7\n"
      "v_exp_f32 v8, v8\n"
      "v_exp_f32 v9, v9\n"
      "v_exp_f32 v10, v10\n"
      "v_smfmac_f32_32x32x32_bf16 v[32:47], v[212:215], v[0:7], v138\n"
      "s_add_u32 m0, s80, 4096\n"
      "s_xor_b32 s66, s98, 256\n"
      "buffer_load_dwordx4 v142, s[28:31], s66 offen offset:0 lds\n"
      "v_exp_f32 v11, v11\n"
      "v_exp_f32 v12, v12\n"
      "v_exp_f32 v13, v13\n"
      "v_exp_f32 v14, v14\n"
      "v_exp_f32 v15, v15\n"
      "v_exp_f32 v16, v16\n"
      "v_exp_f32 v17, v17\n"
      "v_smfmac_f32_32x32x32_bf16 v[32:47], v[216:219], v[8:15], v138\n"
      "buffer_load_dwordx4 v148, s[28:31], s66 offen offset:1024 lds\n"
      "v_exp_f32 v18, v18\n"
      "v_exp_f32 v19, v19\n"
      "v_exp_f32 v20, v20\n"
      "v_mfma_f32_32x32x16_bf16 v[0:15], v[104:107], v[96:99], v[64:79]\n"
      "s_waitcnt vmcnt(20)\n"
      "ds_read_b128 a[100:103], v143 offset:6144\n"
      "ds_read_b128 a[116:119], v144 offset:6144\n"
      "v_exp_f32 v21, v21\n"
      "v_exp_f32 v22, v22\n"
      "v_exp_f32 v23, v23\n"
      "v_exp_f32 v24, v24\n"
      "v_exp_f32 v25, v25\n"
      "v_exp_f32 v26, v26\n"
      "v_smfmac_f32_32x32x32_bf16 v[48:63], v[212:215], v[16:23], v138\n"
      "s_add_u32 m0, s70, 90496\n"
      "s_xor_b32 s67, s69, 83968\n"
      "ds_read_b128 v[212:215], v135 offset:47840\n"
      "buffer_load_dwordx4 v254, s[8:11], s67 offen sc1 lds\n"
      "v_exp_f32 v27, v27\n"
      "v_exp_f32 v28, v28\n"
      "v_exp_f32 v29, v29\n"
      "v_exp_f32 v30, v30\n"
      "v_exp_f32 v31, v31\n"
      "v_exp_f32 v0, v0\n"
      "v_exp_f32 v1, v1\n"
      "v_smfmac_f32_32x32x32_bf16 v[48:63], v[216:219], v[24:31], v138\n"
      "ds_read_b128 v[216:219], v137 offset:47840\n"
      "buffer_load_dwordx4 v254, s[8:11], s67 offen offset:1024 sc1 lds\n"
      "v_exp_f32 v2, v2\n"
      "v_exp_f32 v3, v3\n"
      "v_exp_f32 v4, v4\n"
      "v_mfma_f32_32x32x16_bf16 v[16:31], v[104:107], v[100:103], v[80:95]\n"
      "s_waitcnt lgkmcnt(2)\n"
      "ds_read_b128 v[104:107], v133 offset:49920\n"
      "v_exp_f32 v5, v5\n"
      "v_exp_f32 v6, v6\n"
      "v_exp_f32 v7, v7\n"
      "v_exp_f32 v8, v8\n"
      "v_exp_f32 v9, v9\n"
      "v_exp_f32 v10, v10\n"
      "v_smfmac_f32_32x32x32_bf16 v[32:47], v[108:111], v[0:7], v138\n"
      "s_add_u32 m0, s80, 6144\n"
      "s_xor_b32 s66, s98, 384\n"
      "buffer_load_dwordx4 v142, s[28:31], s66 offen offset:0 lds\n"
      "v_exp_f32 v11, v11\n"
      "v_exp_f32 v12, v12\n"
      "v_exp_f32 v13, v13\n"
      "v_exp_f32 v14, v14\n"
      "v_exp_f32 v15, v15\n"
      "v_exp_f32 v16, v16\n"
      "v_exp_f32 v17, v17\n"
      "v_smfmac_f32_32x32x32_bf16 v[32:47], v[112:115], v[8:15], v138\n"
      "buffer_load_dwordx4 v148, s[28:31], s66 offen offset:1024 lds\n"
      "v_exp_f32 v18, v18\n"
      "v_exp_f32 v19, v19\n"
      "v_exp_f32 v20, v20\n"
      "v_mfma_f32_32x32x16_bf16 v[0:15], v[208:211], v[96:99], v[64:79]\n"
      "s_waitcnt vmcnt(22)\n"
      "ds_read_b128 a[132:135], v143 offset:8192\n"
      "ds_read_b128 a[148:151], v144 offset:8192\n"
      "v_exp_f32 v21, v21\n"
      "v_exp_f32 v22, v22\n"
      "v_exp_f32 v23, v23\n"
      "v_exp_f32 v24, v24\n"
      "v_exp_f32 v25, v25\n"
      "v_exp_f32 v26, v26\n"
      "v_smfmac_f32_32x32x32_bf16 v[48:63], v[108:111], v[16:23], v138\n"
      "ds_read_b128 v[108:111], v135 offset:49920\n"
      "v_exp_f32 v27, v27\n"
      "v_exp_f32 v28, v28\n"
      "v_exp_f32 v29, v29\n"
      "v_exp_f32 v30, v30\n"
      "v_exp_f32 v31, v31\n"
      "v_exp_f32 v0, v0\n"
      "v_exp_f32 v1, v1\n"
      "v_smfmac_f32_32x32x32_bf16 v[48:63], v[112:115], v[24:31], v138\n"
      "ds_read_b128 v[112:115], v137 offset:49920\n"
      "v_exp_f32 v2, v2\n"
      "v_exp_f32 v3, v3\n"
      "v_exp_f32 v4, v4\n"
      "v_mfma_f32_32x32x16_bf16 v[16:31], v[208:211], v[100:103], v[80:95]\n"
      "s_waitcnt lgkmcnt(2)\n"
      "ds_read_b128 v[208:211], v133 offset:52000\n"
      "v_exp_f32 v5, v5\n"
      "v_exp_f32 v6, v6\n"
      "v_exp_f32 v7, v7\n"
      "v_exp_f32 v8, v8\n"
      "v_exp_f32 v9, v9\n"
      "v_exp_f32 v10, v10\n"
      "v_smfmac_f32_32x32x32_bf16 v[32:47], v[212:215], v[0:7], v138\n"
      "s_add_u32 m0, s80, 8192\n"
      "s_xor_b32 s66, s98, 512\n"
      "buffer_load_dwordx4 v142, s[28:31], s66 offen offset:0 lds\n"
      "v_exp_f32 v11, v11\n"
      "v_exp_f32 v12, v12\n"
      "v_exp_f32 v13, v13\n"
      "v_exp_f32 v14, v14\n"
      "v_exp_f32 v15, v15\n"
      "v_exp_f32 v16, v16\n"
      "v_exp_f32 v17, v17\n"
      "v_smfmac_f32_32x32x32_bf16 v[32:47], v[216:219], v[8:15], v138\n"
      "buffer_load_dwordx4 v148, s[28:31], s66 offen offset:1024 lds\n"
      "v_exp_f32 v18, v18\n"
      "v_exp_f32 v19, v19\n"
      "v_exp_f32 v20, v20\n"
      "v_mfma_f32_32x32x16_bf16 v[0:15], v[104:107], v[96:99], v[64:79]\n"
      "s_waitcnt vmcnt(20)\n"
      "ds_read_b128 a[164:167], v143 offset:10240\n"
      "ds_read_b128 a[180:183], v144 offset:10240\n"
      "v_exp_f32 v21, v21\n"
      "v_exp_f32 v22, v22\n"
      "v_exp_f32 v23, v23\n"
      "v_exp_f32 v24, v24\n"
      "v_exp_f32 v25, v25\n"
      "v_exp_f32 v26, v26\n"
      "v_smfmac_f32_32x32x32_bf16 v[48:63], v[212:215], v[16:23], v138\n"
      "ds_read_b128 v[212:215], v135 offset:52000\n"
      "v_exp_f32 v27, v27\n"
      "v_exp_f32 v28, v28\n"
      "v_exp_f32 v29, v29\n"
      "v_exp_f32 v30, v30\n"
      "v_exp_f32 v31, v31\n"
      "v_exp_f32 v0, v0\n"
      "v_exp_f32 v1, v1\n"
      "v_smfmac_f32_32x32x32_bf16 v[48:63], v[216:219], v[24:31], v138\n"
      "ds_read_b128 v[216:219], v137 offset:52000\n"
      "v_exp_f32 v2, v2\n"
      "v_exp_f32 v3, v3\n"
      "v_exp_f32 v4, v4\n"
      "v_mfma_f32_32x32x16_bf16 v[16:31], v[104:107], v[100:103], v[80:95]\n"
      "s_waitcnt lgkmcnt(2)\n"
      "ds_read_b128 v[104:107], v133 offset:54080\n"
      "v_exp_f32 v5, v5\n"
      "v_exp_f32 v6, v6\n"
      "v_exp_f32 v7, v7\n"
      "v_exp_f32 v8, v8\n"
      "v_exp_f32 v9, v9\n"
      "v_exp_f32 v10, v10\n"
      "v_smfmac_f32_32x32x32_bf16 v[32:47], v[108:111], v[0:7], v138\n"
      "s_add_u32 m0, s80, 10240\n"
      "s_xor_b32 s66, s98, 640\n"
      "buffer_load_dwordx4 v142, s[28:31], s66 offen offset:0 lds\n"
      "v_exp_f32 v11, v11\n"
      "v_exp_f32 v12, v12\n"
      "v_exp_f32 v13, v13\n"
      "v_exp_f32 v14, v14\n"
      "v_exp_f32 v15, v15\n"
      "v_exp_f32 v16, v16\n"
      "v_exp_f32 v17, v17\n"
      "v_smfmac_f32_32x32x32_bf16 v[32:47], v[112:115], v[8:15], v138\n"
      "buffer_load_dwordx4 v148, s[28:31], s66 offen offset:1024 lds\n"
      "v_exp_f32 v18, v18\n"
      "v_exp_f32 v19, v19\n"
      "v_exp_f32 v20, v20\n"
      "v_mfma_f32_32x32x16_bf16 v[0:15], v[208:211], v[96:99], v[64:79]\n"
      "s_waitcnt vmcnt(20)\n"
      "ds_read_b128 a[196:199], v143 offset:12288\n"
      "ds_read_b128 a[212:215], v144 offset:12288\n"
      "v_exp_f32 v21, v21\n"
      "v_exp_f32 v22, v22\n"
      "v_exp_f32 v23, v23\n"
      "v_exp_f32 v24, v24\n"
      "v_exp_f32 v25, v25\n"
      "v_exp_f32 v26, v26\n"
      "v_smfmac_f32_32x32x32_bf16 v[48:63], v[108:111], v[16:23], v138\n"
      "ds_read_b128 v[108:111], v135 offset:54080\n"
      "v_exp_f32 v27, v27\n"
      "v_exp_f32 v28, v28\n"
      "v_exp_f32 v29, v29\n"
      "v_exp_f32 v30, v30\n"
      "v_exp_f32 v31, v31\n"
      "v_exp_f32 v0, v0\n"
      "v_exp_f32 v1, v1\n"
      "v_smfmac_f32_32x32x32_bf16 v[48:63], v[112:115], v[24:31], v138\n"
      "ds_read_b128 v[112:115], v137 offset:54080\n"
      "v_exp_f32 v2, v2\n"
      "v_exp_f32 v3, v3\n"
      "v_exp_f32 v4, v4\n"
      "v_mfma_f32_32x32x16_bf16 v[16:31], v[208:211], v[100:103], v[80:95]\n"
      "s_waitcnt lgkmcnt(2)\n"
      "ds_read_b128 v[208:211], v133 offset:56160\n"
      "v_exp_f32 v5, v5\n"
      "v_exp_f32 v6, v6\n"
      "v_exp_f32 v7, v7\n"
      "v_exp_f32 v8, v8\n"
      "v_exp_f32 v9, v9\n"
      "v_exp_f32 v10, v10\n"
      "v_smfmac_f32_32x32x32_bf16 v[32:47], v[212:215], v[0:7], v138\n"
      "s_add_u32 m0, s80, 12288\n"
      "s_xor_b32 s66, s98, 768\n"
      "buffer_load_dwordx4 v142, s[28:31], s66 offen offset:0 lds\n"
      "v_exp_f32 v11, v11\n"
      "v_exp_f32 v12, v12\n"
      "v_exp_f32 v13, v13\n"
      "v_exp_f32 v14, v14\n"
      "v_exp_f32 v15, v15\n"
      "v_exp_f32 v16, v16\n"
      "v_exp_f32 v17, v17\n"
      "v_smfmac_f32_32x32x32_bf16 v[32:47], v[216:219], v[8:15], v138\n"
      "buffer_load_dwordx4 v148, s[28:31], s66 offen offset:1024 lds\n"
      "v_exp_f32 v18, v18\n"
      "v_exp_f32 v19, v19\n"
      "v_exp_f32 v20, v20\n"
      "v_mfma_f32_32x32x16_bf16 v[0:15], v[104:107], v[96:99], v[64:79]\n"
      "s_waitcnt vmcnt(18)\n"
      "ds_read_b128 a[228:231], v143 offset:14336\n"
      "ds_read_b128 a[244:247], v144 offset:14336\n"
      "v_exp_f32 v21, v21\n"
      "v_exp_f32 v22, v22\n"
      "v_exp_f32 v23, v23\n"
      "v_exp_f32 v24, v24\n"
      "v_exp_f32 v25, v25\n"
      "v_exp_f32 v26, v26\n"
      "v_smfmac_f32_32x32x32_bf16 v[48:63], v[212:215], v[16:23], v138\n"
      "ds_read_b128 v[212:215], v135 offset:56160\n"
      "v_exp_f32 v27, v27\n"
      "v_exp_f32 v28, v28\n"
      "v_exp_f32 v29, v29\n"
      "v_exp_f32 v30, v30\n"
      "v_exp_f32 v31, v31\n"
      "v_exp_f32 v0, v0\n"
      "v_exp_f32 v1, v1\n"
      "v_smfmac_f32_32x32x32_bf16 v[48:63], v[216:219], v[24:31], v138\n"
      "ds_read_b128 v[216:219], v137 offset:56160\n"
      "v_exp_f32 v2, v2\n"
      "v_exp_f32 v3, v3\n"
      "v_exp_f32 v4, v4\n"
      "v_mfma_f32_32x32x16_bf16 v[16:31], v[104:107], v[100:103], v[80:95]\n"
      "s_waitcnt lgkmcnt(2)\n"
      "ds_read_b128 v[104:107], v133 offset:58240\n"
      "v_exp_f32 v5, v5\n"
      "v_exp_f32 v6, v6\n"
      "v_exp_f32 v7, v7\n"
      "v_exp_f32 v8, v8\n"
      "v_exp_f32 v9, v9\n"
      "v_exp_f32 v10, v10\n"
      "v_smfmac_f32_32x32x32_bf16 v[32:47], v[108:111], v[0:7], v138\n"
      "s_add_u32 m0, s80, 14336\n"
      "s_xor_b32 s66, s98, 896\n"
      "buffer_load_dwordx4 v142, s[28:31], s66 offen offset:0 lds\n"
      "v_exp_f32 v11, v11\n"
      "v_exp_f32 v12, v12\n"
      "v_exp_f32 v13, v13\n"
      "v_exp_f32 v14, v14\n"
      "v_exp_f32 v15, v15\n"
      "v_exp_f32 v16, v16\n"
      "v_exp_f32 v17, v17\n"
      "v_smfmac_f32_32x32x32_bf16 v[32:47], v[112:115], v[8:15], v138\n"
      "buffer_load_dwordx4 v148, s[28:31], s66 offen offset:1024 lds\n"
      "v_exp_f32 v18, v18\n"
      "v_exp_f32 v19, v19\n"
      "v_exp_f32 v20, v20\n"
      "v_mfma_f32_32x32x16_bf16 v[0:15], v[208:211], v[96:99], v[64:79]\n"
      "s_waitcnt vmcnt(18)\n"
      "ds_read_b128 a[8:11], v143 offset:0\n"
      "ds_read_b128 a[24:27], v144 offset:0\n"
      "v_exp_f32 v21, v21\n"
      "v_exp_f32 v22, v22\n"
      "v_exp_f32 v23, v23\n"
      "v_exp_f32 v24, v24\n"
      "v_exp_f32 v25, v25\n"
      "v_exp_f32 v26, v26\n"
      "v_smfmac_f32_32x32x32_bf16 v[48:63], v[108:111], v[16:23], v138\n"
      "Lhs1_1_%=:\n"
      "s_cbranch_execz Lhr1_%=\n"
      "ds_read_b128 v[108:111], v135 offset:58240\n"
      "v_exp_f32 v27, v27\n"
      "v_exp_f32 v28, v28\n"
      "v_exp_f32 v29, v29\n"
      "v_exp_f32 v30, v30\n"
      "v_exp_f32 v31, v31\n"
      "v_exp_f32 v0, v0\n"
      "v_exp_f32 v1, v1\n"
      "v_smfmac_f32_32x32x32_bf16 v[48:63], v[112:115], v[24:31], v138\n"
      "ds_read_b128 v[112:115], v137 offset:58240\n"
      "v_exp_f32 v2, v2\n"
      "v_exp_f32 v3, v3\n"
      "v_exp_f32 v4, v4\n"
      "v_mfma_f32_32x32x16_bf16 v[16:31], v[208:211], v[100:103], v[80:95]\n"
      "s_waitcnt lgkmcnt(2)\n"
      "ds_read_b128 v[208:211], v133 offset:60320\n"
      "v_exp_f32 v5, v5\n"
      "v_exp_f32 v6, v6\n"
      "v_exp_f32 v7, v7\n"
      "v_exp_f32 v8, v8\n"
      "v_exp_f32 v9, v9\n"
      "v_exp_f32 v10, v10\n"
      "v_smfmac_f32_32x32x32_bf16 v[32:47], v[212:215], v[0:7], v138\n"
      "s_add_u32 m0, s80, 0\n"
      "s_xor_b32 s66, s99, 0\n"
      "buffer_load_dwordx4 v142, s[28:31], s66 offen offset:0 lds\n"
      "v_exp_f32 v11, v11\n"
      "v_exp_f32 v12, v12\n"
      "v_exp_f32 v13, v13\n"
      "v_exp_f32 v14, v14\n"
      "v_exp_f32 v15, v15\n"
      "v_exp_f32 v16, v16\n"
      "v_exp_f32 v17, v17\n"
      "v_smfmac_f32_32x32x32_bf16 v[32:47], v[216:219], v[8:15], v138\n"
      "buffer_load_dwordx4 v148, s[28:31], s66 offen offset:1024 lds\n"
      "v_exp_f32 v18, v18\n"
      "v_exp_f32 v19, v19\n"
      "v_exp_f32 v20, v20\n"
      "v_mfma_f32_32x32x16_bf16 v[0:15], v[104:107], v[96:99], v[64:79]\n"
      "s_waitcnt vmcnt(16)\n"
      "ds_read_b128 a[40:43], v143 offset:2048\n"
      "ds_read_b128 a[56:59], v144 offset:2048\n"
      "v_exp_f32 v21, v21\n"
      "v_exp_f32 v22, v22\n"
      "v_exp_f32 v23, v23\n"
      "v_exp_f32 v24, v24\n"
      "v_exp_f32 v25, v25\n"
      "v_exp_f32 v26, v26\n"
      "v_smfmac_f32_32x32x32_bf16 v[48:63], v[212:215], v[16:23], v138\n"
      "ds_read_b128 v[212:215], v135 offset:60320\n"
      "v_exp_f32 v27, v27\n"
      "v_exp_f32 v28, v28\n"
      "v_exp_f32 v29, v29\n"
      "v_exp_f32 v30, v30\n"
      "v_exp_f32 v31, v31\n"
      "v_exp_f32 v0, v0\n"
      "v_exp_f32 v1, v1\n"
      "v_smfmac_f32_32x32x32_bf16 v[48:63], v[216:219], v[24:31], v138\n"
      "ds_read_b128 v[216:219], v137 offset:60320\n"
      "v_exp_f32 v2, v2\n"
      "v_exp_f32 v3, v3\n"
      "v_exp_f32 v4, v4\n"
      "v_mfma_f32_32x32x16_bf16 v[16:31], v[104:107], v[100:103], v[80:95]\n"
      "s_waitcnt lgkmcnt(2)\n"
      "ds_read_b128 v[104:107], v133 offset:62400\n"
      "v_exp_f32 v5, v5\n"
      "v_exp_f32 v6, v6\n"
      "v_exp_f32 v7, v7\n"
      "v_exp_f32 v8, v8\n"
      "v_exp_f32 v9, v9\n"
      "v_exp_f32 v10, v10\n"
      "v_smfmac_f32_32x32x32_bf16 v[32:47], v[108:111], v[0:7], v138\n"
      "s_add_u32 m0, s80, 2048\n"
      "s_xor_b32 s66, s99, 128\n"
      "buffer_load_dwordx4 v142, s[28:31], s66 offen offset:0 lds\n"
      "v_exp_f32 v11, v11\n"
      "v_exp_f32 v12, v12\n"
      "v_exp_f32 v13, v13\n"
      "v_exp_f32 v14, v14\n"
      "v_exp_f32 v15, v15\n"
      "v_exp_f32 v16, v16\n"
      "v_exp_f32 v17, v17\n"
      "v_smfmac_f32_32x32x32_bf16 v[32:47], v[112:115], v[8:15], v138\n"
      "buffer_load_dwordx4 v148, s[28:31], s66 offen offset:1024 lds\n"
      "v_exp_f32 v18, v18\n"
      "v_exp_f32 v19, v19\n"
      "v_exp_f32 v20, v20\n"
      "v_mfma_f32_32x32x16_bf16 v[0:15], v[208:211], v[96:99], v[64:79]\n"
      "s_waitcnt vmcnt(16)\n"
      "ds_read_b128 a[72:75], v143 offset:4096\n"
      "ds_read_b128 a[88:91], v144 offset:4096\n"
      "v_exp_f32 v21, v21\n"
      "v_exp_f32 v22, v22\n"
      "v_exp_f32 v23, v23\n"
      "v_exp_f32 v24, v24\n"
      "v_exp_f32 v25, v25\n"
      "v_exp_f32 v26, v26\n"
      "v_smfmac_f32_32x32x32_bf16 v[48:63], v[108:111], v[16:23], v138\n"
      "ds_read_b128 v[108:111], v135 offset:62400\n"
      "v_exp_f32 v27, v27\n"
      "v_exp_f32 v28, v28\n"
      "v_exp_f32 v29, v29\n"
      "v_exp_f32 v30, v30\n"
      "v_exp_f32 v31, v31\n"
      "v_exp_f32 v0, v0\n"
      "v_exp_f32 v1, v1\n"
      "v_smfmac_f32_32x32x32_bf16 v[48:63], v[112:115], v[24:31], v138\n"
      "ds_read_b128 v[112:115], v137 offset:62400\n"
      "v_exp_f32 v2, v2\n"
      "v_exp_f32 v3, v3\n"
      "v_exp_f32 v4, v4\n"
      "v_mfma_f32_32x32x16_bf16 v[16:31], v[208:211], v[100:103], v[80:95]\n"
      "s_waitcnt lgkmcnt(2)\n"
      "ds_read_b128 v[208:211], v133 offset:64480\n"
      "v_exp_f32 v5, v5\n"
      "v_exp_f32 v6, v6\n"
      "v_exp_f32 v7, v7\n"
      "v_exp_f32 v8, v8\n"
      "v_exp_f32 v9, v9\n"
      "v_exp_f32 v10, v10\n"
      "v_smfmac_f32_32x32x32_bf16 v[32:47], v[212:215], v[0:7], v138\n"
      "s_add_u32 m0, s80, 4096\n"
      "s_xor_b32 s66, s99, 256\n"
      "buffer_load_dwordx4 v142, s[28:31], s66 offen offset:0 lds\n"
      "v_exp_f32 v11, v11\n"
      "v_exp_f32 v12, v12\n"
      "v_exp_f32 v13, v13\n"
      "v_exp_f32 v14, v14\n"
      "v_exp_f32 v15, v15\n"
      "v_exp_f32 v16, v16\n"
      "v_exp_f32 v17, v17\n"
      "v_smfmac_f32_32x32x32_bf16 v[32:47], v[216:219], v[8:15], v138\n"
      "buffer_load_dwordx4 v148, s[28:31], s66 offen offset:1024 lds\n"
      "v_exp_f32 v18, v18\n"
      "v_exp_f32 v19, v19\n"
      "v_exp_f32 v20, v20\n"
      "v_mfma_f32_32x32x16_bf16 v[0:15], v[104:107], v[96:99], v[64:79]\n"
      "s_waitcnt vmcnt(16)\n"
      "s_barrier\n"
      "s_waitcnt vmcnt(14)\n"
      "ds_read_b128 a[104:107], v143 offset:6144\n"
      "ds_read_b128 a[120:123], v144 offset:6144\n"
      "v_exp_f32 v21, v21\n"
      "v_exp_f32 v22, v22\n"
      "v_exp_f32 v23, v23\n"
      "v_exp_f32 v24, v24\n"
      "v_exp_f32 v25, v25\n"
      "v_exp_f32 v26, v26\n"
      "v_smfmac_f32_32x32x32_bf16 v[48:63], v[212:215], v[16:23], v138\n"
      "ds_read_b128 v[212:215], v135 offset:64480\n"
      "v_exp_f32 v27, v27\n"
      "v_exp_f32 v28, v28\n"
      "v_exp_f32 v29, v29\n"
      "v_exp_f32 v30, v30\n"
      "v_exp_f32 v31, v31\n"
      "v_exp_f32 v0, v0\n"
      "v_exp_f32 v1, v1\n"
      "v_smfmac_f32_32x32x32_bf16 v[48:63], v[216:219], v[24:31], v138\n"
      "ds_read_b128 v[216:219], v137 offset:64480\n"
      "v_exp_f32 v2, v2\n"
      "v_exp_f32 v3, v3\n"
      "v_exp_f32 v4, v4\n"
      "v_mfma_f32_32x32x16_bf16 v[16:31], v[104:107], v[100:103], v[80:95]\n"
      "s_waitcnt lgkmcnt(2)\n"
      "ds_read_b128 v[104:107], v133 offset:0\n"
      "v_exp_f32 v5, v5\n"
      "v_exp_f32 v6, v6\n"
      "v_exp_f32 v7, v7\n"
      "v_exp_f32 v8, v8\n"
      "v_exp_f32 v9, v9\n"
      "v_exp_f32 v10, v10\n"
      "v_smfmac_f32_32x32x32_bf16 v[32:47], v[108:111], v[0:7], v138\n"
      "s_add_u32 m0, s80, 6144\n"
      "s_xor_b32 s66, s99, 384\n"
      "buffer_load_dwordx4 v142, s[28:31], s66 offen offset:0 lds\n"
      "v_exp_f32 v11, v11\n"
      "v_exp_f32 v12, v12\n"
      "v_exp_f32 v13, v13\n"
      "v_exp_f32 v14, v14\n"
      "v_exp_f32 v15, v15\n"
      "v_exp_f32 v16, v16\n"
      "v_exp_f32 v17, v17\n"
      "v_smfmac_f32_32x32x32_bf16 v[32:47], v[112:115], v[8:15], v138\n"
      "buffer_load_dwordx4 v148, s[28:31], s66 offen offset:1024 lds\n"
      "v_exp_f32 v18, v18\n"
      "v_exp_f32 v19, v19\n"
      "v_exp_f32 v20, v20\n"
      "v_mfma_f32_32x32x16_bf16 v[0:15], v[208:211], v[96:99], v[64:79]\n"
      "s_waitcnt vmcnt(14)\n"
      "ds_read_b128 a[136:139], v143 offset:8192\n"
      "ds_read_b128 a[152:155], v144 offset:8192\n"
      "v_exp_f32 v21, v21\n"
      "v_exp_f32 v22, v22\n"
      "v_exp_f32 v23, v23\n"
      "v_exp_f32 v24, v24\n"
      "v_exp_f32 v25, v25\n"
      "v_exp_f32 v26, v26\n"
      "v_smfmac_f32_32x32x32_bf16 v[48:63], v[108:111], v[16:23], v138\n"
      "ds_read_b128 v[108:111], v135 offset:0\n"
      "v_exp_f32 v27, v27\n"
      "v_exp_f32 v28, v28\n"
      "v_exp_f32 v29, v29\n"
      "v_exp_f32 v30, v30\n"
      "v_exp_f32 v31, v31\n"
      "v_exp_f32 v0, v0\n"
      "v_exp_f32 v1, v1\n"
      "v_smfmac_f32_32x32x32_bf16 v[48:63], v[112:115], v[24:31], v138\n"
      "ds_read_b128 v[112:115], v137 offset:0\n"
      "v_exp_f32 v2, v2\n"
      "v_exp_f32 v3, v3\n"
      "v_exp_f32 v4, v4\n"
      "v_mfma_f32_32x32x16_bf16 v[16:31], v[208:211], v[100:103], v[80:95]\n"
      "s_waitcnt lgkmcnt(2)\n"
      "ds_read_b128 v[208:211], v133 offset:2080\n"
      "v_exp_f32 v5, v5\n"
      "v_exp_f32 v6, v6\n"
      "v_exp_f32 v7, v7\n"
      "v_exp_f32 v8, v8\n"
      "v_exp_f32 v9, v9\n"
      "v_exp_f32 v10, v10\n"
      "v_smfmac_f32_32x32x32_bf16 v[32:47], v[212:215], v[0:7], v138\n"
      "s_add_u32 m0, s80, 8192\n"
      "s_xor_b32 s66, s99, 512\n"
      "buffer_load_dwordx4 v142, s[28:31], s66 offen offset:0 lds\n"
      "v_exp_f32 v11, v11\n"
      "v_exp_f32 v12, v12\n"
      "v_exp_f32 v13, v13\n"
      "v_exp_f32 v14, v14\n"
      "v_exp_f32 v15, v15\n"
      "v_exp_f32 v16, v16\n"
      "v_exp_f32 v17, v17\n"
      "v_smfmac_f32_32x32x32_bf16 v[32:47], v[216:219], v[8:15], v138\n"
      "buffer_load_dwordx4 v148, s[28:31], s66 offen offset:1024 lds\n"
      "v_exp_f32 v18, v18\n"
      "v_exp_f32 v19, v19\n"
      "v_exp_f32 v20, v20\n"
      "v_mfma_f32_32x32x16_bf16 v[0:15], v[104:107], v[96:99], v[64:79]\n"
      "s_barrier\n"
      "s_waitcnt vmcnt(14)\n"
      "ds_read_b128 a[168:171], v143 offset:10240\n"
      "ds_read_b128 a[184:187], v144 offset:10240\n"
      "v_exp_f32 v21, v21\n"
      "v_exp_f32 v22, v22\n"
      "v_exp_f32 v23, v23\n"
      "v_exp_f32 v24, v24\n"
      "v_exp_f32 v25, v25\n"
      "v_exp_f32 v26, v26\n"
      "v_smfmac_f32_32x32x32_bf16 v[48:63], v[212:215], v[16:23], v138\n"
      "s_add_u32 m0, s70, 98816\n"
      "s_xor_b32 s67, s69, 98304\n"
      "ds_read_b128 v[212:215], v135 offset:2080\n"
      "buffer_load_dwordx4 v254, s[8:11], s67 offen sc1 lds\n"
      "v_exp_f32 v27, v27\n"
      "v_exp_f32 v28, v28\n"
      "v_exp_f32 v29, v29\n"
      "v_exp_f32 v30, v30\n"
      "v_exp_f32 v31, v31\n"
      "v_exp_f32 v0, v0\n"
      "v_exp_f32 v1, v1\n"
      "v_smfmac_f32_32x32x32_bf16 v[48:63], v[216:219], v[24:31], v138\n"
      "ds_read_b128 v[216:219], v137 offset:2080\n"
      "buffer_load_dwordx4 v254, s[8:11], s67 offen offset:1024 sc1 lds\n"
      "v_exp_f32 v2, v2\n"
      "v_exp_f32 v3, v3\n"
      "v_exp_f32 v4, v4\n"
      "v_mfma_f32_32x32x16_bf16 v[16:31], v[104:107], v[100:103], v[80:95]\n"
      "s_waitcnt lgkmcnt(2)\n"
      "ds_read_b128 v[104:107], v133 offset:4160\n"
      "v_exp_f32 v5, v5\n"
      "v_exp_f32 v6, v6\n"
      "v_exp_f32 v7, v7\n"
      "v_exp_f32 v8, v8\n"
      "v_exp_f32 v9, v9\n"
      "v_exp_f32 v10, v10\n"
      "v_smfmac_f32_32x32x32_bf16 v[32:47], v[108:111], v[0:7], v138\n"
      "s_add_u32 m0, s80, 10240\n"
      "s_xor_b32 s66, s99, 640\n"
      "buffer_load_dwordx4 v142, s[28:31], s66 offen offset:0 lds\n"
      "v_exp_f32 v11, v11\n"
      "v_exp_f32 v12, v12\n"
      "v_exp_f32 v13, v13\n"
      "v_exp_f32 v14, v14\n"
      "v_exp_f32 v15, v15\n"
      "v_exp_f32 v16, v16\n"
      "v_exp_f32 v17, v17\n"
      "v_smfmac_f32_32x32x32_bf16 v[32:47], v[112:115], v[8:15], v138\n"
      "buffer_load_dwordx4 v148, s[28:31], s66 offen offset:1024 lds\n"
      "v_exp_f32 v18, v18\n"
      "v_exp_f32 v19, v19\n"
      "v_exp_f32 v20, v20\n"
      "v_mfma_f32_32x32x16_bf16 v[0:15], v[208:211], v[96:99], v[64:79]\n"
      "s_waitcnt vmcnt(16)\n"
      "ds_read_b128 a[200:203], v143 offset:12288\n"
      "ds_read_b128 a[216:219], v144 offset:12288\n"
      "v_exp_f32 v21, v21\n"
      "v_exp_f32 v22, v22\n"
      "v_exp_f32 v23, v23\n"
      "v_exp_f32 v24, v24\n"
      "v_exp_f32 v25, v25\n"
      "v_exp_f32 v26, v26\n"
      "v_smfmac_f32_32x32x32_bf16 v[48:63], v[108:111], v[16:23], v138\n"
      "ds_read_b128 v[108:111], v135 offset:4160\n"
      "v_exp_f32 v27, v27\n"
      "v_exp_f32 v28, v28\n"
      "v_exp_f32 v29, v29\n"
      "v_exp_f32 v30, v30\n"
      "v_exp_f32 v31, v31\n"
      "v_exp_f32 v0, v0\n"
      "v_exp_f32 v1, v1\n"
      "v_smfmac_f32_32x32x32_bf16 v[48:63], v[112:115], v[24:31], v138\n"
      "ds_read_b128 v[112:115], v137 offset:4160\n"
      "v_exp_f32 v2, v2\n"
      "v_exp_f32 v3, v3\n"
      "v_exp_f32 v4, v4\n"
      "v_mfma_f32_32x32x16_bf16 v[16:31], v[208:211], v[100:103], v[80:95]\n"
      "s_waitcnt lgkmcnt(2)\n"
      "ds_read_b128 v[208:211], v133 offset:6240\n"
      "v_exp_f32 v5, v5\n"
      "v_exp_f32 v6, v6\n"
      "v_exp_f32 v7, v7\n"
      "v_exp_f32 v8, v8\n"
      "v_exp_f32 v9, v9\n"
      "v_exp_f32 v10, v10\n"
      "v_smfmac_f32_32x32x32_bf16 v[32:47], v[212:215], v[0:7], v138\n"
      "s_add_u32 m0, s80, 12288\n"
      "s_xor_b32 s66, s99, 768\n"
      "buffer_load_dwordx4 v142, s[28:31], s66 offen offset:0 lds\n"
      "v_exp_f32 v11, v11\n"
      "v_exp_f32 v12, v12\n"
      "v_exp_f32 v13, v13\n"
      "v_exp_f32 v14, v14\n"
      "v_exp_f32 v15, v15\n"
      "v_exp_f32 v16, v16\n"
      "v_exp_f32 v17, v17\n"
      "v_smfmac_f32_32x32x32_bf16 v[32:47], v[216:219], v[8:15], v138\n"
      "buffer_load_dwordx4 v148, s[28:31], s66 offen offset:1024 lds\n"
      "v_exp_f32 v18, v18\n"
      "v_exp_f32 v19, v19\n"
      "v_exp_f32 v20, v20\n"
      "v_mfma_f32_32x32x16_bf16 v[0:15], v[104:107], v[96:99], v[64:79]\n"
      "s_waitcnt vmcnt(16)\n"
      "ds_read_b128 a[232:235], v143 offset:14336\n"
      "ds_read_b128 a[248:251], v144 offset:14336\n"
      "v_exp_f32 v21, v21\n"
      "v_exp_f32 v22, v22\n"
      "v_exp_f32 v23, v23\n"
      "v_exp_f32 v24, v24\n"
      "v_exp_f32 v25, v25\n"
      "v_exp_f32 v26, v26\n"
      "v_smfmac_f32_32x32x32_bf16 v[48:63], v[212:215], v[16:23], v138\n"
      "s_add_u32 m0, s70, 107136\n"
      "s_xor_b32 s67, s69, 100352\n"
      "ds_read_b128 v[212:215], v135 offset:6240\n"
      "buffer_load_dwordx4 v254, s[8:11], s67 offen sc1 lds\n"
      "v_exp_f32 v27, v27\n"
      "v_exp_f32 v28, v28\n"
      "v_exp_f32 v29, v29\n"
      "v_exp_f32 v30, v30\n"
      "v_exp_f32 v31, v31\n"
      "v_exp_f32 v0, v0\n"
      "v_exp_f32 v1, v1\n"
      "v_smfmac_f32_32x32x32_bf16 v[48:63], v[216:219], v[24:31], v138\n"
      "ds_read_b128 v[216:219], v137 offset:6240\n"
      "buffer_load_dwordx4 v254, s[8:11], s67 offen offset:1024 sc1 lds\n"
      "v_exp_f32 v2, v2\n"
      "v_exp_f32 v3, v3\n"
      "v_exp_f32 v4, v4\n"
      "v_mfma_f32_32x32x16_bf16 v[16:31], v[104:107], v[100:103], v[80:95]\n"
      "s_waitcnt lgkmcnt(2)\n"
      "ds_read_b128 v[104:107], v133 offset:8320\n"
      "v_exp_f32 v5, v5\n"
      "v_exp_f32 v6, v6\n"
      "v_exp_f32 v7, v7\n"
      "v_exp_f32 v8, v8\n"
      "v_exp_f32 v9, v9\n"
      "v_exp_f32 v10, v10\n"
      "v_smfmac_f32_32x32x32_bf16 v[32:47], v[108:111], v[0:7], v138\n"
      "s_add_u32 m0, s80, 14336\n"
      "s_xor_b32 s66, s99, 896\n"
      "buffer_load_dwordx4 v142, s[28:31], s66 offen offset:0 lds\n"
      "v_exp_f32 v11, v11\n"
      "v_exp_f32 v12, v12\n"
      "v_exp_f32 v13, v13\n"
      "v_exp_f32 v14, v14\n"
      "v_exp_f32 v15, v15\n"
      "v_exp_f32 v16, v16\n"
      "v_exp_f32 v17, v17\n"
      "v_smfmac_f32_32x32x32_bf16 v[32:47], v[112:115], v[8:15], v138\n"
      "buffer_load_dwordx4 v148, s[28:31], s66 offen offset:1024 lds\n"
      "v_exp_f32 v18, v18\n"
      "v_exp_f32 v19, v19\n"
      "v_exp_f32 v20, v20\n"
      "v_mfma_f32_32x32x16_bf16 v[0:15], v[208:211], v[96:99], v[64:79]\n"
      "s_waitcnt vmcnt(18)\n"
      "ds_read_b128 a[12:15], v143 offset:0\n"
      "ds_read_b128 a[28:31], v144 offset:0\n"
      "v_exp_f32 v21, v21\n"
      "v_exp_f32 v22, v22\n"
      "v_exp_f32 v23, v23\n"
      "v_exp_f32 v24, v24\n"
      "v_exp_f32 v25, v25\n"
      "v_exp_f32 v26, v26\n"
      "v_smfmac_f32_32x32x32_bf16 v[48:63], v[108:111], v[16:23], v138\n"
      "ds_read_b128 v[108:111], v135 offset:8320\n"
      "v_exp_f32 v27, v27\n"
      "v_exp_f32 v28, v28\n"
      "v_exp_f32 v29, v29\n"
      "v_exp_f32 v30, v30\n"
      "v_exp_f32 v31, v31\n"
      "v_exp_f32 v0, v0\n"
      "v_exp_f32 v1, v1\n"
      "v_smfmac_f32_32x32x32_bf16 v[48:63], v[112:115], v[24:31], v138\n"
      "ds_read_b128 v[112:115], v137 offset:8320\n"
      "v_exp_f32 v2, v2\n"
      "v_exp_f32 v3, v3\n"
      "v_exp_f32 v4, v4\n"
      "v_mfma_f32_32x32x16_bf16 v[16:31], v[208:211], v[100:103], v[80:95]\n"
      "s_waitcnt lgkmcnt(4)\n"
      "ds_read_b128 v[208:211], v133 offset:10400\n"
      "v_exp_f32 v5, v5\n"
      "v_exp_f32 v6, v6\n"
      "v_exp_f32 v7, v7\n"
      "v_exp_f32 v8, v8\n"
      "v_exp_f32 v9, v9\n"
      "v_exp_f32 v10, v10\n"
      "v_smfmac_f32_32x32x32_bf16 v[32:47], v[212:215], v[0:7], v138\n"
      "v_exp_f32 v11, v11\n"
      "v_exp_f32 v12, v12\n"
      "v_exp_f32 v13, v13\n"
      "v_exp_f32 v14, v14\n"
      "v_exp_f32 v15, v15\n"
      "v_exp_f32 v16, v16\n"
      "v_exp_f32 v17, v17\n"
      "v_smfmac_f32_32x32x32_bf16 v[32:47], v[216:219], v[8:15], v138\n"
      "v_exp_f32 v18, v18\n"
      "v_exp_f32 v19, v19\n"
      "v_exp_f32 v20, v20\n"
      "v_mfma_f32_32x32x16_bf16 v[0:15], v[104:107], v[96:99], v[64:79]\n"
      "s_waitcnt vmcnt(16)\n"
      "ds_read_b128 a[44:47], v143 offset:2048\n"
      "ds_read_b128 a[60:63], v144 offset:2048\n"
      "v_exp_f32 v21, v21\n"
      "v_exp_f32 v22, v22\n"
      "v_exp_f32 v23, v23\n"
      "v_exp_f32 v24, v24\n"
      "v_exp_f32 v25, v25\n"
      "v_exp_f32 v26, v26\n"
      "v_smfmac_f32_32x32x32_bf16 v[48:63], v[212:215], v[16:23], v138\n"
      "s_add_u32 m0, s70, 115456\n"
      "s_xor_b32 s67, s69, 114688\n"
      "ds_read_b128 v[212:215], v135 offset:10400\n"
      "buffer_load_dwordx4 v254, s[8:11], s67 offen sc1 lds\n"
      "v_exp_f32 v27, v27\n"
      "v_exp_f32 v28, v28\n"
      "v_exp_f32 v29, v29\n"
      "v_exp_f32 v30, v30\n"
      "v_exp_f32 v31, v31\n"
      "v_exp_f32 v0, v0\n"
      "v_exp_f32 v1, v1\n"
      "v_smfmac_f32_32x32x32_bf16 v[48:63], v[216:219], v[24:31], v138\n"
      "ds_read_b128 v[216:219], v137 offset:10400\n"
      "buffer_load_dwordx4 v254, s[8:11], s67 offen offset:1024 sc1 lds\n"
      "v_exp_f32 v2, v2\n"
      "v_exp_f32 v3, v3\n"
      "v_exp_f32 v4, v4\n"
      "v_mfma_f32_32x32x16_bf16 v[16:31], v[104:107], v[100:103], v[80:95]\n"
      "s_waitcnt lgkmcnt(4)\n"
      "ds_read_b128 v[104:107], v133 offset:12480\n"
      "v_exp_f32 v5, v5\n"
      "v_exp_f32 v6, v6\n"
      "v_exp_f32 v7, v7\n"
      "v_exp_f32 v8, v8\n"
      "v_exp_f32 v9, v9\n"
      "v_exp_f32 v10, v10\n"
      "v_smfmac_f32_32x32x32_bf16 v[32:47], v[108:111], v[0:7], v138\n"
      "buffer_load_dwordx4 v[160:163], v228, s[8:11], s84 offen sc1\n"
      "v_exp_f32 v11, v11\n"
      "v_exp_f32 v12, v12\n"
      "v_exp_f32 v13, v13\n"
      "v_exp_f32 v14, v14\n"
      "v_exp_f32 v15, v15\n"
      "v_exp_f32 v16, v16\n"
      "v_exp_f32 v17, v17\n"
      "v_smfmac_f32_32x32x32_bf16 v[32:47], v[112:115], v[8:15], v138\n"
      "buffer_load_dwordx2 v[232:233], v141, s[12:15], s82 offen offset:0\n"
      "v_exp_f32 v18, v18\n"
      "v_exp_f32 v19, v19\n"
      "v_exp_f32 v20, v20\n"
      "v_mfma_f32_32x32x16_bf16 v[0:15], v[208:211], v[96:99], v[64:79]\n"
      "s_waitcnt vmcnt(18)\n"
      "ds_read_b128 a[76:79], v143 offset:4096\n"
      "ds_read_b128 a[92:95], v144 offset:4096\n"
      "v_exp_f32 v21, v21\n"
      "v_exp_f32 v22, v22\n"
      "v_exp_f32 v23, v23\n"
      "v_exp_f32 v24, v24\n"
      "v_exp_f32 v25, v25\n"
      "v_exp_f32 v26, v26\n"
      "v_smfmac_f32_32x32x32_bf16 v[48:63], v[108:111], v[16:23], v138\n"
      "ds_read_b128 v[108:111], v135 offset:12480\n"
      "v_exp_f32 v27, v27\n"
      "v_exp_f32 v28, v28\n"
      "v_exp_f32 v29, v29\n"
      "v_exp_f32 v30, v30\n"
      "v_exp_f32 v31, v31\n"
      "v_exp_f32 v0, v0\n"
      "v_exp_f32 v1, v1\n"
      "v_smfmac_f32_32x32x32_bf16 v[48:63], v[112:115], v[24:31], v138\n"
      "ds_read_b128 v[112:115], v137 offset:12480\n"
      "v_exp_f32 v2, v2\n"
      "v_exp_f32 v3, v3\n"
      "v_exp_f32 v4, v4\n"
      "v_mfma_f32_32x32x16_bf16 v[16:31], v[208:211], v[100:103], v[80:95]\n"
      "s_waitcnt lgkmcnt(4)\n"
      "ds_read_b128 v[208:211], v133 offset:14560\n"
      "v_exp_f32 v5, v5\n"
      "v_exp_f32 v6, v6\n"
      "v_exp_f32 v7, v7\n"
      "v_exp_f32 v8, v8\n"
      "v_exp_f32 v9, v9\n"
      "v_exp_f32 v10, v10\n"
      "v_smfmac_f32_32x32x32_bf16 v[32:47], v[212:215], v[0:7], v138\n"
      "buffer_load_dwordx2 v[234:235], v141, s[12:15], s82 offen offset:16\n"
      "v_exp_f32 v11, v11\n"
      "v_exp_f32 v12, v12\n"
      "v_exp_f32 v13, v13\n"
      "v_exp_f32 v14, v14\n"
      "v_exp_f32 v15, v15\n"
      "v_exp_f32 v16, v16\n"
      "v_exp_f32 v17, v17\n"
      "v_smfmac_f32_32x32x32_bf16 v[32:47], v[216:219], v[8:15], v138\n"
      "buffer_load_dwordx2 v[236:237], v141, s[12:15], s82 offen offset:32\n"
      "v_exp_f32 v18, v18\n"
      "v_exp_f32 v19, v19\n"
      "v_exp_f32 v20, v20\n"
      "v_mfma_f32_32x32x16_bf16 v[0:15], v[104:107], v[96:99], v[64:79]\n"
      "s_waitcnt vmcnt(18)\n"
      "ds_read_b128 a[108:111], v143 offset:6144\n"
      "ds_read_b128 a[124:127], v144 offset:6144\n"
      "v_exp_f32 v21, v21\n"
      "v_exp_f32 v22, v22\n"
      "v_exp_f32 v23, v23\n"
      "v_exp_f32 v24, v24\n"
      "v_exp_f32 v25, v25\n"
      "v_exp_f32 v26, v26\n"
      "v_smfmac_f32_32x32x32_bf16 v[48:63], v[212:215], v[16:23], v138\n"
      "s_add_u32 m0, s70, 123776\n"
      "s_xor_b32 s67, s69, 116736\n"
      "ds_read_b128 v[212:215], v135 offset:14560\n"
      "buffer_load_dwordx4 v254, s[8:11], s67 offen sc1 lds\n"
      "v_exp_f32 v27, v27\n"
      "v_exp_f32 v28, v28\n"
      "v_exp_f32 v29, v29\n"
      "v_exp_f32 v30, v30\n"
      "v_exp_f32 v31, v31\n"
      "v_exp_f32 v0, v0\n"
      "v_exp_f32 v1, v1\n"
      "v_smfmac_f32_32x32x32_bf16 v[48:63], v[216:219], v[24:31], v138\n"
      "ds_read_b128 v[216:219], v137 offset:14560\n"
      "buffer_load_dwordx4 v254, s[8:11], s67 offen offset:1024 sc1 lds\n"
      "v_exp_f32 v2, v2\n"
      "v_exp_f32 v3, v3\n"
      "v_exp_f32 v4, v4\n"
      "v_mfma_f32_32x32x16_bf16 v[16:31], v[104:107], v[100:103], v[80:95]\n"
      "s_waitcnt lgkmcnt(4)\n"
      "ds_read_b128 v[104:107], v133 offset:16640\n"
      "v_exp_f32 v5, v5\n"
      "v_exp_f32 v6, v6\n"
      "v_exp_f32 v7, v7\n"
      "v_exp_f32 v8, v8\n"
      "v_exp_f32 v9, v9\n"
      "v_exp_f32 v10, v10\n"
      "v_smfmac_f32_32x32x32_bf16 v[32:47], v[108:111], v[0:7], v138\n"
      "buffer_load_dwordx2 v[238:239], v141, s[12:15], s82 offen offset:48\n"
      "v_exp_f32 v11, v11\n"
      "v_exp_f32 v12, v12\n"
      "v_exp_f32 v13, v13\n"
      "v_exp_f32 v14, v14\n"
      "v_exp_f32 v15, v15\n"
      "v_exp_f32 v16, v16\n"
      "v_exp_f32 v17, v17\n"
      "v_smfmac_f32_32x32x32_bf16 v[32:47], v[112:115], v[8:15], v138\n"
      "buffer_load_dword v250, v149, s[20:23], 0 offen\n"
      "v_exp_f32 v18, v18\n"
      "v_exp_f32 v19, v19\n"
      "v_exp_f32 v20, v20\n"
      "v_mfma_f32_32x32x16_bf16 v[0:15], v[208:211], v[96:99], v[64:79]\n"
      "s_waitcnt vmcnt(20)\n"
      "ds_read_b128 a[140:143], v143 offset:8192\n"
      "ds_read_b128 a[156:159], v144 offset:8192\n"
      "v_exp_f32 v21, v21\n"
      "v_exp_f32 v22, v22\n"
      "v_exp_f32 v23, v23\n"
      "v_exp_f32 v24, v24\n"
      "v_exp_f32 v25, v25\n"
      "v_exp_f32 v26, v26\n"
      "v_smfmac_f32_32x32x32_bf16 v[48:63], v[108:111], v[16:23], v138\n"
      "ds_read_b128 v[108:111], v135 offset:16640\n"
      "v_exp_f32 v27, v27\n"
      "v_exp_f32 v28, v28\n"
      "v_exp_f32 v29, v29\n"
      "v_exp_f32 v30, v30\n"
      "v_exp_f32 v31, v31\n"
      "v_exp_f32 v0, v0\n"
      "v_exp_f32 v1, v1\n"
      "v_smfmac_f32_32x32x32_bf16 v[48:63], v[112:115], v[24:31], v138\n"
      "ds_read_b128 v[112:115], v137 offset:16640\n"
      "v_exp_f32 v2, v2\n"
      "v_exp_f32 v3, v3\n"
      "v_exp_f32 v4, v4\n"
      "v_mfma_f32_32x32x16_bf16 v[16:31], v[208:211], v[100:103], v[80:95]\n"
      "s_waitcnt lgkmcnt(4)\n"
      "ds_read_b128 v[208:211], v133 offset:18720\n"
      "v_exp_f32 v5, v5\n"
      "v_exp_f32 v6, v6\n"
      "v_exp_f32 v7, v7\n"
      "v_exp_f32 v8, v8\n"
      "v_exp_f32 v9, v9\n"
      "v_exp_f32 v10, v10\n"
      "v_smfmac_f32_32x32x32_bf16 v[32:47], v[212:215], v[0:7], v138\n"
      "buffer_load_dword v250, v149, s[32:35], 0 offen\n"
      "v_exp_f32 v11, v11\n"
      "v_exp_f32 v12, v12\n"
      "v_exp_f32 v13, v13\n"
      "v_exp_f32 v14, v14\n"
      "v_exp_f32 v15, v15\n"
      "v_exp_f32 v16, v16\n"
      "v_exp_f32 v17, v17\n"
      "v_smfmac_f32_32x32x32_bf16 v[32:47], v[216:219], v[8:15], v138\n"
      "s_lshl_b32 s67, s58, 7\n"
      "buffer_load_dword v250, off, s[48:51], s67\n"
      "v_exp_f32 v18, v18\n"
      "v_exp_f32 v19, v19\n"
      "v_exp_f32 v20, v20\n"
      "v_mfma_f32_32x32x16_bf16 v[0:15], v[104:107], v[96:99], v[64:79]\n"
      "s_waitcnt vmcnt(18)\n"
      "ds_read_b128 a[172:175], v143 offset:10240\n"
      "ds_read_b128 a[188:191], v144 offset:10240\n"
      "v_exp_f32 v21, v21\n"
      "v_exp_f32 v22, v22\n"
      "v_exp_f32 v23, v23\n"
      "v_exp_f32 v24, v24\n"
      "v_exp_f32 v25, v25\n"
      "v_exp_f32 v26, v26\n"
      "v_smfmac_f32_32x32x32_bf16 v[48:63], v[212:215], v[16:23], v138\n"
      "ds_read_b128 v[212:215], v135 offset:18720\n"
      "v_exp_f32 v27, v27\n"
      "v_exp_f32 v28, v28\n"
      "v_exp_f32 v29, v29\n"
      "v_exp_f32 v30, v30\n"
      "v_exp_f32 v31, v31\n"
      "v_exp_f32 v0, v0\n"
      "v_exp_f32 v1, v1\n"
      "v_smfmac_f32_32x32x32_bf16 v[48:63], v[216:219], v[24:31], v138\n"
      "ds_read_b128 v[216:219], v137 offset:18720\n"
      "v_exp_f32 v2, v2\n"
      "v_exp_f32 v3, v3\n"
      "v_exp_f32 v4, v4\n"
      "v_mfma_f32_32x32x16_bf16 v[16:31], v[104:107], v[100:103], v[80:95]\n"
      "s_waitcnt lgkmcnt(4)\n"
      "ds_read_b128 v[104:107], v133 offset:20800\n"
      "v_exp_f32 v5, v5\n"
      "v_exp_f32 v6, v6\n"
      "v_exp_f32 v7, v7\n"
      "v_exp_f32 v8, v8\n"
      "v_exp_f32 v9, v9\n"
      "v_exp_f32 v10, v10\n"
      "v_smfmac_f32_32x32x32_bf16 v[32:47], v[108:111], v[0:7], v138\n"
      "buffer_load_dwordx2 v[126:127], v248, s[8:11], s89 offen sc1\n"
      "v_exp_f32 v11, v11\n"
      "v_exp_f32 v12, v12\n"
      "v_exp_f32 v13, v13\n"
      "v_exp_f32 v14, v14\n"
      "v_exp_f32 v15, v15\n"
      "v_exp_f32 v16, v16\n"
      "v_exp_f32 v17, v17\n"
      "v_smfmac_f32_32x32x32_bf16 v[32:47], v[112:115], v[8:15], v138\n"
      "buffer_load_dwordx4 v[116:119], v145, s[8:11], s87 offen sc1\n"
      "v_exp_f32 v18, v18\n"
      "v_exp_f32 v19, v19\n"
      "v_exp_f32 v20, v20\n"
      "v_mfma_f32_32x32x16_bf16 v[0:15], v[208:211], v[96:99], v[64:79]\n"
      "s_waitcnt vmcnt(18)\n"
      "ds_read_b128 a[204:207], v143 offset:12288\n"
      "ds_read_b128 a[220:223], v144 offset:12288\n"
      "v_exp_f32 v21, v21\n"
      "v_exp_f32 v22, v22\n"
      "v_exp_f32 v23, v23\n"
      "v_exp_f32 v24, v24\n"
      "v_exp_f32 v25, v25\n"
      "v_exp_f32 v26, v26\n"
      "v_smfmac_f32_32x32x32_bf16 v[48:63], v[108:111], v[16:23], v138\n"
      "ds_read_b128 v[108:111], v135 offset:20800\n"
      "v_exp_f32 v27, v27\n"
      "v_exp_f32 v28, v28\n"
      "v_exp_f32 v29, v29\n"
      "v_exp_f32 v30, v30\n"
      "v_exp_f32 v31, v31\n"
      "v_exp_f32 v0, v0\n"
      "v_exp_f32 v1, v1\n"
      "v_smfmac_f32_32x32x32_bf16 v[48:63], v[112:115], v[24:31], v138\n"
      "ds_read_b128 v[112:115], v137 offset:20800\n"
      "v_exp_f32 v2, v2\n"
      "v_exp_f32 v3, v3\n"
      "v_exp_f32 v4, v4\n"
      "v_mfma_f32_32x32x16_bf16 v[16:31], v[208:211], v[100:103], v[80:95]\n"
      "s_waitcnt lgkmcnt(4)\n"
      "ds_read_b128 v[208:211], v133 offset:22880\n"
      "v_exp_f32 v5, v5\n"
      "v_exp_f32 v6, v6\n"
      "v_exp_f32 v7, v7\n"
      "v_exp_f32 v8, v8\n"
      "v_exp_f32 v9, v9\n"
      "v_exp_f32 v10, v10\n"
      "v_smfmac_f32_32x32x32_bf16 v[32:47], v[212:215], v[0:7], v138\n"
      "v_exp_f32 v11, v11\n"
      "v_exp_f32 v12, v12\n"
      "v_exp_f32 v13, v13\n"
      "v_exp_f32 v14, v14\n"
      "v_exp_f32 v15, v15\n"
      "v_exp_f32 v16, v16\n"
      "v_exp_f32 v17, v17\n"
      "v_smfmac_f32_32x32x32_bf16 v[32:47], v[216:219], v[8:15], v138\n"
      "v_exp_f32 v18, v18\n"
      "v_exp_f32 v19, v19\n"
      "v_exp_f32 v20, v20\n"
      "v_mfma_f32_32x32x16_bf16 v[0:15], v[104:107], v[96:99], v[64:79]\n"
      "s_waitcnt vmcnt(14)\n"
      "ds_read_b128 a[236:239], v143 offset:14336\n"
      "ds_read_b128 a[252:255], v144 offset:14336\n"
      "v_exp_f32 v21, v21\n"
      "v_exp_f32 v22, v22\n"
      "v_exp_f32 v23, v23\n"
      "v_exp_f32 v24, v24\n"
      "v_exp_f32 v25, v25\n"
      "v_exp_f32 v26, v26\n"
      "v_smfmac_f32_32x32x32_bf16 v[48:63], v[212:215], v[16:23], v138\n"
      "ds_read_b128 v[212:215], v135 offset:22880\n"
      "v_exp_f32 v27, v27\n"
      "v_exp_f32 v28, v28\n"
      "v_exp_f32 v29, v29\n"
      "v_exp_f32 v30, v30\n"
      "v_exp_f32 v31, v31\n"
      "v_exp_f32 v0, v0\n"
      "v_exp_f32 v1, v1\n"
      "v_smfmac_f32_32x32x32_bf16 v[48:63], v[216:219], v[24:31], v138\n"
      "ds_read_b128 v[216:219], v137 offset:22880\n"
      "v_exp_f32 v2, v2\n"
      "v_exp_f32 v3, v3\n"
      "v_exp_f32 v4, v4\n"
      "v_mfma_f32_32x32x16_bf16 v[16:31], v[104:107], v[100:103], v[80:95]\n"
      "s_waitcnt lgkmcnt(4)\n"
      "ds_read_b128 v[104:107], v133 offset:24960\n"
      "v_exp_f32 v5, v5\n"
      "v_exp_f32 v6, v6\n"
      "v_exp_f32 v7, v7\n"
      "v_exp_f32 v8, v8\n"
      "v_exp_f32 v9, v9\n"
      "v_exp_f32 v10, v10\n"
      "v_smfmac_f32_32x32x32_bf16 v[32:47], v[108:111], v[0:7], v138\n"
      "v_exp_f32 v11, v11\n"
      "v_exp_f32 v12, v12\n"
      "v_exp_f32 v13, v13\n"
      "v_exp_f32 v14, v14\n"
      "v_exp_f32 v15, v15\n"
      "v_exp_f32 v16, v16\n"
      "v_exp_f32 v17, v17\n"
      "v_smfmac_f32_32x32x32_bf16 v[32:47], v[112:115], v[8:15], v138\n"
      "v_exp_f32 v18, v18\n"
      "v_exp_f32 v19, v19\n"
      "v_exp_f32 v20, v20\n"
      "v_mfma_f32_32x32x16_bf16 v[0:15], v[208:211], v[96:99], v[64:79]\n"
      "v_exp_f32 v21, v21\n"
      "v_exp_f32 v22, v22\n"
      "v_exp_f32 v23, v23\n"
      "v_exp_f32 v24, v24\n"
      "v_exp_f32 v25, v25\n"
      "v_exp_f32 v26, v26\n"
      "v_smfmac_f32_32x32x32_bf16 v[48:63], v[108:111], v[16:23], v138\n"
      "ds_read_b128 v[108:111], v135 offset:24960\n"
      "v_exp_f32 v27, v27\n"
      "v_exp_f32 v28, v28\n"
      "v_exp_f32 v29, v29\n"
      "v_exp_f32 v30, v30\n"
      "v_exp_f32 v31, v31\n"
      "v_exp_f32 v0, v0\n"
      "v_exp_f32 v1, v1\n"
      "v_smfmac_f32_32x32x32_bf16 v[48:63], v[112:115], v[24:31], v138\n"
      "ds_read_b128 v[112:115], v137 offset:24960\n"
      "Lhs1_2_%=:\n"
      "s_cbranch_execz Lhr1_%=\n"
      "v_exp_f32 v2, v2\n"
      "v_exp_f32 v3, v3\n"
      "v_exp_f32 v4, v4\n"
      "v_mfma_f32_32x32x16_bf16 v[16:31], v[208:211], v[100:103], v[80:95]\n"
      "s_waitcnt lgkmcnt(2)\n"
      "ds_read_b128 v[208:211], v133 offset:27040\n"
      "v_exp_f32 v5, v5\n"
      "v_exp_f32 v6, v6\n"
      "v_exp_f32 v7, v7\n"
      "v_exp_f32 v8, v8\n"
      "v_exp_f32 v9, v9\n"
      "v_exp_f32 v10, v10\n"
      "v_smfmac_f32_32x32x32_bf16 v[32:47], v[212:215], v[0:7], v138\n"
      "v_exp_f32 v11, v11\n"
      "v_exp_f32 v12, v12\n"
      "v_exp_f32 v13, v13\n"
      "v_exp_f32 v14, v14\n"
      "v_exp_f32 v15, v15\n"
      "v_exp_f32 v16, v16\n"
      "v_exp_f32 v17, v17\n"
      "v_smfmac_f32_32x32x32_bf16 v[32:47], v[216:219], v[8:15], v138\n"
      "v_exp_f32 v18, v18\n"
      "v_exp_f32 v19, v19\n"
      "v_exp_f32 v20, v20\n"
      "v_mfma_f32_32x32x16_bf16 v[0:15], v[104:107], v[96:99], v[64:79]\n"
      "v_exp_f32 v21, v21\n"
      "v_exp_f32 v22, v22\n"
      "v_exp_f32 v23, v23\n"
      "v_exp_f32 v24, v24\n"
      "v_exp_f32 v25, v25\n"
      "v_exp_f32 v26, v26\n"
      "v_smfmac_f32_32x32x32_bf16 v[48:63], v[212:215], v[16:23], v138\n"
      "ds_read_b128 v[212:215], v135 offset:27040\n"
      "v_exp_f32 v27, v27\n"
      "v_exp_f32 v28, v28\n"
      "v_exp_f32 v29, v29\n"
      "v_exp_f32 v30, v30\n"
      "v_exp_f32 v31, v31\n"
      "v_exp_f32 v0, v0\n"
      "v_exp_f32 v1, v1\n"
      "v_smfmac_f32_32x32x32_bf16 v[48:63], v[216:219], v[24:31], v138\n"
      "ds_read_b128 v[216:219], v137 offset:27040\n"
      "v_exp_f32 v2, v2\n"
      "v_exp_f32 v3, v3\n"
      "v_exp_f32 v4, v4\n"
      "v_mfma_f32_32x32x16_bf16 v[16:31], v[104:107], v[100:103], v[80:95]\n"
      "s_waitcnt lgkmcnt(2)\n"
      "ds_read_b128 v[104:107], v133 offset:29120\n"
      "v_exp_f32 v5, v5\n"
      "v_exp_f32 v6, v6\n"
      "v_exp_f32 v7, v7\n"
      "v_exp_f32 v8, v8\n"
      "v_exp_f32 v9, v9\n"
      "v_exp_f32 v10, v10\n"
      "v_smfmac_f32_32x32x32_bf16 v[32:47], v[108:111], v[0:7], v138\n"
      "v_exp_f32 v11, v11\n"
      "v_exp_f32 v12, v12\n"
      "v_exp_f32 v13, v13\n"
      "v_exp_f32 v14, v14\n"
      "v_exp_f32 v15, v15\n"
      "v_exp_f32 v16, v16\n"
      "v_exp_f32 v17, v17\n"
      "v_smfmac_f32_32x32x32_bf16 v[32:47], v[112:115], v[8:15], v138\n"
      "v_exp_f32 v18, v18\n"
      "v_exp_f32 v19, v19\n"
      "v_exp_f32 v20, v20\n"
      "v_mfma_f32_32x32x16_bf16 v[0:15], v[208:211], v[96:99], v[64:79]\n"
      "v_exp_f32 v21, v21\n"
      "v_exp_f32 v22, v22\n"
      "v_exp_f32 v23, v23\n"
      "v_exp_f32 v24, v24\n"
      "v_exp_f32 v25, v25\n"
      "v_exp_f32 v26, v26\n"
      "v_smfmac_f32_32x32x32_bf16 v[48:63], v[108:111], v[16:23], v138\n"
      "ds_read_b128 v[108:111], v135 offset:29120\n"
      "v_exp_f32 v27, v27\n"
      "v_exp_f32 v28, v28\n"
      "v_exp_f32 v29, v29\n"
      "v_exp_f32 v30, v30\n"
      "v_exp_f32 v31, v31\n"
      "v_exp_f32 v0, v0\n"
      "v_exp_f32 v1, v1\n"
      "v_smfmac_f32_32x32x32_bf16 v[48:63], v[112:115], v[24:31], v138\n"
      "ds_read_b128 v[112:115], v137 offset:29120\n"
      "v_exp_f32 v2, v2\n"
      "v_exp_f32 v3, v3\n"
      "v_exp_f32 v4, v4\n"
      "v_mfma_f32_32x32x16_bf16 v[16:31], v[208:211], v[100:103], v[80:95]\n"
      "s_waitcnt lgkmcnt(2)\n"
      "ds_read_b128 v[208:211], v133 offset:31200\n"
      "v_exp_f32 v5, v5\n"
      "v_exp_f32 v6, v6\n"
      "v_exp_f32 v7, v7\n"
      "v_exp_f32 v8, v8\n"
      "v_exp_f32 v9, v9\n"
      "v_exp_f32 v10, v10\n"
      "v_smfmac_f32_32x32x32_bf16 v[32:47], v[212:215], v[0:7], v138\n"
      "v_exp_f32 v11, v11\n"
      "v_exp_f32 v12, v12\n"
      "v_exp_f32 v13, v13\n"
      "v_exp_f32 v14, v14\n"
      "v_exp_f32 v15, v15\n"
      "v_exp_f32 v16, v16\n"
      "v_exp_f32 v17, v17\n"
      "v_smfmac_f32_32x32x32_bf16 v[32:47], v[216:219], v[8:15], v138\n"
      "v_exp_f32 v18, v18\n"
      "v_exp_f32 v19, v19\n"
      "v_exp_f32 v20, v20\n"
      "v_mfma_f32_32x32x16_bf16 v[0:15], v[104:107], v[96:99], v[64:79]\n"
      "s_waitcnt vmcnt(1)\n"
      "v_cmp_ne_u64 vcc, s[56:57], v[126:127]\n"
      "s_nop 0\n"
      "s_cmp_eq_u64 vcc, 0\n"
      "s_cbranch_scc1 Lg14_%=\n"
      "buffer_load_dwordx2 v[126:127], v248, s[8:11], s89 offen sc1\n"
      "Lp15_%=:\n"
      "s_waitcnt vmcnt(0)\n"
      "v_cmp_ne_u64 vcc, s[56:57], v[126:127]\n"
      "s_nop 0\n"
      "s_cmp_eq_u64 vcc, 0\n"
      "s_cbranch_scc1 Lpd16_%=\n"
      "s_cmp_eq_u32 s62, 0\n"
      "s_cbranch_scc1 Lpd16_%=\n"
      "s_sub_u32 s62, s62, 1\n"
      "s_sleep 1\n"
      "buffer_load_dwordx2 v[126:127], v248, s[8:11], s89 offen sc1\n"
      "s_branch Lp15_%=\n"
      "Lpd16_%=:\n"
      "Lg14_%=:\n"
      "s_waitcnt lgkmcnt(12)\n"
      "s_barrier\n"
      "v_exp_f32 v21, v21\n"
      "v_exp_f32 v22, v22\n"
      "v_exp_f32 v23, v23\n"
      "v_exp_f32 v24, v24\n"
      "v_exp_f32 v25, v25\n"
      "v_exp_f32 v26, v26\n"
      "v_smfmac_f32_32x32x32_bf16 v[48:63], v[212:215], v[16:23], v138\n"
      "s_add_u32 m0, s40, 135168\n"
      "s_add_u32 s94, s92, 0\n"
      "ds_read_b128 v[212:215], v135 offset:31200\n"
      "buffer_load_dwordx4 v254, s[8:11], s94 offen offset:0 sc1 lds\n"
      "v_exp_f32 v27, v27\n"
      "v_exp_f32 v28, v28\n"
      "v_exp_f32 v29, v29\n"
      "v_exp_f32 v30, v30\n"
      "v_exp_f32 v31, v31\n"
      "v_exp_f32 v0, v0\n"
      "v_exp_f32 v1, v1\n"
      "v_smfmac_f32_32x32x32_bf16 v[48:63], v[216:219], v[24:31], v138\n"
      "ds_read_b128 v[216:219], v137 offset:31200\n"
      "v_exp_f32 v2, v2\n"
      "v_exp_f32 v3, v3\n"
      "v_exp_f32 v4, v4\n"
      "v_mfma_f32_32x32x16_bf16 v[16:31], v[104:107], v[100:103], v[80:95]\n"
      "s_waitcnt lgkmcnt(2)\n"
      "ds_read_b128 v[104:107], v133 offset:33280\n"
      "v_exp_f32 v5, v5\n"
      "v_exp_f32 v6, v6\n"
      "v_exp_f32 v7, v7\n"
      "v_exp_f32 v8, v8\n"
      "v_exp_f32 v9, v9\n"
      "v_exp_f32 v10, v10\n"
      "v_smfmac_f32_32x32x32_bf16 v[32:47], v[108:111], v[0:7], v138\n"
      "s_add_u32 m0, s73, 0\n"
      "s_xor_b32 s66, s72, 0\n"
      "buffer_load_dwordx4 v249, s[20:23], s66 offen offset:0 lds\n"
      "v_exp_f32 v11, v11\n"
      "v_exp_f32 v12, v12\n"
      "v_exp_f32 v13, v13\n"
      "v_exp_f32 v14, v14\n"
      "v_exp_f32 v15, v15\n"
      "v_exp_f32 v16, v16\n"
      "v_exp_f32 v17, v17\n"
      "v_smfmac_f32_32x32x32_bf16 v[32:47], v[112:115], v[8:15], v138\n"
      "buffer_load_dwordx4 v147, s[20:23], s66 offen offset:1024 lds\n"
      "v_exp_f32 v18, v18\n"
      "v_exp_f32 v19, v19\n"
      "v_exp_f32 v20, v20\n"
      "v_mfma_f32_32x32x16_bf16 v[0:15], v[208:211], v[96:99], v[64:79]\n"
      "s_add_u32 s67, s85, 0\n"
      "buffer_load_dwordx4 v[192:195], v229, s[8:11], s67 offen sc1\n"
      "v_exp_f32 v21, v21\n"
      "v_exp_f32 v22, v22\n"
      "v_exp_f32 v23, v23\n"
      "v_exp_f32 v24, v24\n"
      "v_exp_f32 v25, v25\n"
      "v_exp_f32 v26, v26\n"
      "v_smfmac_f32_32x32x32_bf16 v[48:63], v[108:111], v[16:23], v138\n"
      "s_add_u32 m0, s40, 135168\n"
      "ds_read_b128 v[108:111], v135 offset:33280\n"
      "buffer_load_dwordx4 v254, s[8:11], s94 offen offset:1024 sc1 lds\n"
      "v_exp_f32 v27, v27\n"
      "v_exp_f32 v28, v28\n"
      "v_exp_f32 v29, v29\n"
      "v_exp_f32 v30, v30\n"
      "v_exp_f32 v31, v31\n"
      "v_exp_f32 v0, v0\n"
      "v_exp_f32 v1, v1\n"
      "v_smfmac_f32_32x32x32_bf16 v[48:63], v[112:115], v[24:31], v138\n"
      "ds_read_b128 v[112:115], v137 offset:33280\n"
      "v_exp_f32 v2, v2\n"
      "v_exp_f32 v3, v3\n"
      "v_exp_f32 v4, v4\n"
      "v_mfma_f32_32x32x16_bf16 v[16:31], v[208:211], v[100:103], v[80:95]\n"
      "s_waitcnt lgkmcnt(2)\n"
      "ds_read_b128 v[208:211], v133 offset:35360\n"
      "v_exp_f32 v5, v5\n"
      "v_exp_f32 v6, v6\n"
      "v_exp_f32 v7, v7\n"
      "v_exp_f32 v8, v8\n"
      "v_exp_f32 v9, v9\n"
      "v_exp_f32 v10, v10\n"
      "v_smfmac_f32_32x32x32_bf16 v[32:47], v[212:215], v[0:7], v138\n"
      "s_add_u32 m0, s73, 8192\n"
      "s_xor_b32 s66, s72, 65536\n"
      "buffer_load_dwordx4 v249, s[20:23], s66 offen offset:0 lds\n"
      "v_exp_f32 v11, v11\n"
      "v_exp_f32 v12, v12\n"
      "v_exp_f32 v13, v13\n"
      "v_exp_f32 v14, v14\n"
      "v_exp_f32 v15, v15\n"
      "v_exp_f32 v16, v16\n"
      "v_exp_f32 v17, v17\n"
      "v_smfmac_f32_32x32x32_bf16 v[32:47], v[216:219], v[8:15], v138\n"
      "buffer_load_dwordx4 v147, s[20:23], s66 offen offset:1024 lds\n"
      "v_exp_f32 v18, v18\n"
      "v_exp_f32 v19, v19\n"
      "v_exp_f32 v20, v20\n"
      "v_mfma_f32_32x32x16_bf16 v[0:15], v[104:107], v[96:99], v[64:79]\n"
      "v_exp_f32 v21, v21\n"
      "v_exp_f32 v22, v22\n"
      "v_exp_f32 v23, v23\n"
      "v_exp_f32 v24, v24\n"
      "v_exp_f32 v25, v25\n"
      "v_exp_f32 v26, v26\n"
      "v_smfmac_f32_32x32x32_bf16 v[48:63], v[212:215], v[16:23], v138\n"
      "s_add_u32 m0, s40, 135168\n"
      "ds_read_b128 v[212:215], v135 offset:35360\n"
      "buffer_load_dwordx4 v254, s[8:11], s94 offen offset:2048 sc1 lds\n"
      "v_exp_f32 v27, v27\n"
      "v_exp_f32 v28, v28\n"
      "v_exp_f32 v29, v29\n"
      "v_exp_f32 v30, v30\n"
      "v_exp_f32 v31, v31\n"
      "v_exp_f32 v0, v0\n"
      "v_exp_f32 v1, v1\n"
      "v_smfmac_f32_32x32x32_bf16 v[48:63], v[216:219], v[24:31], v138\n"
      "ds_read_b128 v[216:219], v137 offset:35360\n"
      "v_exp_f32 v2, v2\n"
      "v_exp_f32 v3, v3\n"
      "v_exp_f32 v4, v4\n"
      "v_mfma_f32_32x32x16_bf16 v[16:31], v[104:107], v[100:103], v[80:95]\n"
      "s_waitcnt lgkmcnt(2)\n"
      "ds_read_b128 v[104:107], v133 offset:37440\n"
      "v_exp_f32 v5, v5\n"
      "v_exp_f32 v6, v6\n"
      "v_exp_f32 v7, v7\n"
      "v_exp_f32 v8, v8\n"
      "v_exp_f32 v9, v9\n"
      "v_exp_f32 v10, v10\n"
      "v_smfmac_f32_32x32x32_bf16 v[32:47], v[108:111], v[0:7], v138\n"
      "s_add_u32 m0, s73, 16384\n"
      "s_xor_b32 s66, s72, 131072\n"
      "buffer_load_dwordx4 v249, s[20:23], s66 offen offset:0 lds\n"
      "v_exp_f32 v11, v11\n"
      "v_exp_f32 v12, v12\n"
      "v_exp_f32 v13, v13\n"
      "v_exp_f32 v14, v14\n"
      "v_exp_f32 v15, v15\n"
      "v_exp_f32 v16, v16\n"
      "v_exp_f32 v17, v17\n"
      "v_smfmac_f32_32x32x32_bf16 v[32:47], v[112:115], v[8:15], v138\n"
      "buffer_load_dwordx4 v147, s[20:23], s66 offen offset:1024 lds\n"
      "v_exp_f32 v18, v18\n"
      "v_exp_f32 v19, v19\n"
      "v_exp_f32 v20, v20\n"
      "v_mfma_f32_32x32x16_bf16 v[0:15], v[208:211], v[96:99], v[64:79]\n"
      "s_add_u32 s67, s85, 14336\n"
      "buffer_load_dwordx4 v[196:199], v229, s[8:11], s67 offen sc1\n"
      "v_exp_f32 v21, v21\n"
      "v_exp_f32 v22, v22\n"
      "v_exp_f32 v23, v23\n"
      "v_exp_f32 v24, v24\n"
      "v_exp_f32 v25, v25\n"
      "v_exp_f32 v26, v26\n"
      "v_smfmac_f32_32x32x32_bf16 v[48:63], v[108:111], v[16:23], v138\n"
      "s_add_u32 m0, s40, 135168\n"
      "ds_read_b128 v[108:111], v135 offset:37440\n"
      "buffer_load_dwordx4 v254, s[8:11], s94 offen offset:3072 sc1 lds\n"
      "v_exp_f32 v27, v27\n"
      "v_exp_f32 v28, v28\n"
      "v_exp_f32 v29, v29\n"
      "v_exp_f32 v30, v30\n"
      "v_exp_f32 v31, v31\n"
      "v_exp_f32 v0, v0\n"
      "v_exp_f32 v1, v1\n"
      "v_smfmac_f32_32x32x32_bf16 v[48:63], v[112:115], v[24:31], v138\n"
      "ds_read_b128 v[112:115], v137 offset:37440\n"
      "v_exp_f32 v2, v2\n"
      "v_exp_f32 v3, v3\n"
      "v_exp_f32 v4, v4\n"
      "v_mfma_f32_32x32x16_bf16 v[16:31], v[208:211], v[100:103], v[80:95]\n"
      "s_waitcnt lgkmcnt(2)\n"
      "ds_read_b128 v[208:211], v133 offset:39520\n"
      "v_exp_f32 v5, v5\n"
      "v_exp_f32 v6, v6\n"
      "v_exp_f32 v7, v7\n"
      "v_exp_f32 v8, v8\n"
      "v_exp_f32 v9, v9\n"
      "v_exp_f32 v10, v10\n"
      "v_smfmac_f32_32x32x32_bf16 v[32:47], v[212:215], v[0:7], v138\n"
      "s_add_u32 m0, s73, 24576\n"
      "s_xor_b32 s66, s72, 196608\n"
      "buffer_load_dwordx4 v249, s[20:23], s66 offen offset:0 lds\n"
      "v_exp_f32 v11, v11\n"
      "v_exp_f32 v12, v12\n"
      "v_exp_f32 v13, v13\n"
      "v_exp_f32 v14, v14\n"
      "v_exp_f32 v15, v15\n"
      "v_exp_f32 v16, v16\n"
      "v_exp_f32 v17, v17\n"
      "v_smfmac_f32_32x32x32_bf16 v[32:47], v[216:219], v[8:15], v138\n"
      "buffer_load_dwordx4 v147, s[20:23], s66 offen offset:1024 lds\n"
      "v_exp_f32 v18, v18\n"
      "v_exp_f32 v19, v19\n"
      "v_exp_f32 v20, v20\n"
      "v_mfma_f32_32x32x16_bf16 v[0:15], v[104:107], v[96:99], v[64:79]\n"
      "v_exp_f32 v21, v21\n"
      "v_exp_f32 v22, v22\n"
      "v_exp_f32 v23, v23\n"
      "v_exp_f32 v24, v24\n"
      "v_exp_f32 v25, v25\n"
      "v_exp_f32 v26, v26\n"
      "v_smfmac_f32_32x32x32_bf16 v[48:63], v[212:215], v[16:23], v138\n"
      "s_add_u32 m0, s41, 151552\n"
      "s_add_u32 s94, s93, 0\n"
      "ds_read_b128 v[212:215], v135 offset:39520\n"
      "buffer_load_dwordx4 v254, s[8:11], s94 offen offset:0 sc1 lds\n"
      "v_exp_f32 v27, v27\n"
      "v_exp_f32 v28, v28\n"
      "v_exp_f32 v29, v29\n"
      "v_exp_f32 v30, v30\n"
      "v_exp_f32 v31, v31\n"
      "v_exp_f32 v0, v0\n"
      "v_exp_f32 v1, v1\n"
      "v_smfmac_f32_32x32x32_bf16 v[48:63], v[216:219], v[24:31], v138\n"
      "ds_read_b128 v[216:219], v137 offset:39520\n"
      "v_exp_f32 v2, v2\n"
      "v_exp_f32 v3, v3\n"
      "v_exp_f32 v4, v4\n"
      "v_mfma_f32_32x32x16_bf16 v[16:31], v[104:107], v[100:103], v[80:95]\n"
      "s_waitcnt lgkmcnt(2)\n"
      "ds_read_b128 v[104:107], v133 offset:41600\n"
      "v_exp_f32 v5, v5\n"
      "v_exp_f32 v6, v6\n"
      "v_exp_f32 v7, v7\n"
      "v_exp_f32 v8, v8\n"
      "v_exp_f32 v9, v9\n"
      "v_exp_f32 v10, v10\n"
      "v_smfmac_f32_32x32x32_bf16 v[32:47], v[108:111], v[0:7], v138\n"
      "s_add_u32 m0, s73, 32768\n"
      "s_xor_b32 s66, s72, 262144\n"
      "buffer_load_dwordx4 v249, s[20:23], s66 offen offset:0 lds\n"
      "v_exp_f32 v11, v11\n"
      "v_exp_f32 v12, v12\n"
      "v_exp_f32 v13, v13\n"
      "v_exp_f32 v14, v14\n"
      "v_exp_f32 v15, v15\n"
      "v_exp_f32 v16, v16\n"
      "v_exp_f32 v17, v17\n"
      "v_smfmac_f32_32x32x32_bf16 v[32:47], v[112:115], v[8:15], v138\n"
      "buffer_load_dwordx4 v147, s[20:23], s66 offen offset:1024 lds\n"
      "v_exp_f32 v18, v18\n"
      "v_exp_f32 v19, v19\n"
      "v_exp_f32 v20, v20\n"
      "v_mfma_f32_32x32x16_bf16 v[0:15], v[208:211], v[96:99], v[64:79]\n"
      "s_add_u32 s67, s86, 0\n"
      "buffer_load_dwordx4 v[200:203], v229, s[8:11], s67 offen sc1\n"
      "v_exp_f32 v21, v21\n"
      "v_exp_f32 v22, v22\n"
      "v_exp_f32 v23, v23\n"
      "v_exp_f32 v24, v24\n"
      "v_exp_f32 v25, v25\n"
      "v_exp_f32 v26, v26\n"
      "v_smfmac_f32_32x32x32_bf16 v[48:63], v[108:111], v[16:23], v138\n"
      "s_add_u32 m0, s41, 151552\n"
      "ds_read_b128 v[108:111], v135 offset:41600\n"
      "buffer_load_dwordx4 v254, s[8:11], s94 offen offset:1024 sc1 lds\n"
      "v_exp_f32 v27, v27\n"
      "v_exp_f32 v28, v28\n"
      "v_exp_f32 v29, v29\n"
      "v_exp_f32 v30, v30\n"
      "v_exp_f32 v31, v31\n"
      "v_exp_f32 v0, v0\n"
      "v_exp_f32 v1, v1\n"
      "v_smfmac_f32_32x32x32_bf16 v[48:63], v[112:115], v[24:31], v138\n"
      "ds_read_b128 v[112:115], v137 offset:41600\n"
      "v_exp_f32 v2, v2\n"
      "v_exp_f32 v3, v3\n"
      "v_exp_f32 v4, v4\n"
      "v_mfma_f32_32x32x16_bf16 v[16:31], v[208:211], v[100:103], v[80:95]\n"
      "s_waitcnt lgkmcnt(2)\n"
      "ds_read_b128 v[208:211], v133 offset:43680\n"
      "v_exp_f32 v5, v5\n"
      "v_exp_f32 v6, v6\n"
      "v_exp_f32 v7, v7\n"
      "v_exp_f32 v8, v8\n"
      "v_exp_f32 v9, v9\n"
      "v_exp_f32 v10, v10\n"
      "v_smfmac_f32_32x32x32_bf16 v[32:47], v[212:215], v[0:7], v138\n"
      "s_add_u32 m0, s73, 40960\n"
      "s_xor_b32 s66, s72, 327680\n"
      "buffer_load_dwordx4 v249, s[20:23], s66 offen offset:0 lds\n"
      "v_exp_f32 v11, v11\n"
      "v_exp_f32 v12, v12\n"
      "v_exp_f32 v13, v13\n"
      "v_exp_f32 v14, v14\n"
      "v_exp_f32 v15, v15\n"
      "v_exp_f32 v16, v16\n"
      "v_exp_f32 v17, v17\n"
      "v_smfmac_f32_32x32x32_bf16 v[32:47], v[216:219], v[8:15], v138\n"
      "buffer_load_dwordx4 v147, s[20:23], s66 offen offset:1024 lds\n"
      "v_exp_f32 v18, v18\n"
      "v_exp_f32 v19, v19\n"
      "v_exp_f32 v20, v20\n"
      "v_mfma_f32_32x32x16_bf16 v[0:15], v[104:107], v[96:99], v[64:79]\n"
      "v_exp_f32 v21, v21\n"
      "v_exp_f32 v22, v22\n"
      "v_exp_f32 v23, v23\n"
      "v_exp_f32 v24, v24\n"
      "v_exp_f32 v25, v25\n"
      "v_exp_f32 v26, v26\n"
      "v_smfmac_f32_32x32x32_bf16 v[48:63], v[212:215], v[16:23], v138\n"
      "s_add_u32 m0, s41, 151552\n"
      "ds_read_b128 v[212:215], v135 offset:43680\n"
      "buffer_load_dwordx4 v254, s[8:11], s94 offen offset:2048 sc1 lds\n"
      "v_exp_f32 v27, v27\n"
      "v_exp_f32 v28, v28\n"
      "v_exp_f32 v29, v29\n"
      "v_exp_f32 v30, v30\n"
      "v_exp_f32 v31, v31\n"
      "v_exp_f32 v0, v0\n"
      "v_exp_f32 v1, v1\n"
      "v_smfmac_f32_32x32x32_bf16 v[48:63], v[216:219], v[24:31], v138\n"
      "ds_read_b128 v[216:219], v137 offset:43680\n"
      "v_exp_f32 v2, v2\n"
      "v_exp_f32 v3, v3\n"
      "v_exp_f32 v4, v4\n"
      "v_mfma_f32_32x32x16_bf16 v[16:31], v[104:107], v[100:103], v[80:95]\n"
      "s_waitcnt lgkmcnt(2)\n"
      "ds_read_b128 v[104:107], v133 offset:45760\n"
      "v_exp_f32 v5, v5\n"
      "v_exp_f32 v6, v6\n"
      "v_exp_f32 v7, v7\n"
      "v_exp_f32 v8, v8\n"
      "v_exp_f32 v9, v9\n"
      "v_exp_f32 v10, v10\n"
      "v_smfmac_f32_32x32x32_bf16 v[32:47], v[108:111], v[0:7], v138\n"
      "s_add_u32 m0, s73, 49152\n"
      "s_xor_b32 s66, s72, 393216\n"
      "buffer_load_dwordx4 v249, s[20:23], s66 offen offset:0 lds\n"
      "v_exp_f32 v11, v11\n"
      "v_exp_f32 v12, v12\n"
      "v_exp_f32 v13, v13\n"
      "v_exp_f32 v14, v14\n"
      "v_exp_f32 v15, v15\n"
      "v_exp_f32 v16, v16\n"
      "v_exp_f32 v17, v17\n"
      "v_smfmac_f32_32x32x32_bf16 v[32:47], v[112:115], v[8:15], v138\n"
      "buffer_load_dwordx4 v147, s[20:23], s66 offen offset:1024 lds\n"
      "v_exp_f32 v18, v18\n"
      "v_exp_f32 v19, v19\n"
      "v_exp_f32 v20, v20\n"
      "v_mfma_f32_32x32x16_bf16 v[0:15], v[208:211], v[96:99], v[64:79]\n"
      "s_add_u32 s67, s86, 14336\n"
      "buffer_load_dwordx4 v[204:207], v229, s[8:11], s67 offen sc1\n"
      "v_exp_f32 v21, v21\n"
      "v_exp_f32 v22, v22\n"
      "v_exp_f32 v23, v23\n"
      "v_exp_f32 v24, v24\n"
      "v_exp_f32 v25, v25\n"
      "v_exp_f32 v26, v26\n"
      "v_smfmac_f32_32x32x32_bf16 v[48:63], v[108:111], v[16:23], v138\n"
      "ds_read_b128 v[108:111], v135 offset:45760\n"
      "v_exp_f32 v27, v27\n"
      "v_exp_f32 v28, v28\n"
      "v_exp_f32 v29, v29\n"
      "v_exp_f32 v30, v30\n"
      "v_exp_f32 v31, v31\n"
      "v_exp_f32 v0, v0\n"
      "v_exp_f32 v1, v1\n"
      "v_smfmac_f32_32x32x32_bf16 v[48:63], v[112:115], v[24:31], v138\n"
      "ds_read_b128 v[112:115], v137 offset:45760\n"
      "v_exp_f32 v2, v2\n"
      "v_exp_f32 v3, v3\n"
      "v_exp_f32 v4, v4\n"
      "v_mfma_f32_32x32x16_bf16 v[16:31], v[208:211], v[100:103], v[80:95]\n"
      "s_waitcnt lgkmcnt(2)\n"
      "ds_read_b128 v[208:211], v133 offset:47840\n"
      "v_exp_f32 v5, v5\n"
      "v_exp_f32 v6, v6\n"
      "v_exp_f32 v7, v7\n"
      "v_exp_f32 v8, v8\n"
      "v_exp_f32 v9, v9\n"
      "v_exp_f32 v10, v10\n"
      "v_smfmac_f32_32x32x32_bf16 v[32:47], v[212:215], v[0:7], v138\n"
      "s_add_u32 m0, s73, 57344\n"
      "s_xor_b32 s66, s72, 458752\n"
      "buffer_load_dwordx4 v249, s[20:23], s66 offen offset:0 lds\n"
      "v_exp_f32 v11, v11\n"
      "v_exp_f32 v12, v12\n"
      "v_exp_f32 v13, v13\n"
      "v_exp_f32 v14, v14\n"
      "v_exp_f32 v15, v15\n"
      "v_exp_f32 v16, v16\n"
      "v_exp_f32 v17, v17\n"
      "v_smfmac_f32_32x32x32_bf16 v[32:47], v[216:219], v[8:15], v138\n"
      "buffer_load_dwordx4 v147, s[20:23], s66 offen offset:1024 lds\n"
      "v_exp_f32 v18, v18\n"
      "v_exp_f32 v19, v19\n"
      "v_exp_f32 v20, v20\n"
      "v_mfma_f32_32x32x16_bf16 v[0:15], v[104:107], v[96:99], v[64:79]\n"
      "v_exp_f32 v21, v21\n"
      "v_exp_f32 v22, v22\n"
      "v_exp_f32 v23, v23\n"
      "v_exp_f32 v24, v24\n"
      "v_exp_f32 v25, v25\n"
      "v_exp_f32 v26, v26\n"
      "v_smfmac_f32_32x32x32_bf16 v[48:63], v[212:215], v[16:23], v138\n"
      "ds_read_b128 v[212:215], v135 offset:47840\n"
      "v_exp_f32 v27, v27\n"
      "v_exp_f32 v28, v28\n"
      "v_exp_f32 v29, v29\n"
      "v_exp_f32 v30, v30\n"
      "v_exp_f32 v31, v31\n"
      "v_exp_f32 v0, v0\n"
      "v_exp_f32 v1, v1\n"
      "v_smfmac_f32_32x32x32_bf16 v[48:63], v[216:219], v[24:31], v138\n"
      "ds_read_b128 v[216:219], v137 offset:47840\n"
      "v_exp_f32 v2, v2\n"
      "v_exp_f32 v3, v3\n"
      "v_exp_f32 v4, v4\n"
      "v_mfma_f32_32x32x16_bf16 v[16:31], v[104:107], v[100:103], v[80:95]\n"
      "s_waitcnt lgkmcnt(2)\n"
      "ds_read_b128 v[104:107], v133 offset:49920\n"
      "v_exp_f32 v5, v5\n"
      "v_exp_f32 v6, v6\n"
      "v_exp_f32 v7, v7\n"
      "v_exp_f32 v8, v8\n"
      "v_exp_f32 v9, v9\n"
      "v_exp_f32 v10, v10\n"
      "v_smfmac_f32_32x32x32_bf16 v[32:47], v[108:111], v[0:7], v138\n"
      "v_exp_f32 v11, v11\n"
      "v_exp_f32 v12, v12\n"
      "v_exp_f32 v13, v13\n"
      "v_exp_f32 v14, v14\n"
      "v_exp_f32 v15, v15\n"
      "v_exp_f32 v16, v16\n"
      "v_exp_f32 v17, v17\n"
      "v_smfmac_f32_32x32x32_bf16 v[32:47], v[112:115], v[8:15], v138\n"
      "v_exp_f32 v18, v18\n"
      "v_exp_f32 v19, v19\n"
      "v_exp_f32 v20, v20\n"
      "v_mfma_f32_32x32x16_bf16 v[0:15], v[208:211], v[96:99], v[64:79]\n"
      "buffer_load_dword v250, v230, s[8:11], s95 offen sc1\n"
      "v_exp_f32 v21, v21\n"
      "v_exp_f32 v22, v22\n"
      "v_exp_f32 v23, v23\n"
      "v_exp_f32 v24, v24\n"
      "v_exp_f32 v25, v25\n"
      "v_exp_f32 v26, v26\n"
      "v_smfmac_f32_32x32x32_bf16 v[48:63], v[108:111], v[16:23], v138\n"
      "ds_read_b128 v[108:111], v135 offset:49920\n"
      "v_exp_f32 v27, v27\n"
      "v_exp_f32 v28, v28\n"
      "v_exp_f32 v29, v29\n"
      "v_exp_f32 v30, v30\n"
      "v_exp_f32 v31, v31\n"
      "v_exp_f32 v0, v0\n"
      "v_exp_f32 v1, v1\n"
      "v_smfmac_f32_32x32x32_bf16 v[48:63], v[112:115], v[24:31], v138\n"
      "ds_read_b128 v[112:115], v137 offset:49920\n"
      "v_exp_f32 v2, v2\n"
      "v_exp_f32 v3, v3\n"
      "v_exp_f32 v4, v4\n"
      "v_mfma_f32_32x32x16_bf16 v[16:31], v[208:211], v[100:103], v[80:95]\n"
      "s_waitcnt lgkmcnt(2)\n"
      "ds_read_b128 v[208:211], v133 offset:52000\n"
      "v_exp_f32 v5, v5\n"
      "v_exp_f32 v6, v6\n"
      "v_exp_f32 v7, v7\n"
      "v_exp_f32 v8, v8\n"
      "v_exp_f32 v9, v9\n"
      "v_exp_f32 v10, v10\n"
      "v_smfmac_f32_32x32x32_bf16 v[32:47], v[212:215], v[0:7], v138\n"
      "v_exp_f32 v11, v11\n"
      "v_exp_f32 v12, v12\n"
      "v_exp_f32 v13, v13\n"
      "v_exp_f32 v14, v14\n"
      "v_exp_f32 v15, v15\n"
      "v_exp_f32 v16, v16\n"
      "v_exp_f32 v17, v17\n"
      "v_smfmac_f32_32x32x32_bf16 v[32:47], v[216:219], v[8:15], v138\n"
      "v_exp_f32 v18, v18\n"
      "v_exp_f32 v19, v19\n"
      "v_exp_f32 v20, v20\n"
      "v_mfma_f32_32x32x16_bf16 v[0:15], v[104:107], v[96:99], v[64:79]\n"
      "buffer_load_dword v250, v231, s[8:11], s95 offen sc1\n"
      "v_exp_f32 v21, v21\n"
      "v_exp_f32 v22, v22\n"
      "v_exp_f32 v23, v23\n"
      "v_exp_f32 v24, v24\n"
      "v_exp_f32 v25, v25\n"
      "v_exp_f32 v26, v26\n"
      "v_smfmac_f32_32x32x32_bf16 v[48:63], v[212:215], v[16:23], v138\n"
      "ds_read_b128 v[212:215], v135 offset:52000\n"
      "v_exp_f32 v27, v27\n"
      "v_exp_f32 v28, v28\n"
      "v_exp_f32 v29, v29\n"
      "v_exp_f32 v30, v30\n"
      "v_exp_f32 v31, v31\n"
      "v_exp_f32 v0, v0\n"
      "v_exp_f32 v1, v1\n"
      "v_smfmac_f32_32x32x32_bf16 v[48:63], v[216:219], v[24:31], v138\n"
      "ds_read_b128 v[216:219], v137 offset:52000\n"
      "v_exp_f32 v2, v2\n"
      "v_exp_f32 v3, v3\n"
      "v_exp_f32 v4, v4\n"
      "v_mfma_f32_32x32x16_bf16 v[16:31], v[104:107], v[100:103], v[80:95]\n"
      "s_waitcnt lgkmcnt(2)\n"
      "ds_read_b128 v[104:107], v133 offset:54080\n"
      "v_exp_f32 v5, v5\n"
      "v_exp_f32 v6, v6\n"
      "v_exp_f32 v7, v7\n"
      "v_exp_f32 v8, v8\n"
      "v_exp_f32 v9, v9\n"
      "v_exp_f32 v10, v10\n"
      "v_smfmac_f32_32x32x32_bf16 v[32:47], v[108:111], v[0:7], v138\n"
      "s_getpc_b64 s[36:37]\n"
      "Lpc2_%=:\n"
      "s_and_b32 s37, s37, 0xffff\n"
      "v_exp_f32 v11, v11\n"
      "v_exp_f32 v12, v12\n"
      "v_exp_f32 v13, v13\n"
      "v_exp_f32 v14, v14\n"
      "v_exp_f32 v15, v15\n"
      "v_exp_f32 v16, v16\n"
      "v_exp_f32 v17, v17\n"
      "v_smfmac_f32_32x32x32_bf16 v[32:47], v[112:115], v[8:15], v138\n"
      "s_mov_b32 s38, Lcode_%=-Lpc2_%=\n"
      "v_exp_f32 v18, v18\n"
      "v_exp_f32 v19, v19\n"
      "v_exp_f32 v20, v20\n"
      "v_mfma_f32_32x32x16_bf16 v[0:15], v[208:211], v[96:99], v[64:79]\n"
      "buffer_load_dword v250, v140, s[32:35], 0 offen\n"
      "v_exp_f32 v21, v21\n"
      "v_exp_f32 v22, v22\n"
      "v_exp_f32 v23, v23\n"
      "v_exp_f32 v24, v24\n"
      "v_exp_f32 v25, v25\n"
      "v_exp_f32 v26, v26\n"
      "v_smfmac_f32_32x32x32_bf16 v[48:63], v[108:111], v[16:23], v138\n"
      "ds_read_b128 v[108:111], v135 offset:54080\n"
      "v_exp_f32 v27, v27\n"
      "v_exp_f32 v28, v28\n"
      "v_exp_f32 v29, v29\n"
      "v_exp_f32 v30, v30\n"
      "v_exp_f32 v31, v31\n"
      "v_exp_f32 v0, v0\n"
      "v_exp_f32 v1, v1\n"
      "v_smfmac_f32_32x32x32_bf16 v[48:63], v[112:115], v[24:31], v138\n"
      "ds_read_b128 v[112:115], v137 offset:54080\n"
      "v_exp_f32 v2, v2\n"
      "v_exp_f32 v3, v3\n"
      "v_exp_f32 v4, v4\n"
      "v_mfma_f32_32x32x16_bf16 v[16:31], v[208:211], v[100:103], v[80:95]\n"
      "s_waitcnt lgkmcnt(2)\n"
      "ds_read_b128 v[208:211], v133 offset:56160\n"
      "v_exp_f32 v5, v5\n"
      "v_exp_f32 v6, v6\n"
      "v_exp_f32 v7, v7\n"
      "v_exp_f32 v8, v8\n"
      "v_exp_f32 v9, v9\n"
      "v_exp_f32 v10, v10\n"
      "v_smfmac_f32_32x32x32_bf16 v[32:47], v[212:215], v[0:7], v138\n"
      "buffer_load_dword v250, v139, s[36:39], 0 offen\n"
      "v_exp_f32 v11, v11\n"
      "v_exp_f32 v12, v12\n"
      "v_exp_f32 v13, v13\n"
      "v_exp_f32 v14, v14\n"
      "v_exp_f32 v15, v15\n"
      "v_exp_f32 v16, v16\n"
      "v_exp_f32 v17, v17\n"
      "v_smfmac_f32_32x32x32_bf16 v[32:47], v[216:219], v[8:15], v138\n"
      "v_exp_f32 v18, v18\n"
      "v_exp_f32 v19, v19\n"
      "v_exp_f32 v20, v20\n"
      "v_mfma_f32_32x32x16_bf16 v[0:15], v[104:107], v[96:99], v[64:79]\n"
      "v_exp_f32 v21, v21\n"
      "v_exp_f32 v22, v22\n"
      "v_exp_f32 v23, v23\n"
      "v_exp_f32 v24, v24\n"
      "v_exp_f32 v25, v25\n"
      "v_exp_f32 v26, v26\n"
      "v_smfmac_f32_32x32x32_bf16 v[48:63], v[212:215], v[16:23], v138\n"
      "ds_read_b128 v[212:215], v135 offset:56160\n"
      "v_exp_f32 v27, v27\n"
      "v_exp_f32 v28, v28\n"
      "v_exp_f32 v29, v29\n"
      "v_exp_f32 v30, v30\n"
      "v_exp_f32 v31, v31\n"
      "v_exp_f32 v0, v0\n"
      "v_exp_f32 v1, v1\n"
      "v_smfmac_f32_32x32x32_bf16 v[48:63], v[216:219], v[24:31], v138\n"
      "ds_read_b128 v[216:219], v137 offset:56160\n"
      "v_exp_f32 v2, v2\n"
      "v_exp_f32 v3, v3\n"
      "v_exp_f32 v4, v4\n"
      "v_mfma_f32_32x32x16_bf16 v[16:31], v[104:107], v[100:103], v[80:95]\n"
      "s_waitcnt lgkmcnt(2)\n"
      "ds_read_b128 v[104:107], v133 offset:58240\n"
      "v_exp_f32 v5, v5\n"
      "v_exp_f32 v6, v6\n"
      "v_exp_f32 v7, v7\n"
      "v_exp_f32 v8, v8\n"
      "v_exp_f32 v9, v9\n"
      "v_exp_f32 v10, v10\n"
      "v_smfmac_f32_32x32x32_bf16 v[32:47], v[108:111], v[0:7], v138\n"
      "v_exp_f32 v11, v11\n"
      "v_exp_f32 v12, v12\n"
      "v_exp_f32 v13, v13\n"
      "v_exp_f32 v14, v14\n"
      "v_exp_f32 v15, v15\n"
      "v_exp_f32 v16, v16\n"
      "v_exp_f32 v17, v17\n"
      "v_smfmac_f32_32x32x32_bf16 v[32:47], v[112:115], v[8:15], v138\n"
      "v_exp_f32 v18, v18\n"
      "v_exp_f32 v19, v19\n"
      "v_exp_f32 v20, v20\n"
      "v_mfma_f32_32x32x16_bf16 v[0:15], v[208:211], v[96:99], v[64:79]\n"
      "v_exp_f32 v21, v21\n"
      "v_exp_f32 v22, v22\n"
      "v_exp_f32 v23, v23\n"
      "v_exp_f32 v24, v24\n"
      "v_exp_f32 v25, v25\n"
      "v_exp_f32 v26, v26\n"
      "v_smfmac_f32_32x32x32_bf16 v[48:63], v[108:111], v[16:23], v138\n"
      "ds_read_b128 v[108:111], v135 offset:58240\n"
      "v_exp_f32 v27, v27\n"
      "v_exp_f32 v28, v28\n"
      "v_exp_f32 v29, v29\n"
      "v_exp_f32 v30, v30\n"
      "v_exp_f32 v31, v31\n"
      "v_exp_f32 v0, v0\n"
      "v_exp_f32 v1, v1\n"
      "v_smfmac_f32_32x32x32_bf16 v[48:63], v[112:115], v[24:31], v138\n"
      "ds_read_b128 v[112:115], v137 offset:58240\n"
      "v_exp_f32 v2, v2\n"
      "v_exp_f32 v3, v3\n"
      "v_exp_f32 v4, v4\n"
      "v_mfma_f32_32x32x16_bf16 v[16:31], v[208:211], v[100:103], v[80:95]\n"
      "s_waitcnt lgkmcnt(2)\n"
      "ds_read_b128 v[208:211], v133 offset:60320\n"
      "v_exp_f32 v5, v5\n"
      "v_exp_f32 v6, v6\n"
      "v_exp_f32 v7, v7\n"
      "v_exp_f32 v8, v8\n"
      "v_exp_f32 v9, v9\n"
      "v_exp_f32 v10, v10\n"
      "v_smfmac_f32_32x32x32_bf16 v[32:47], v[212:215], v[0:7], v138\n"
      "v_exp_f32 v11, v11\n"
      "v_exp_f32 v12, v12\n"
      "v_exp_f32 v13, v13\n"
      "v_exp_f32 v14, v14\n"
      "v_exp_f32 v15, v15\n"
      "v_exp_f32 v16, v16\n"
      "v_exp_f32 v17, v17\n"
      "v_smfmac_f32_32x32x32_bf16 v[32:47], v[216:219], v[8:15], v138\n"
      "v_exp_f32 v18, v18\n"
      "v_exp_f32 v19, v19\n"
      "v_exp_f32 v20, v20\n"
      "v_mfma_f32_32x32x16_bf16 v[0:15], v[104:107], v[96:99], v[64:79]\n"
      "v_exp_f32 v21, v21\n"
      "v_exp_f32 v22, v22\n"
      "v_exp_f32 v23, v23\n"
      "v_exp_f32 v24, v24\n"
      "v_exp_f32 v25, v25\n"
      "v_exp_f32 v26, v26\n"
      "v_smfmac_f32_32x32x32_bf16 v[48:63], v[212:215], v[16:23], v138\n"
      "ds_read_b128 v[212:215], v135 offset:60320\n"
      "v_exp_f32 v27, v27\n"
      "v_exp_f32 v28, v28\n"
      "v_exp_f32 v29, v29\n"
      "v_exp_f32 v30, v30\n"
      "v_exp_f32 v31, v31\n"
      "v_exp_f32 v0, v0\n"
      "v_exp_f32 v1, v1\n"
      "v_smfmac_f32_32x32x32_bf16 v[48:63], v[216:219], v[24:31], v138\n"
      "ds_read_b128 v[216:219], v137 offset:60320\n"
      "v_exp_f32 v2, v2\n"
      "v_exp_f32 v3, v3\n"
      "v_exp_f32 v4, v4\n"
      "v_mfma_f32_32x32x16_bf16 v[16:31], v[104:107], v[100:103], v[80:95]\n"
      "s_waitcnt lgkmcnt(2)\n"
      "ds_read_b128 v[104:107], v133 offset:62400\n"
      "v_exp_f32 v5, v5\n"
      "v_exp_f32 v6, v6\n"
      "v_exp_f32 v7, v7\n"
      "v_exp_f32 v8, v8\n"
      "v_exp_f32 v9, v9\n"
      "v_exp_f32 v10, v10\n"
      "v_smfmac_f32_32x32x32_bf16 v[32:47], v[108:111], v[0:7], v138\n"
      "Lhs1_3_%=:\n"
      "s_cbranch_execz Lhr1_%=\n"
      "v_exp_f32 v11, v11\n"
      "v_exp_f32 v12, v12\n"
      "v_exp_f32 v13, v13\n"
      "v_exp_f32 v14, v14\n"
      "v_exp_f32 v15, v15\n"
      "v_exp_f32 v16, v16\n"
      "v_exp_f32 v17, v17\n"
      "v_smfmac_f32_32x32x32_bf16 v[32:47], v[112:115], v[8:15], v138\n"
      "v_exp_f32 v18, v18\n"
      "v_exp_f32 v19, v19\n"
      "v_exp_f32 v20, v20\n"
      "v_mfma_f32_32x32x16_bf16 v[0:15], v[208:211], v[96:99], v[64:79]\n"
      "v_exp_f32 v21, v21\n"
      "v_exp_f32 v22, v22\n"
      "v_exp_f32 v23, v23\n"
      "v_exp_f32 v24, v24\n"
      "v_exp_f32 v25, v25\n"
      "v_exp_f32 v26, v26\n"
      "v_smfmac_f32_32x32x32_bf16 v[48:63], v[108:111], v[16:23], v138\n"
      "ds_read_b128 v[108:111], v135 offset:62400\n"
      "v_exp_f32 v27, v27\n"
      "v_exp_f32 v28, v28\n"
      "v_exp_f32 v29, v29\n"
      "v_exp_f32 v30, v30\n"
      "v_exp_f32 v31, v31\n"
      "v_exp_f32 v0, v0\n"
      "v_exp_f32 v1, v1\n"
      "v_smfmac_f32_32x32x32_bf16 v[48:63], v[112:115], v[24:31], v138\n"
      "ds_read_b128 v[112:115], v137 offset:62400\n"
      "v_exp_f32 v2, v2\n"
      "v_exp_f32 v3, v3\n"
      "v_exp_f32 v4, v4\n"
      "v_mfma_f32_32x32x16_bf16 v[16:31], v[208:211], v[100:103], v[80:95]\n"
      "s_waitcnt lgkmcnt(2)\n"
      "ds_read_b128 v[208:211], v133 offset:64480\n"
      "v_exp_f32 v5, v5\n"
      "v_exp_f32 v6, v6\n"
      "v_exp_f32 v7, v7\n"
      "v_exp_f32 v8, v8\n"
      "v_exp_f32 v9, v9\n"
      "v_exp_f32 v10, v10\n"
      "v_smfmac_f32_32x32x32_bf16 v[32:47], v[212:215], v[0:7], v138\n"
      "v_exp_f32 v11, v11\n"
      "v_exp_f32 v12, v12\n"
      "v_exp_f32 v13, v13\n"
      "v_exp_f32 v14, v14\n"
      "v_exp_f32 v15, v15\n"
      "v_exp_f32 v16, v16\n"
      "v_exp_f32 v17, v17\n"
      "v_smfmac_f32_32x32x32_bf16 v[32:47], v[216:219], v[8:15], v138\n"
      "v_exp_f32 v18, v18\n"
      "v_exp_f32 v19, v19\n"
      "v_exp_f32 v20, v20\n"
      "v_mfma_f32_32x32x16_bf16 v[0:15], v[104:107], v[96:99], v[64:79]\n"
      "v_exp_f32 v21, v21\n"
      "v_exp_f32 v22, v22\n"
      "v_exp_f32 v23, v23\n"
      "v_exp_f32 v24, v24\n"
      "v_exp_f32 v25, v25\n"
      "v_exp_f32 v26, v26\n"
      "v_smfmac_f32_32x32x32_bf16 v[48:63], v[212:215], v[16:23], v138\n"
      "ds_read_b128 v[212:215], v135 offset:64480\n"
      "v_exp_f32 v27, v27\n"
      "v_exp_f32 v28, v28\n"
      "v_exp_f32 v29, v29\n"
      "v_exp_f32 v30, v30\n"
      "v_exp_f32 v31, v31\n"
      "v_exp_f32 v0, v0\n"
      "v_exp_f32 v1, v1\n"
      "v_smfmac_f32_32x32x32_bf16 v[48:63], v[216:219], v[24:31], v138\n"
      "ds_read_b128 v[216:219], v137 offset:64480\n"
      "v_exp_f32 v2, v2\n"
      "v_exp_f32 v3, v3\n"
      "v_exp_f32 v4, v4\n"
      "v_mfma_f32_32x32x16_bf16 v[16:31], v[104:107], v[100:103], v[80:95]\n"
      "s_waitcnt lgkmcnt(2)\n"
      "v_exp_f32 v5, v5\n"
      "v_exp_f32 v6, v6\n"
      "v_exp_f32 v7, v7\n"
      "v_exp_f32 v8, v8\n"
      "v_exp_f32 v9, v9\n"
      "v_exp_f32 v10, v10\n"
      "v_smfmac_f32_32x32x32_bf16 v[32:47], v[108:111], v[0:7], v138\n"
      "v_exp_f32 v11, v11\n"
      "v_exp_f32 v12, v12\n"
      "v_exp_f32 v13, v13\n"
      "v_exp_f32 v14, v14\n"
      "v_exp_f32 v15, v15\n"
      "v_exp_f32 v16, v16\n"
      "v_exp_f32 v17, v17\n"
      "v_smfmac_f32_32x32x32_bf16 v[32:47], v[112:115], v[8:15], v138\n"
      "v_exp_f32 v18, v18\n"
      "v_exp_f32 v19, v19\n"
      "v_exp_f32 v20, v20\n"
      "v_mfma_f32_32x32x16_bf16 v[0:15], v[208:211], v[96:99], v[64:79]\n"
      "v_exp_f32 v21, v21\n"
      "v_exp_f32 v22, v22\n"
      "v_exp_f32 v23, v23\n"
      "v_exp_f32 v24, v24\n"
      "v_exp_f32 v25, v25\n"
      "v_exp_f32 v26, v26\n"
      "v_smfmac_f32_32x32x32_bf16 v[48:63], v[108:111], v[16:23], v138\n"
      "v_exp_f32 v27, v27\n"
      "v_exp_f32 v28, v28\n"
      "v_exp_f32 v29, v29\n"
      "v_exp_f32 v30, v30\n"
      "v_exp_f32 v31, v31\n"
      "v_exp_f32 v0, v0\n"
      "v_exp_f32 v1, v1\n"
      "v_smfmac_f32_32x32x32_bf16 v[48:63], v[112:115], v[24:31], v138\n"
      "v_exp_f32 v2, v2\n"
      "v_exp_f32 v3, v3\n"
      "v_exp_f32 v4, v4\n"
      "v_mfma_f32_32x32x16_bf16 v[16:31], v[208:211], v[100:103], v[80:95]\n"
      "s_waitcnt lgkmcnt(0)\n"
      "v_exp_f32 v5, v5\n"
      "v_exp_f32 v6, v6\n"
      "v_exp_f32 v7, v7\n"
      "v_exp_f32 v8, v8\n"
      "v_exp_f32 v9, v9\n"
      "v_exp_f32 v10, v10\n"
      "v_smfmac_f32_32x32x32_bf16 v[32:47], v[212:215], v[0:7], v138\n"
      "v_exp_f32 v11, v11\n"
      "v_exp_f32 v12, v12\n"
      "v_exp_f32 v13, v13\n"
      "v_exp_f32 v14, v14\n"
      "v_exp_f32 v15, v15\n"
      "v_exp_f32 v16, v16\n"
      "v_exp_f32 v17, v17\n"
      "v_smfmac_f32_32x32x32_bf16 v[32:47], v[216:219], v[8:15], v138\n"
      "v_exp_f32 v18, v18\n"
      "v_exp_f32 v19, v19\n"
      "v_exp_f32 v20, v20\n"
      "v_exp_f32 v21, v21\n"
      "v_exp_f32 v22, v22\n"
      "v_exp_f32 v23, v23\n"
      "v_exp_f32 v24, v24\n"
      "v_exp_f32 v25, v25\n"
      "v_exp_f32 v26, v26\n"
      "v_smfmac_f32_32x32x32_bf16 v[48:63], v[212:215], v[16:23], v138\n"
      "v_exp_f32 v27, v27\n"
      "v_exp_f32 v28, v28\n"
      "v_exp_f32 v29, v29\n"
      "v_exp_f32 v30, v30\n"
      "v_exp_f32 v31, v31\n"
      "s_nop 1\n"
      "v_smfmac_f32_32x32x32_bf16 v[48:63], v[216:219], v[24:31], v138\n"
      "s_mov_b32 s74, 0\n"
      "s_lshl2_add_u32 s63, s91, s53\n"
      "s_lshl_b32 s64, s63, 6\n"
      "s_add_u32 s64, s64, s68\n"
      "s_lshl_b32 s64, s64, 10\n"
      "s_add_u32 s64, s64, 7340032\n"
      "s_add_u32 s65, s64, 32768\n"
      "s_and_b32 s63, s68, 3\n"
      "s_lshl2_add_u32 s63, s91, s63\n"
      "s_mul_i32 s90, s63, 256\n"
      "s_lshl3_add_u32 s90, s54, s90\n"
      "s_add_u32 s90, s90, 21569536\n"
      "Lfin17_%=:\n"
      "v_mov_b32 v41, v40\n"
      "s_nop 1\n"
      "v_permlane32_swap_b32 v40, v41\n"
      "v_rcp_f32 v42, v40\n"
      "s_nop 0\n"
      "s_nop 0\n"
      "v_pk_mul_f32 v[32:33], v[32:33], v[42:43] op_sel_hi:[1,0]\n"
      "v_pk_mul_f32 v[34:35], v[34:35], v[42:43] op_sel_hi:[1,0]\n"
      "v_pk_mul_f32 v[36:37], v[36:37], v[42:43] op_sel_hi:[1,0]\n"
      "v_pk_mul_f32 v[38:39], v[38:39], v[42:43] op_sel_hi:[1,0]\n"
      "v_cvt_pk_bf16_f32 v44, v32, v33\n"
      "v_cvt_pk_bf16_f32 v45, v34, v35\n"
      "v_cvt_pk_bf16_f32 v46, v36, v37\n"
      "v_cvt_pk_bf16_f32 v47, v38, v39\n"
      "buffer_store_dwordx4 v[44:47], v254, s[8:11], s64 offen sc1\n"
      "v_mov_b32 v57, v56\n"
      "s_nop 1\n"
      "v_permlane32_swap_b32 v56, v57\n"
      "v_rcp_f32 v58, v56\n"
      "s_nop 0\n"
      "s_nop 0\n"
      "v_pk_mul_f32 v[48:49], v[48:49], v[58:59] op_sel_hi:[1,0]\n"
      "v_pk_mul_f32 v[50:51], v[50:51], v[58:59] op_sel_hi:[1,0]\n"
      "v_pk_mul_f32 v[52:53], v[52:53], v[58:59] op_sel_hi:[1,0]\n"
      "v_pk_mul_f32 v[54:55], v[54:55], v[58:59] op_sel_hi:[1,0]\n"
      "v_cvt_pk_bf16_f32 v60, v48, v49\n"
      "v_cvt_pk_bf16_f32 v61, v50, v51\n"
      "v_cvt_pk_bf16_f32 v62, v52, v53\n"
      "v_cvt_pk_bf16_f32 v63, v54, v55\n"
      "buffer_store_dwordx4 v[60:63], v254, s[8:11], s65 offen sc1\n"
      "v_mov_b32 v124, 0x207\n"
      "v_mul_f32 v43, 0x4d800000, v40\n"
      "v_mul_f32 v41, 0x61800000, v42\n"
      "v_mul_f32_e64 v43, 0, v43\n"
      "v_fma_f32 v43, 0, v41, v43\n"
      "v_fma_f32 v43, 0, v32, v43\n"
      "v_fma_f32 v43, 0, v33, v43\n"
      "v_fma_f32 v43, 0, v34, v43\n"
      "v_fma_f32 v43, 0, v35, v43\n"
      "v_fma_f32 v43, 0, v36, v43\n"
      "v_fma_f32 v43, 0, v37, v43\n"
      "v_fma_f32 v43, 0, v38, v43\n"
      "v_fma_f32 v43, 0, v39, v43\n"
      "v_mul_f32 v59, 0x4d800000, v56\n"
      "v_mul_f32 v57, 0x61800000, v58\n"
      "v_mul_f32_e64 v59, 0, v59\n"
      "v_fma_f32 v59, 0, v57, v59\n"
      "v_fma_f32 v59, 0, v48, v59\n"
      "v_fma_f32 v59, 0, v49, v59\n"
      "v_fma_f32 v59, 0, v50, v59\n"
      "v_fma_f32 v59, 0, v51, v59\n"
      "v_fma_f32 v59, 0, v52, v59\n"
      "v_fma_f32 v59, 0, v53, v59\n"
      "v_fma_f32 v59, 0, v54, v59\n"
      "v_fma_f32 v59, 0, v55, v59\n"
      "v_add_f32 v43, v43, v59\n"
      "v_cmp_class_f32 vcc, v43, v124\n"
      "s_nop 0\n"
      "s_cmp_eq_u64 vcc, 0\n"
      "s_cbranch_scc0 Lfb19_%=\n"
      "Lok18_%=:\n"
      "s_waitcnt vmcnt(8)\n"
      "v_xor_b32 v13, s56, v161\n"
      "v_bitop3_b32 v13, s57, v163, v13 bitop3:0xbe\n"
      "v_cmp_ne_u32 vcc, 0, v13\n"
      "s_nop 0\n"
      "s_cmp_eq_u64 vcc, 0\n"
      "s_cbranch_scc1 Lg22_%=\n"
      "buffer_load_dwordx4 v[160:163], v228, s[8:11], s84 offen sc1\n"
      "Lp23_%=:\n"
      "s_waitcnt vmcnt(0)\n"
      "v_xor_b32 v13, s56, v161\n"
      "v_bitop3_b32 v13, s57, v163, v13 bitop3:0xbe\n"
      "v_cmp_ne_u32 vcc, 0, v13\n"
      "s_nop 0\n"
      "s_cmp_eq_u64 vcc, 0\n"
      "s_cbranch_scc1 Lpd24_%=\n"
      "s_cmp_eq_u32 s62, 0\n"
      "s_cbranch_scc1 Lpd24_%=\n"
      "s_sub_u32 s62, s62, 1\n"
      "s_sleep 1\n"
      "buffer_load_dwordx4 v[160:163], v228, s[8:11], s84 offen sc1\n"
      "s_branch Lp23_%=\n"
      "Lpd24_%=:\n"
      "Lg22_%=:\n"
      "s_lshl_b32 s63, s53, 9\n"
      "s_add_u32 s63, s63, 133120\n"
      "v_and_b32 v12, 60, v255\n"
      "v_bfe_u32 v13, v255, 1, 1\n"
      "v_add_u32 v12, v12, v13\n"
      "v_and_b32 v13, 1, v255\n"
      "v_lshl_add_u32 v12, v13, 1, v12\n"
      "v_lshl_add_u32 v12, v12, 3, s63\n"
      "v_mov_b32 v161, v162\n"
      "ds_write_b64 v12, v[160:161]\n"
      "v_and_b32 v14, 48, v255\n"
      "v_lshl_add_u32 v14, v13, 8, v14\n"
      "v_add_u32 v14, s63, v14\n"
      "ds_read_b128 v[164:167], v14 offset:0\n"
      "ds_read_b128 v[168:171], v14 offset:64\n"
      "ds_read_b128 v[172:175], v14 offset:128\n"
      "ds_read_b128 v[176:179], v14 offset:192\n"
      "v_and_b32 v12, 15, v255\n"
      "v_lshl_add_u32 v12, v12, 3, s63\n"
      "s_waitcnt lgkmcnt(3)\n"
      "v_mfma_f32_16x16x32_bf16 v[8:11], v[164:167], v[192:195], 0\n"
      "s_waitcnt lgkmcnt(2)\n"
      "v_mfma_f32_16x16x32_bf16 v[8:11], v[168:171], v[196:199], v[8:11]\n"
      "s_waitcnt lgkmcnt(1)\n"
      "v_mfma_f32_16x16x32_bf16 v[8:11], v[172:175], v[200:203], v[8:11]\n"
      "s_waitcnt lgkmcnt(0)\n"
      "v_mfma_f32_16x16x32_bf16 v[8:11], v[176:179], v[204:207], v[8:11]\n"
      "s_waitcnt vmcnt(33)\n"
      "v_xor_b32 v13, s56, v117\n"
      "v_bitop3_b32 v13, s57, v119, v13 bitop3:0xbe\n"
      "v_cmp_ne_u32 vcc, 0, v13\n"
      "s_nop 0\n"
      "s_cmp_eq_u64 vcc, 0\n"
      "s_cbranch_scc1 Lg25_%=\n"
      "buffer_load_dwordx4 v[116:119], v145, s[8:11], s87 offen sc1\n"
      "Lp26_%=:\n"
      "s_waitcnt vmcnt(0)\n"
      "v_xor_b32 v13, s56, v117\n"
      "v_bitop3_b32 v13, s57, v119, v13 bitop3:0xbe\n"
      "v_cmp_ne_u32 vcc, 0, v13\n"
      "s_nop 0\n"
      "s_cmp_eq_u64 vcc, 0\n"
      "s_cbranch_scc1 Lpd27_%=\n"
      "s_cmp_eq_u32 s62, 0\n"
      "s_cbranch_scc1 Lpd27_%=\n"
      "s_sub_u32 s62, s62, 1\n"
      "s_sleep 1\n"
      "buffer_load_dwordx4 v[116:119], v145, s[8:11], s87 offen sc1\n"
      "s_branch Lp26_%=\n"
      "Lpd27_%=:\n"
      "Lg25_%=:\n"
      "v_and_b32 v120, 24, v255\n"
      "v_and_b32 v121, 1, v255\n"
      "v_lshl_add_u32 v120, v121, 2, v120\n"
      "v_bfe_u32 v121, v255, 1, 2\n"
      "v_add_u32 v120, v120, v121\n"
      "s_lshl_b32 s63, s53, 8\n"
      "s_add_u32 s63, s63, 132096\n"
      "v_lshl_add_u32 v120, v120, 3, s63\n"
      "v_mov_b32 v117, v118\n"
      "ds_write_b64 v120, v[116:117]\n"
      "ds_write_b64 v12, v[8:9]\n"
      "s_cmp_lg_u32 s53, 0\n"
      "s_waitcnt vmcnt(0) lgkmcnt(0)\n"
      "s_barrier\n"
      "s_cbranch_scc1 Lf28_%=\n"
      "s_mov_b64 exec, 1\n"
      "buffer_store_dwordx2 v[252:253], off, s[8:11], s90 sc1\n"
      "s_mov_b64 exec, -1\n"
      "Lf28_%=:\n"
      "s_barrier\n"
      "v_and_b32 v208, 3, v255\n"
      "v_lshlrev_b32 v208, 3, v208\n"
      "v_bfe_u32 v209, v255, 2, 2\n"
      "v_bfe_u32 v210, v255, 4, 1\n"
      "v_lshrrev_b32 v211, 5, v255\n"
      "v_lshl_add_u32 v212, v211, 2, v209\n"
      "v_lshlrev_b32 v212, 7, v212\n"
      "v_lshl_add_u32 v212, v210, 5, v212\n"
      "v_add_u32 v212, v212, v208\n"
      "v_lshrrev_b32 v213, 1, v209\n"
      "v_xor_b32 v213, s61, v213\n"
      "v_lshl_add_u32 v212, v213, 6, v212\n"
      "v_and_b32 v213, 31, v255\n"
      "v_lshlrev_b32 v214, 3, v213\n"
      "v_lshlrev_b32 v215, 4, v211\n"
      "s_lshl_b32 s63, s60, 15\n"
      "s_add_u32 s63, s63, 65536\n"
      "v_add_u32 v216, s63, v254\n"
      "s_lshl_b32 s68, s58, 16\n"
      "s_lshl_b32 s69, s53, 10\n"
      "s_add_u32 s68, s68, s69\n"
      "s_add_u32 s68, s68, 7340032\n"
      "s_lshl_b32 s63, s59, 12\n"
      "s_add_u32 s68, s68, s63\n"
      "s_and_b32 s70, s58, 28\n"
      "s_add_u32 s70, s70, s53\n"
      "s_mul_i32 s70, s70, 256\n"
      "s_add_u32 s70, s70, 21569536\n"
      "v_bfe_u32 v217, v213, 2, 1\n"
      "v_lshlrev_b32 v217, 10, v217\n"
      "v_lshl_add_u32 v217, v211, 8, v217\n"
      "v_lshrrev_b32 v218, 3, v213\n"
      "v_lshl_add_u32 v217, v218, 6, v217\n"
      "v_lshl_add_u32 v217, v208, 1, v217\n"
      "v_lshl_add_u32 v218, v211, 2, v214\n"
      "v_and_b32 v219, 0xf0, v254\n"
      "v_lshrrev_b32 v224, 4, v255\n"
      "v_min_u32 v223, 2, v224\n"
      "v_lshl_add_u32 v223, v223, 9, v219\n"
      "v_lshl_add_u32 v224, v224, 9, v219\n"
      "s_lshl1_add_u32 s63, s59, s61\n"
      "s_lshl4_add_u32 s63, s58, s63\n"
      "s_lshl1_add_u32 s63, s63, s60\n"
      "s_lshl_b32 s71, s63, 11\n"
      "s_add_u32 s71, s71, 9437184\n"
      "s_lshl_b32 s72, s63, 8\n"
      "s_add_u32 s72, s72, 98304\n"
      "s_mul_i32 s73, s59, 14\n"
      "s_add_u32 s73, s73, s61\n"
      "s_lshl_b32 s73, s73, 8\n"
      "s_add_u32 s73, s73, 65536\n"
      "s_cmp_lg_u32 s53, 3\n"
      "s_cbranch_scc1 Lv29_%=\n"
      "s_cmp_ge_u32 s58, 14\n"
      "s_cbranch_scc1 Lv29_%=\n"
      "v_lshlrev_b32 v200, 3, v255\n"
      "v_add_u32 v200, 133120, v200\n"
      "ds_read_b64 v[192:193], v200 offset:0\n"
      "ds_read_b64 v[194:195], v200 offset:512\n"
      "ds_read_b64 v[196:197], v200 offset:1024\n"
      "ds_read_b64 v[198:199], v200 offset:1536\n"
      "s_mul_i32 s63, s59, 14\n"
      "s_add_u32 s63, s63, s58\n"
      "s_lshl_b32 s63, s63, 8\n"
      "s_add_u32 s63, s63, 65536\n"
      "s_waitcnt lgkmcnt(0)\n"
      "v_pk_add_f32 v[192:193], v[192:193], v[194:195]\n"
      "v_pk_add_f32 v[196:197], v[196:197], v[198:199]\n"
      "v_pk_add_f32 v[192:193], v[192:193], v[196:197]\n"
      "v_mov_b32 v195, s57\n"
      "v_mov_b32 v194, v193\n"
      "v_mov_b32 v193, s56\n"
      "s_mov_b64 exec, 0xffff\n"
      "buffer_store_dwordx4 v[192:195], v254, s[8:11], s63 offen sc1\n"
      "s_mov_b64 exec, -1\n"
      "Lv29_%=:\n"
      "s_lshl_b32 s63, s53, 8\n"
      "s_add_u32 s63, s63, 132096\n"
      "v_lshl_add_u32 v222, v215, 1, s63\n"
      "ds_read_b128 v[160:163], v222 offset:0\n"
      "ds_read_b128 v[164:167], v222 offset:16\n"
      "ds_read_b128 v[168:171], v222 offset:128\n"
      "ds_read_b128 v[172:175], v222 offset:144\n"
      "ds_read_b64_tr_b16 v[0:1], v212 offset:0\n"
      "ds_read_b64_tr_b16 v[2:3], v212 offset:1024\n"
      "ds_read_b64_tr_b16 v[4:5], v212 offset:2048\n"
      "ds_read_b64_tr_b16 v[6:7], v212 offset:3072\n"
      "ds_read_b64_tr_b16 v[8:9], v212 offset:4096\n"
      "ds_read_b64_tr_b16 v[10:11], v212 offset:5120\n"
      "ds_read_b64_tr_b16 v[12:13], v212 offset:6144\n"
      "ds_read_b64_tr_b16 v[14:15], v212 offset:7168\n"
      "ds_read_b64_tr_b16 v[16:17], v212 offset:8192\n"
      "ds_read_b64_tr_b16 v[18:19], v212 offset:9216\n"
      "ds_read_b64_tr_b16 v[20:21], v212 offset:10240\n"
      "ds_read_b64_tr_b16 v[22:23], v212 offset:11264\n"
      "s_waitcnt lgkmcnt(12)\n"
      "v_lshlrev_b32 v128, 16, v160\n"
      "ds_read_b64_tr_b16 v[24:25], v212 offset:12288\n"
      "v_and_b32 v129, 0xffff0000, v160\n"
      "ds_read_b64_tr_b16 v[26:27], v212 offset:13312\n"
      "v_lshlrev_b32 v130, 16, v161\n"
      "ds_read_b64_tr_b16 v[28:29], v212 offset:14336\n"
      "v_and_b32 v131, 0xffff0000, v161\n"
      "ds_read_b64_tr_b16 v[30:31], v212 offset:15360\n"
      "v_lshlrev_b32 v132, 16, v162\n"
      "ds_read_b64_tr_b16 v[32:33], v212 offset:16384\n"
      "v_and_b32 v133, 0xffff0000, v162\n"
      "ds_read_b64_tr_b16 v[34:35], v212 offset:17408\n"
      "v_lshlrev_b32 v134, 16, v163\n"
      "ds_read_b64_tr_b16 v[36:37], v212 offset:18432\n"
      "v_and_b32 v135, 0xffff0000, v163\n"
      "ds_read_b64_tr_b16 v[38:39], v212 offset:19456\n"
      "v_lshlrev_b32 v136, 16, v164\n"
      "ds_read_b64_tr_b16 v[40:41], v212 offset:20480\n"
      "v_and_b32 v137, 0xffff0000, v164\n"
      "ds_read_b64_tr_b16 v[42:43], v212 offset:21504\n"
      "v_lshlrev_b32 v138, 16, v165\n"
      "ds_read_b64_tr_b16 v[44:45], v212 offset:22528\n"
      "v_and_b32 v139, 0xffff0000, v165\n"
      "ds_read_b64_tr_b16 v[46:47], v212 offset:23552\n"
      "v_lshlrev_b32 v140, 16, v166\n"
      "v_and_b32 v141, 0xffff0000, v166\n"
      "v_lshlrev_b32 v142, 16, v167\n"
      "v_and_b32 v143, 0xffff0000, v167\n"
      "v_lshlrev_b32 v144, 16, v168\n"
      "v_and_b32 v145, 0xffff0000, v168\n"
      "v_lshlrev_b32 v146, 16, v169\n"
      "v_and_b32 v147, 0xffff0000, v169\n"
      "v_lshlrev_b32 v148, 16, v170\n"
      "v_and_b32 v149, 0xffff0000, v170\n"
      "v_lshlrev_b32 v150, 16, v171\n"
      "v_and_b32 v151, 0xffff0000, v171\n"
      "v_lshlrev_b32 v152, 16, v172\n"
      "v_and_b32 v153, 0xffff0000, v172\n"
      "v_lshlrev_b32 v154, 16, v173\n"
      "v_and_b32 v155, 0xffff0000, v173\n"
      "v_lshlrev_b32 v156, 16, v174\n"
      "v_and_b32 v157, 0xffff0000, v174\n"
      "v_lshlrev_b32 v158, 16, v175\n"
      "v_and_b32 v159, 0xffff0000, v175\n"
      "buffer_load_dwordx2 v[220:221], v248, s[8:11], s70 offen sc1\n"
      "s_mul_i32 s90, s58, 256\n"
      "s_lshl3_add_u32 s90, s59, s90\n"
      "s_add_u32 s90, s90, 21577728\n"
      "Lp30_%=:\n"
      "s_waitcnt vmcnt(0)\n"
      "v_cmp_ne_u64 vcc, s[56:57], v[220:221]\n"
      "s_nop 0\n"
      "s_cmp_eq_u64 vcc, 0\n"
      "s_cbranch_scc1 Lpd31_%=\n"
      "s_cmp_eq_u32 s62, 0\n"
      "s_cbranch_scc1 Lpd31_%=\n"
      "s_sub_u32 s62, s62, 1\n"
      "s_sleep 1\n"
      "buffer_load_dwordx2 v[220:221], v248, s[8:11], s70 offen sc1\n"
      "s_branch Lp30_%=\n"
      "Lpd31_%=:\n"
      "s_add_u32 m0, s69, 65536\n"
      "s_xor_b32 s67, s68, 0\n"
      "buffer_load_dwordx4 v254, s[8:11], s67 offen sc1 lds\n"
      "ds_read_b64_tr_b16 v[48:49], v212 offset:24576\n"
      "ds_read_b64_tr_b16 v[50:51], v212 offset:25600\n"
      "ds_read_b64_tr_b16 v[52:53], v212 offset:26624\n"
      "ds_read_b64_tr_b16 v[54:55], v212 offset:27648\n"
      "ds_read_b64_tr_b16 v[56:57], v212 offset:28672\n"
      "s_add_u32 m0, s69, 98304\n"
      "s_xor_b32 s67, s68, 32768\n"
      "buffer_load_dwordx4 v254, s[8:11], s67 offen sc1 lds\n"
      "ds_read_b64_tr_b16 v[58:59], v212 offset:29696\n"
      "ds_read_b64_tr_b16 v[60:61], v212 offset:30720\n"
      "ds_read_b64_tr_b16 v[62:63], v212 offset:31744\n"
      "ds_read_b64_tr_b16 v[64:65], v212 offset:32768\n"
      "ds_read_b64_tr_b16 v[66:67], v212 offset:33792\n"
      "s_add_u32 m0, s69, 69632\n"
      "s_xor_b32 s67, s68, 4096\n"
      "buffer_load_dwordx4 v254, s[8:11], s67 offen sc1 lds\n"
      "ds_read_b64_tr_b16 v[68:69], v212 offset:34816\n"
      "ds_read_b64_tr_b16 v[70:71], v212 offset:35840\n"
      "ds_read_b64_tr_b16 v[72:73], v212 offset:36864\n"
      "ds_read_b64_tr_b16 v[74:75], v212 offset:37888\n"
      "ds_read_b64_tr_b16 v[76:77], v212 offset:38912\n"
      "s_add_u32 m0, s69, 102400\n"
      "s_xor_b32 s67, s68, 36864\n"
      "buffer_load_dwordx4 v254, s[8:11], s67 offen sc1 lds\n"
      "ds_read_b64_tr_b16 v[78:79], v212 offset:39936\n"
      "ds_read_b64_tr_b16 v[80:81], v212 offset:40960\n"
      "ds_read_b64_tr_b16 v[82:83], v212 offset:41984\n"
      "ds_read_b64_tr_b16 v[84:85], v212 offset:43008\n"
      "ds_read_b64_tr_b16 v[86:87], v212 offset:44032\n"
      "s_add_u32 m0, s69, 73728\n"
      "s_xor_b32 s67, s68, 8192\n"
      "buffer_load_dwordx4 v254, s[8:11], s67 offen sc1 lds\n"
      "ds_read_b64_tr_b16 v[88:89], v212 offset:45056\n"
      "ds_read_b64_tr_b16 v[90:91], v212 offset:46080\n"
      "ds_read_b64_tr_b16 v[92:93], v212 offset:47104\n"
      "ds_read_b64_tr_b16 v[94:95], v212 offset:48128\n"
      "ds_read_b64_tr_b16 v[96:97], v212 offset:49152\n"
      "s_add_u32 m0, s69, 106496\n"
      "s_xor_b32 s67, s68, 40960\n"
      "buffer_load_dwordx4 v254, s[8:11], s67 offen sc1 lds\n"
      "ds_read_b64_tr_b16 v[98:99], v212 offset:50176\n"
      "ds_read_b64_tr_b16 v[100:101], v212 offset:51200\n"
      "ds_read_b64_tr_b16 v[102:103], v212 offset:52224\n"
      "ds_read_b64_tr_b16 v[104:105], v212 offset:53248\n"
      "ds_read_b64_tr_b16 v[106:107], v212 offset:54272\n"
      "s_add_u32 m0, s69, 77824\n"
      "s_xor_b32 s67, s68, 12288\n"
      "buffer_load_dwordx4 v254, s[8:11], s67 offen sc1 lds\n"
      "ds_read_b64_tr_b16 v[108:109], v212 offset:55296\n"
      "ds_read_b64_tr_b16 v[110:111], v212 offset:56320\n"
      "ds_read_b64_tr_b16 v[112:113], v212 offset:57344\n"
      "ds_read_b64_tr_b16 v[114:115], v212 offset:58368\n"
      "ds_read_b64_tr_b16 v[116:117], v212 offset:59392\n"
      "s_add_u32 m0, s69, 110592\n"
      "s_xor_b32 s67, s68, 45056\n"
      "buffer_load_dwordx4 v254, s[8:11], s67 offen sc1 lds\n"
      "ds_read_b64_tr_b16 v[118:119], v212 offset:60416\n"
      "ds_read_b64_tr_b16 v[120:121], v212 offset:61440\n"
      "ds_read_b64_tr_b16 v[122:123], v212 offset:62464\n"
      "ds_read_b64_tr_b16 v[124:125], v212 offset:63488\n"
      "ds_read_b64_tr_b16 v[126:127], v212 offset:64512\n"
      "s_add_u32 m0, s69, 81920\n"
      "s_xor_b32 s67, s68, 16384\n"
      "buffer_load_dwordx4 v254, s[8:11], s67 offen sc1 lds\n"
      "s_add_u32 m0, s69, 114688\n"
      "s_xor_b32 s67, s68, 49152\n"
      "buffer_load_dwordx4 v254, s[8:11], s67 offen sc1 lds\n"
      "s_add_u32 m0, s69, 86016\n"
      "s_xor_b32 s67, s68, 20480\n"
      "buffer_load_dwordx4 v254, s[8:11], s67 offen sc1 lds\n"
      "s_add_u32 m0, s69, 118784\n"
      "s_xor_b32 s67, s68, 53248\n"
      "buffer_load_dwordx4 v254, s[8:11], s67 offen sc1 lds\n"
      "s_waitcnt lgkmcnt(0)\n"
      "s_waitcnt vmcnt(8)\n"
      "s_barrier\n"
      "ds_read_b128 v[160:163], v216 offset:0\n"
      "ds_read_b128 v[164:167], v216 offset:1024\n"
      "ds_read_b128 v[168:171], v216 offset:2048\n"
      "ds_read_b128 v[172:175], v216 offset:3072\n"
      "ds_read_b128 v[176:179], v216 offset:4096\n"
      "s_waitcnt lgkmcnt(4)\n"
      "v_mfma_f32_32x32x16_bf16 v[192:207], v[0:3], v[160:163], 0\n"
      "s_waitcnt lgkmcnt(3)\n"
      "ds_read_b128 v[180:183], v216 offset:5120\n"
      "s_add_u32 m0, s69, 90112\n"
      "s_xor_b32 s67, s68, 24576\n"
      "buffer_load_dwordx4 v254, s[8:11], s67 offen sc1 lds\n"
      "v_mfma_f32_32x32x16_bf16 v[192:207], v[4:7], v[164:167], v[192:207]\n"
      "s_waitcnt lgkmcnt(3)\n"
      "ds_read_b128 v[184:187], v216 offset:6144\n"
      "s_add_u32 m0, s69, 122880\n"
      "s_xor_b32 s67, s68, 57344\n"
      "buffer_load_dwordx4 v254, s[8:11], s67 offen sc1 lds\n"
      "v_mfma_f32_32x32x16_bf16 v[192:207], v[8:11], v[168:171], v[192:207]\n"
      "s_waitcnt lgkmcnt(3)\n"
      "ds_read_b128 v[188:191], v216 offset:7168\n"
      "s_add_u32 m0, s69, 94208\n"
      "s_xor_b32 s67, s68, 28672\n"
      "buffer_load_dwordx4 v254, s[8:11], s67 offen sc1 lds\n"
      "v_mfma_f32_32x32x16_bf16 v[192:207], v[12:15], v[172:175], v[192:207]\n"
      "s_waitcnt lgkmcnt(3)\n"
      "s_waitcnt vmcnt(5)\n"
      "s_barrier\n"
      "ds_read_b128 v[160:163], v216 offset:8192\n"
      "s_add_u32 m0, s69, 126976\n"
      "s_xor_b32 s67, s68, 61440\n"
      "buffer_load_dwordx4 v254, s[8:11], s67 offen sc1 lds\n"
      "v_mfma_f32_32x32x16_bf16 v[192:207], v[16:19], v[176:179], v[192:207]\n"
      "s_waitcnt lgkmcnt(3)\n"
      "ds_read_b128 v[164:167], v216 offset:9216\n"
      "s_add_u32 m0, s40, 0\n"
      "s_add_u32 s94, s92, 28672\n"
      "buffer_load_dwordx4 v254, s[8:11], s94 offen offset:0 sc1 lds\n"
      "v_mfma_f32_32x32x16_bf16 v[192:207], v[20:23], v[180:183], v[192:207]\n"
      "s_waitcnt lgkmcnt(3)\n"
      "ds_read_b128 v[168:171], v216 offset:10240\n"
      "v_mfma_f32_32x32x16_bf16 v[192:207], v[24:27], v[184:187], v[192:207]\n"
      "s_waitcnt lgkmcnt(3)\n"
      "ds_read_b128 v[172:175], v216 offset:11264\n"
      "buffer_load_dwordx4 v254, s[8:11], s94 offen offset:1024 sc1 lds\n"
      "v_mfma_f32_32x32x16_bf16 v[192:207], v[28:31], v[188:191], v[192:207]\n"
      "s_waitcnt lgkmcnt(3)\n"
      "ds_read_b128 v[176:179], v216 offset:12288\n"
      "v_mfma_f32_32x32x16_bf16 v[192:207], v[32:35], v[160:163], v[192:207]\n"
      "s_waitcnt lgkmcnt(3)\n"
      "ds_read_b128 v[180:183], v216 offset:13312\n"
      "buffer_load_dwordx4 v254, s[8:11], s94 offen offset:2048 sc1 lds\n"
      "v_mfma_f32_32x32x16_bf16 v[192:207], v[36:39], v[164:167], v[192:207]\n"
      "s_waitcnt lgkmcnt(3)\n"
      "ds_read_b128 v[184:187], v216 offset:14336\n"
      "v_mfma_f32_32x32x16_bf16 v[192:207], v[40:43], v[168:171], v[192:207]\n"
      "s_waitcnt lgkmcnt(3)\n"
      "ds_read_b128 v[188:191], v216 offset:15360\n"
      "buffer_load_dwordx4 v254, s[8:11], s94 offen offset:3072 sc1 lds\n"
      "v_mfma_f32_32x32x16_bf16 v[192:207], v[44:47], v[172:175], v[192:207]\n"
      "s_waitcnt lgkmcnt(3)\n"
      "ds_read_b128 v[160:163], v216 offset:16384\n"
      "v_mfma_f32_32x32x16_bf16 v[192:207], v[48:51], v[176:179], v[192:207]\n"
      "s_waitcnt lgkmcnt(3)\n"
      "ds_read_b128 v[164:167], v216 offset:17408\n"
      "s_add_u32 m0, s40, 16384\n"
      "s_add_u32 s94, s92, 57344\n"
      "buffer_load_dwordx4 v254, s[8:11], s94 offen offset:0 sc1 lds\n"
      "v_mfma_f32_32x32x16_bf16 v[192:207], v[52:55], v[180:183], v[192:207]\n"
      "s_waitcnt lgkmcnt(3)\n"
      "ds_read_b128 v[168:171], v216 offset:18432\n"
      "v_mfma_f32_32x32x16_bf16 v[192:207], v[56:59], v[184:187], v[192:207]\n"
      "s_waitcnt lgkmcnt(3)\n"
      "ds_read_b128 v[172:175], v216 offset:19456\n"
      "buffer_load_dwordx4 v254, s[8:11], s94 offen offset:1024 sc1 lds\n"
      "v_mfma_f32_32x32x16_bf16 v[192:207], v[60:63], v[188:191], v[192:207]\n"
      "s_waitcnt lgkmcnt(3)\n"
      "s_waitcnt vmcnt(6)\n"
      "s_barrier\n"
      "ds_read_b128 v[176:179], v216 offset:20480\n"
      "v_mfma_f32_32x32x16_bf16 v[192:207], v[64:67], v[160:163], v[192:207]\n"
      "s_waitcnt lgkmcnt(3)\n"
      "ds_read_b128 v[180:183], v216 offset:21504\n"
      "buffer_load_dwordx4 v254, s[8:11], s94 offen offset:2048 sc1 lds\n"
      "v_mfma_f32_32x32x16_bf16 v[192:207], v[68:71], v[164:167], v[192:207]\n"
      "s_waitcnt lgkmcnt(3)\n"
      "ds_read_b128 v[184:187], v216 offset:22528\n"
      "v_mfma_f32_32x32x16_bf16 v[192:207], v[72:75], v[168:171], v[192:207]\n"
      "s_waitcnt lgkmcnt(3)\n"
      "ds_read_b128 v[188:191], v216 offset:23552\n"
      "buffer_load_dwordx4 v254, s[8:11], s94 offen offset:3072 sc1 lds\n"
      "v_mfma_f32_32x32x16_bf16 v[192:207], v[76:79], v[172:175], v[192:207]\n"
      "s_waitcnt lgkmcnt(3)\n"
      "ds_read_b128 v[160:163], v216 offset:24576\n"
      "v_mfma_f32_32x32x16_bf16 v[192:207], v[80:83], v[176:179], v[192:207]\n"
      "s_waitcnt lgkmcnt(3)\n"
      "ds_read_b128 v[164:167], v216 offset:25600\n"
      "buffer_load_dwordx4 v[48:51], v224, s[8:11], s73 offen offset:0 sc1\n"
      "v_lshlrev_b32 v0, 16, v232\n"
      "v_and_b32 v1, 0xffff0000, v232\n"
      "v_mfma_f32_32x32x16_bf16 v[192:207], v[84:87], v[180:183], v[192:207]\n"
      "s_waitcnt lgkmcnt(3)\n"
      "ds_read_b128 v[168:171], v216 offset:26624\n"
      "v_lshlrev_b32 v2, 16, v233\n"
      "v_and_b32 v3, 0xffff0000, v233\n"
      "v_mfma_f32_32x32x16_bf16 v[192:207], v[88:91], v[184:187], v[192:207]\n"
      "s_waitcnt lgkmcnt(3)\n"
      "ds_read_b128 v[172:175], v216 offset:27648\n"
      "buffer_load_dwordx4 v[52:55], v223, s[8:11], s73 offen offset:2048 sc1\n"
      "v_lshlrev_b32 v4, 16, v234\n"
      "v_and_b32 v5, 0xffff0000, v234\n"
      "v_mfma_f32_32x32x16_bf16 v[192:207], v[92:95], v[188:191], v[192:207]\n"
      "s_waitcnt lgkmcnt(3)\n"
      "ds_read_b128 v[176:179], v216 offset:28672\n"
      "v_lshlrev_b32 v6, 16, v235\n"
      "v_and_b32 v7, 0xffff0000, v235\n"
      "v_mfma_f32_32x32x16_bf16 v[192:207], v[96:99], v[160:163], v[192:207]\n"
      "s_waitcnt lgkmcnt(3)\n"
      "ds_read_b128 v[180:183], v216 offset:29696\n"
      "v_lshlrev_b32 v8, 16, v236\n"
      "v_and_b32 v9, 0xffff0000, v236\n"
      "v_mfma_f32_32x32x16_bf16 v[192:207], v[100:103], v[164:167], v[192:207]\n"
      "s_waitcnt lgkmcnt(3)\n"
      "ds_read_b128 v[184:187], v216 offset:30720\n"
      "v_lshlrev_b32 v10, 16, v237\n"
      "v_and_b32 v11, 0xffff0000, v237\n"
      "v_mfma_f32_32x32x16_bf16 v[192:207], v[104:107], v[168:171], v[192:207]\n"
      "s_waitcnt lgkmcnt(3)\n"
      "ds_read_b128 v[188:191], v216 offset:31744\n"
      "v_lshlrev_b32 v12, 16, v238\n"
      "v_and_b32 v13, 0xffff0000, v238\n"
      "v_mfma_f32_32x32x16_bf16 v[192:207], v[108:111], v[172:175], v[192:207]\n"
      "Lhs1_4_%=:\n"
      "s_cbranch_execz Lhr1_%=\n"
      "s_waitcnt lgkmcnt(3)\n"
      "v_lshlrev_b32 v14, 16, v239\n"
      "v_and_b32 v15, 0xffff0000, v239\n"
      "v_mfma_f32_32x32x16_bf16 v[192:207], v[112:115], v[176:179], v[192:207]\n"
      "s_waitcnt lgkmcnt(2)\n"
      "v_mfma_f32_32x32x16_bf16 v[192:207], v[116:119], v[180:183], v[192:207]\n"
      "s_waitcnt lgkmcnt(1)\n"
      "v_mfma_f32_32x32x16_bf16 v[192:207], v[120:123], v[184:187], v[192:207]\n"
      "s_waitcnt lgkmcnt(0)\n"
      "v_mfma_f32_32x32x16_bf16 v[192:207], v[124:127], v[188:191], v[192:207]\n"
      "s_nop 11\n"
      "s_nop 0\n"
      "v_pk_fma_f32 v[192:193], v[192:193], v[128:129], v[0:1]\n"
      "v_pk_fma_f32 v[194:195], v[194:195], v[130:131], v[2:3]\n"
      "v_pk_fma_f32 v[196:197], v[196:197], v[132:133], v[4:5]\n"
      "v_pk_fma_f32 v[198:199], v[198:199], v[134:135], v[6:7]\n"
      "v_cvt_pk_bf16_f32 v240, v192, v193\n"
      "v_cvt_pk_bf16_f32 v241, v194, v195\n"
      "v_cvt_pk_bf16_f32 v242, v196, v197\n"
      "v_cvt_pk_bf16_f32 v243, v198, v199\n"
      "v_lshlrev_b32 v192, 16, v240\n"
      "v_and_b32 v193, 0xffff0000, v240\n"
      "v_lshlrev_b32 v194, 16, v241\n"
      "v_and_b32 v195, 0xffff0000, v241\n"
      "v_lshlrev_b32 v196, 16, v242\n"
      "v_and_b32 v197, 0xffff0000, v242\n"
      "v_lshlrev_b32 v198, 16, v243\n"
      "v_and_b32 v199, 0xffff0000, v243\n"
      "v_pk_mul_f32 v[144:145], v[192:193], v[144:145]\n"
      "v_pk_mul_f32 v[146:147], v[194:195], v[146:147]\n"
      "v_pk_mul_f32 v[148:149], v[196:197], v[148:149]\n"
      "v_pk_mul_f32 v[150:151], v[198:199], v[150:151]\n"
      "v_cvt_pk_bf16_f32 v80, v144, v145\n"
      "v_cvt_pk_bf16_f32 v81, v146, v147\n"
      "v_cvt_pk_bf16_f32 v82, v148, v149\n"
      "v_cvt_pk_bf16_f32 v83, v150, v151\n"
      "buffer_store_dwordx4 v[80:83], v217, s[8:11], s71 offen offset:0 sc1\n"
      "v_pk_fma_f32 v[200:201], v[200:201], v[136:137], v[8:9]\n"
      "v_pk_fma_f32 v[202:203], v[202:203], v[138:139], v[10:11]\n"
      "v_pk_fma_f32 v[204:205], v[204:205], v[140:141], v[12:13]\n"
      "v_pk_fma_f32 v[206:207], v[206:207], v[142:143], v[14:15]\n"
      "v_cvt_pk_bf16_f32 v244, v200, v201\n"
      "v_cvt_pk_bf16_f32 v245, v202, v203\n"
      "v_cvt_pk_bf16_f32 v246, v204, v205\n"
      "v_cvt_pk_bf16_f32 v247, v206, v207\n"
      "v_lshlrev_b32 v200, 16, v244\n"
      "v_and_b32 v201, 0xffff0000, v244\n"
      "v_lshlrev_b32 v202, 16, v245\n"
      "v_and_b32 v203, 0xffff0000, v245\n"
      "v_lshlrev_b32 v204, 16, v246\n"
      "v_and_b32 v205, 0xffff0000, v246\n"
      "v_lshlrev_b32 v206, 16, v247\n"
      "v_and_b32 v207, 0xffff0000, v247\n"
      "v_pk_mul_f32 v[152:153], v[200:201], v[152:153]\n"
      "v_pk_mul_f32 v[154:155], v[202:203], v[154:155]\n"
      "v_pk_mul_f32 v[156:157], v[204:205], v[156:157]\n"
      "v_pk_mul_f32 v[158:159], v[206:207], v[158:159]\n"
      "v_cvt_pk_bf16_f32 v84, v152, v153\n"
      "v_cvt_pk_bf16_f32 v85, v154, v155\n"
      "v_cvt_pk_bf16_f32 v86, v156, v157\n"
      "v_cvt_pk_bf16_f32 v87, v158, v159\n"
      "buffer_store_dwordx4 v[84:87], v217, s[8:11], s71 offen offset:512 sc1\n"
      "v_pk_add_f32 v[0:1], v[192:193], v[194:195]\n"
      "v_pk_mul_f32 v[2:3], v[192:193], v[192:193]\n"
      "v_pk_fma_f32 v[2:3], v[194:195], v[194:195], v[2:3]\n"
      "v_pk_add_f32 v[0:1], v[0:1], v[196:197]\n"
      "v_pk_fma_f32 v[2:3], v[196:197], v[196:197], v[2:3]\n"
      "v_pk_add_f32 v[0:1], v[0:1], v[198:199]\n"
      "v_pk_fma_f32 v[2:3], v[198:199], v[198:199], v[2:3]\n"
      "v_pk_add_f32 v[0:1], v[0:1], v[200:201]\n"
      "v_pk_fma_f32 v[2:3], v[200:201], v[200:201], v[2:3]\n"
      "v_pk_add_f32 v[0:1], v[0:1], v[202:203]\n"
      "v_pk_fma_f32 v[2:3], v[202:203], v[202:203], v[2:3]\n"
      "v_pk_add_f32 v[0:1], v[0:1], v[204:205]\n"
      "v_pk_fma_f32 v[2:3], v[204:205], v[204:205], v[2:3]\n"
      "v_pk_add_f32 v[0:1], v[0:1], v[206:207]\n"
      "v_pk_fma_f32 v[2:3], v[206:207], v[206:207], v[2:3]\n"
      "v_add_f32 v0, v0, v1\n"
      "v_add_f32 v2, v2, v3\n"
      "s_nop 1\n"
      "v_permlane32_swap_b32 v0, v2\n"
      "v_add_f32 v0, v0, v2\n"
      "buffer_store_dword v0, v218, s[8:11], s72 offen sc1\n"
      "s_or_b32 s63, s52, s53\n"
      "s_cbranch_scc1 Le32_%=\n"
      "s_mov_b64 exec, 1\n"
      "buffer_store_dwordx2 v[252:253], off, s[8:11], 0 sc1\n"
      "s_mov_b64 exec, -1\n"
      "Le32_%=:\n"
      "s_waitcnt vmcnt(3)\n"
      "v_xor_b32 v222, s56, v49\n"
      "v_bitop3_b32 v222, s56, v53, v222 bitop3:0xbe\n"
      "v_bitop3_b32 v222, s57, v51, v222 bitop3:0xbe\n"
      "v_bitop3_b32 v222, s57, v55, v222 bitop3:0xbe\n"
      "v_cmp_ne_u32 vcc, 0, v222\n"
      "s_nop 0\n"
      "s_cmp_eq_u64 vcc, 0\n"
      "s_cbranch_scc1 Lg33_%=\n"
      "buffer_load_dwordx4 v[48:51], v224, s[8:11], s73 offen offset:0 sc1\n"
      "buffer_load_dwordx4 v[52:55], v223, s[8:11], s73 offen offset:2048 sc1\n"
      "Lp34_%=:\n"
      "s_waitcnt vmcnt(0)\n"
      "v_xor_b32 v222, s56, v49\n"
      "v_bitop3_b32 v222, s56, v53, v222 bitop3:0xbe\n"
      "v_bitop3_b32 v222, s57, v51, v222 bitop3:0xbe\n"
      "v_bitop3_b32 v222, s57, v55, v222 bitop3:0xbe\n"
      "v_cmp_ne_u32 vcc, 0, v222\n"
      "s_nop 0\n"
      "s_cmp_eq_u64 vcc, 0\n"
      "s_cbranch_scc1 Lpd35_%=\n"
      "s_cmp_eq_u32 s62, 0\n"
      "s_cbranch_scc1 Lpd35_%=\n"
      "s_sub_u32 s62, s62, 1\n"
      "s_sleep 1\n"
      "buffer_load_dwordx4 v[48:51], v224, s[8:11], s73 offen offset:0 sc1\n"
      "buffer_load_dwordx4 v[52:55], v223, s[8:11], s73 offen offset:2048 sc1\n"
      "s_branch Lp34_%=\n"
      "Lpd35_%=:\n"
      "Lg33_%=:\n"
      "s_lshl_b32 s63, s61, 10\n"
      "s_add_u32 s63, s63, 133120\n"
      "v_lshl_add_u32 v56, v255, 3, s63\n"
      "v_lshrrev_b32 v57, 1, v219\n"
      "v_add_u32 v57, s63, v57\n"
      "v_mov_b32 v49, v50\n"
      "v_mov_b32 v53, v54\n"
      "ds_write_b64 v56, v[48:49]\n"
      "ds_write_b64 v56, v[52:53] offset:512\n"
      "ds_read_b64 v[140:141], v57 offset:0\n"
      "ds_read_b64 v[142:143], v57 offset:128\n"
      "ds_read_b64 v[144:145], v57 offset:256\n"
      "ds_read_b64 v[146:147], v57 offset:384\n"
      "ds_read_b64 v[148:149], v57 offset:512\n"
      "ds_read_b64 v[150:151], v57 offset:640\n"
      "ds_read_b64 v[152:153], v57 offset:768\n"
      "s_lshl_b32 s63, s61, 10\n"
      "v_add_u32 v192, s63, v254\n"
      "v_add_u32 v193, 135168, v192\n"
      "s_lshl_b32 s63, s60, 11\n"
      "s_add_u32 s63, s63, 65536\n"
      "v_add_u32 v194, s63, v254\n"
      "s_lshl_b32 s63, s53, 8\n"
      "s_add_u32 s63, s63, 131072\n"
      "v_lshl_add_u32 v195, v213, 2, s63\n"
      "v_lshrrev_b32 v201, 4, v255\n"
      "v_lshl_add_u32 v196, v201, 5, s63\n"
      "v_lshl_add_u32 v197, v211, 12, v214\n"
      "v_lshlrev_b32 v197, 1, v197\n"
      "v_lshl_add_u32 v199, v201, 9, v219\n"
      "v_lshrrev_b32 v202, 1, v219\n"
      "v_add_u32 v202, s63, v202\n"
      "v_and_b32 v200, 1, v255\n"
      "v_lshrrev_b32 v201, 2, v255\n"
      "v_lshl_add_u32 v200, v201, 1, v200\n"
      "v_lshlrev_b32 v198, 4, v200\n"
      "s_lshl_b32 s76, s58, 16\n"
      "s_lshl_b32 s77, s53, 10\n"
      "s_add_u32 s76, s76, s77\n"
      "s_add_u32 s76, s76, 9437184\n"
      "s_lshl_b32 s63, s59, 13\n"
      "s_add_u32 s76, s76, s63\n"
      "s_mul_i32 s78, s58, 256\n"
      "s_add_u32 s78, s78, 21577728\n"
      "s_lshl_b32 s79, s58, 13\n"
      "s_lshl_b32 s65, s60, 8\n"
      "s_add_u32 s79, s79, s65\n"
      "s_lshl_b32 s80, s79, 1\n"
      "s_add_u32 s79, s79, 98304\n"
      "s_add_u32 s80, s80, 360448\n"
      "s_add_u32 s81, s80, 4096\n"
      "s_lshl1_add_u32 s82, s59, s61\n"
      "s_lshl_b32 s82, s82, 10\n"
      "s_add_u32 s82, s82, s80\n"
      "s_mul_i32 s83, s59, 458752\n"
      "s_lshl1_add_u32 s64, s58, s60\n"
      "s_mul_i32 s64, s64, 7168\n"
      "s_add_u32 s83, s83, s64\n"
      "s_lshl_b32 s64, s61, 10\n"
      "s_add_u32 s83, s83, s64\n"
      "s_add_u32 s83, s83, 11534336\n"
      "s_add_u32 s84, s83, 4096\n"
      "s_lshl_b32 s64, s59, 7\n"
      "s_lshl_b32 s65, s61, 13\n"
      "s_add_u32 s64, s64, s65\n"
      "s_xor_b32 s63, s59, s60\n"
      "s_xor_b32 s96, s63, 0\n"
      "s_mul_i32 s96, s96, 114688\n"
      "s_add_u32 s96, s96, s64\n"
      "s_xor_b32 s97, s63, 2\n"
      "s_mul_i32 s97, s97, 114688\n"
      "s_add_u32 s97, s97, s64\n"
      "s_xor_b32 s98, s63, 4\n"
      "s_mul_i32 s98, s98, 114688\n"
      "s_add_u32 s98, s98, s64\n"
      "s_xor_b32 s99, s63, 6\n"
      "s_mul_i32 s99, s99, 114688\n"
      "s_add_u32 s99, s99, s64\n"
      "s_mul_i32 s85, s60, 14336\n"
      "s_lshl_b32 s65, s61, 10\n"
      "s_add_u32 s85, s85, s65\n"
      "s_mov_b32 s42, s85\n"
      "s_mov_b32 s88, 0xc0135761\n"
      "s_mov_b32 s89, 0xbdd2d3e8\n"
      "s_cmp_lg_u32 s53, 0\n"
      "s_waitcnt vmcnt(0) lgkmcnt(0)\n"
      "s_barrier\n"
      "s_cbranch_scc1 Lf36_%=\n"
      "s_mov_b64 exec, 1\n"
      "buffer_store_dwordx2 v[252:253], off, s[8:11], s90 sc1\n"
      "s_mov_b64 exec, -1\n"
      "Lf36_%=:\n"
      "s_barrier\n"
      "s_add_u32 m0, s77, 65536\n"
      "s_xor_b32 s67, s76, 0\n"
      "buffer_load_dwordx4 v254, s[8:11], s67 offen sc1 lds\n"
      "s_add_u32 m0, s77, 69632\n"
      "s_xor_b32 s67, s76, 4096\n"
      "buffer_load_dwordx4 v254, s[8:11], s67 offen sc1 lds\n"
      "s_add_u32 m0, s40, 32768\n"
      "s_add_u32 s94, s92, 86016\n"
      "buffer_load_dwordx4 v254, s[8:11], s94 offen offset:0 sc1 lds\n"
      "s_add_u32 m0, s40, 32768\n"
      "s_nop 0\n"
      "buffer_load_dwordx4 v254, s[8:11], s94 offen offset:1024 sc1 lds\n"
      "s_add_u32 m0, s40, 32768\n"
      "s_nop 0\n"
      "buffer_load_dwordx4 v254, s[8:11], s94 offen offset:2048 sc1 lds\n"
      "s_add_u32 m0, s40, 32768\n"
      "s_nop 0\n"
      "buffer_load_dwordx4 v254, s[8:11], s94 offen offset:3072 sc1 lds\n"
      "s_add_u32 m0, s40, 49152\n"
      "s_add_u32 s94, s92, 114688\n"
      "buffer_load_dwordx4 v254, s[8:11], s94 offen offset:0 sc1 lds\n"
      "s_add_u32 m0, s40, 49152\n"
      "s_nop 0\n"
      "buffer_load_dwordx4 v254, s[8:11], s94 offen offset:1024 sc1 lds\n"
      "s_add_u32 m0, s40, 49152\n"
      "s_nop 0\n"
      "buffer_load_dwordx4 v254, s[8:11], s94 offen offset:2048 sc1 lds\n"
      "s_add_u32 m0, s40, 49152\n"
      "s_nop 0\n"
      "buffer_load_dwordx4 v254, s[8:11], s94 offen offset:3072 sc1 lds\n"
      "s_mov_b64 exec, 0\n"
      "s_cmp_eq_u32 s53, 0\n"
      "s_cbranch_scc1 Lhs2_3_%=\n"
      "s_cmp_eq_u32 s53, 1\n"
      "s_cbranch_scc1 Lhs2_0_%=\n"
      "s_cmp_eq_u32 s53, 2\n"
      "s_cbranch_scc1 Lhs2_1_%=\n"
      "s_branch Lhs2_2_%=\n"
      "Lhr2_%=:\n"
      "s_mov_b64 exec, -1\n"
      "buffer_load_dwordx2 v[220:221], v248, s[8:11], s78 offen sc1\n"
      "s_lshr_b32 s63, s58, 3\n"
      "s_lshl2_add_u32 s63, s59, s63\n"
      "s_and_b32 s64, s58, 7\n"
      "s_mul_i32 s90, s63, 256\n"
      "s_lshl3_add_u32 s90, s64, s90\n"
      "s_add_u32 s90, s90, 21585920\n"
      "Lp37_%=:\n"
      "s_waitcnt vmcnt(0)\n"
      "v_cmp_ne_u64 vcc, s[56:57], v[220:221]\n"
      "s_nop 0\n"
      "s_cmp_eq_u64 vcc, 0\n"
      "s_cbranch_scc1 Lpd38_%=\n"
      "s_cmp_eq_u32 s62, 0\n"
      "s_cbranch_scc1 Lpd38_%=\n"
      "s_sub_u32 s62, s62, 1\n"
      "s_sleep 1\n"
      "buffer_load_dwordx2 v[220:221], v248, s[8:11], s78 offen sc1\n"
      "s_branch Lp37_%=\n"
      "Lpd38_%=:\n"
      "s_add_u32 m0, s77, 73728\n"
      "s_xor_b32 s67, s76, 8192\n"
      "buffer_load_dwordx4 v254, s[8:11], s67 offen sc1 lds\n"
      "s_add_u32 m0, s77, 77824\n"
      "s_xor_b32 s67, s76, 12288\n"
      "buffer_load_dwordx4 v254, s[8:11], s67 offen sc1 lds\n"
      "s_add_u32 m0, s77, 81920\n"
      "s_xor_b32 s67, s76, 16384\n"
      "buffer_load_dwordx4 v254, s[8:11], s67 offen sc1 lds\n"
      "s_add_u32 m0, s77, 86016\n"
      "s_xor_b32 s67, s76, 20480\n"
      "buffer_load_dwordx4 v254, s[8:11], s67 offen sc1 lds\n"
      "s_barrier\n"
      "ds_read_b128 v[56:59], v194 offset:0\n"
      "ds_read_b128 v[60:63], v194 offset:1024\n"
      "ds_read_b128 v[64:67], v193 offset:0\n"
      "ds_read_b128 v[68:71], v193 offset:2048\n"
      "ds_read_b128 v[72:75], v193 offset:4096\n"
      "ds_read_b128 v[76:79], v193 offset:6144\n"
      "ds_read_b128 v[80:83], v194 offset:4096\n"
      "ds_read_b128 v[84:87], v194 offset:5120\n"
      "ds_read_b128 v[88:91], v193 offset:8192\n"
      "ds_read_b128 v[92:95], v193 offset:10240\n"
      "ds_read_b128 v[96:99], v193 offset:12288\n"
      "ds_read_b128 v[100:103], v193 offset:14336\n"
      "s_add_u32 m0, s77, 90112\n"
      "s_xor_b32 s67, s76, 24576\n"
      "buffer_load_dwordx4 v254, s[8:11], s67 offen sc1 lds\n"
      "s_add_u32 m0, s77, 94208\n"
      "s_xor_b32 s67, s76, 28672\n"
      "buffer_load_dwordx4 v254, s[8:11], s67 offen sc1 lds\n"
      "s_add_u32 m0, s77, 98304\n"
      "s_xor_b32 s67, s76, 32768\n"
      "buffer_load_dwordx4 v254, s[8:11], s67 offen sc1 lds\n"
      "s_add_u32 m0, s77, 102400\n"
      "s_xor_b32 s67, s76, 36864\n"
      "buffer_load_dwordx4 v254, s[8:11], s67 offen sc1 lds\n"
      "s_waitcnt lgkmcnt(6)\n"
      "v_mfma_f32_16x16x32_bf16 v[0:3], v[56:59], v[64:67], 0\n"
      "v_mfma_f32_16x16x32_bf16 v[4:7], v[60:63], v[64:67], 0\n"
      "v_mfma_f32_16x16x32_bf16 v[8:11], v[56:59], v[68:71], 0\n"
      "v_mfma_f32_16x16x32_bf16 v[12:15], v[60:63], v[68:71], 0\n"
      "v_mfma_f32_16x16x32_bf16 v[16:19], v[56:59], v[72:75], 0\n"
      "s_add_u32 m0, s77, 106496\n"
      "s_xor_b32 s67, s76, 40960\n"
      "buffer_load_dwordx4 v254, s[8:11], s67 offen sc1 lds\n"
      "v_mfma_f32_16x16x32_bf16 v[20:23], v[60:63], v[72:75], 0\n"
      "v_mfma_f32_16x16x32_bf16 v[24:27], v[56:59], v[76:79], 0\n"
      "s_add_u32 m0, s77, 110592\n"
      "s_xor_b32 s67, s76, 45056\n"
      "buffer_load_dwordx4 v254, s[8:11], s67 offen sc1 lds\n"
      "v_mfma_f32_16x16x32_bf16 v[28:31], v[60:63], v[76:79], 0\n"
      "s_waitcnt lgkmcnt(0)\n"
      "v_mfma_f32_16x16x32_bf16 v[0:3], v[80:83], v[88:91], v[0:3]\n"
      "s_add_u32 m0, s77, 114688\n"
      "s_xor_b32 s67, s76, 49152\n"
      "buffer_load_dwordx4 v254, s[8:11], s67 offen sc1 lds\n"
      "v_mfma_f32_16x16x32_bf16 v[4:7], v[84:87], v[88:91], v[4:7]\n"
      "v_mfma_f32_16x16x32_bf16 v[8:11], v[80:83], v[92:95], v[8:11]\n"
      "s_add_u32 m0, s77, 118784\n"
      "s_xor_b32 s67, s76, 53248\n"
      "buffer_load_dwordx4 v254, s[8:11], s67 offen sc1 lds\n"
      "v_mfma_f32_16x16x32_bf16 v[12:15], v[84:87], v[92:95], v[12:15]\n"
      "v_mfma_f32_16x16x32_bf16 v[16:19], v[80:83], v[96:99], v[16:19]\n"
      "s_add_u32 m0, s77, 122880\n"
      "s_xor_b32 s67, s76, 57344\n"
      "buffer_load_dwordx4 v254, s[8:11], s67 offen sc1 lds\n"
      "v_mfma_f32_16x16x32_bf16 v[20:23], v[84:87], v[96:99], v[20:23]\n"
      "v_mfma_f32_16x16x32_bf16 v[24:27], v[80:83], v[100:103], v[24:27]\n"
      "s_add_u32 m0, s77, 126976\n"
      "s_xor_b32 s67, s76, 61440\n"
      "buffer_load_dwordx4 v254, s[8:11], s67 offen sc1 lds\n"
      "v_mfma_f32_16x16x32_bf16 v[28:31], v[84:87], v[100:103], v[28:31]\n"
      "s_waitcnt vmcnt(10)\n"
      "s_barrier\n"
      "ds_read_b128 v[104:107], v194 offset:8192\n"
      "ds_read_b128 v[108:111], v194 offset:9216\n"
      "ds_read_b128 v[112:115], v192 offset:0\n"
      "ds_read_b128 v[116:119], v192 offset:2048\n"
      "ds_read_b128 v[120:123], v192 offset:4096\n"
      "ds_read_b128 v[124:127], v192 offset:6144\n"
      "ds_read_b128 v[56:59], v194 offset:12288\n"
      "ds_read_b128 v[60:63], v194 offset:13312\n"
      "ds_read_b128 v[64:67], v192 offset:8192\n"
      "ds_read_b128 v[68:71], v192 offset:10240\n"
      "ds_read_b128 v[72:75], v192 offset:12288\n"
      "ds_read_b128 v[76:79], v192 offset:14336\n"
      "s_waitcnt lgkmcnt(6)\n"
      "v_mfma_f32_16x16x32_bf16 v[0:3], v[104:107], v[112:115], v[0:3]\n"
      "ds_read_b128 v[80:83], v194 offset:16384\n"
      "v_mfma_f32_16x16x32_bf16 v[4:7], v[108:111], v[112:115], v[4:7]\n"
      "ds_read_b128 v[84:87], v194 offset:17408\n"
      "v_mfma_f32_16x16x32_bf16 v[8:11], v[104:107], v[116:119], v[8:11]\n"
      "ds_read_b128 v[88:91], v192 offset:16384\n"
      "v_mfma_f32_16x16x32_bf16 v[12:15], v[108:111], v[116:119], v[12:15]\n"
      "s_add_u32 m0, s40, 135168\n"
      "s_add_u32 s94, s92, 143360\n"
      "buffer_load_dwordx4 v254, s[8:11], s94 offen offset:0 sc1 lds\n"
      "v_mfma_f32_16x16x32_bf16 v[16:19], v[104:107], v[120:123], v[16:19]\n"
      "ds_read_b128 v[92:95], v192 offset:18432\n"
      "v_mfma_f32_16x16x32_bf16 v[20:23], v[108:111], v[120:123], v[20:23]\n"
      "ds_read_b128 v[96:99], v192 offset:20480\n"
      "v_mfma_f32_16x16x32_bf16 v[24:27], v[104:107], v[124:127], v[24:27]\n"
      "ds_read_b128 v[100:103], v192 offset:22528\n"
      "v_mfma_f32_16x16x32_bf16 v[28:31], v[108:111], v[124:127], v[28:31]\n"
      "s_waitcnt lgkmcnt(6)\n"
      "buffer_load_dwordx4 v254, s[8:11], s94 offen offset:1024 sc1 lds\n"
      "v_mfma_f32_16x16x32_bf16 v[0:3], v[56:59], v[64:67], v[0:3]\n"
      "ds_read_b128 v[104:107], v194 offset:20480\n"
      "v_mfma_f32_16x16x32_bf16 v[4:7], v[60:63], v[64:67], v[4:7]\n"
      "ds_read_b128 v[108:111], v194 offset:21504\n"
      "v_mfma_f32_16x16x32_bf16 v[8:11], v[56:59], v[68:71], v[8:11]\n"
      "ds_read_b128 v[112:115], v192 offset:24576\n"
      "v_mfma_f32_16x16x32_bf16 v[12:15], v[60:63], v[68:71], v[12:15]\n"
      "buffer_load_dwordx4 v254, s[8:11], s94 offen offset:2048 sc1 lds\n"
      "v_mfma_f32_16x16x32_bf16 v[16:19], v[56:59], v[72:75], v[16:19]\n"
      "ds_read_b128 v[116:119], v192 offset:26624\n"
      "v_mfma_f32_16x16x32_bf16 v[20:23], v[60:63], v[72:75], v[20:23]\n"
      "ds_read_b128 v[120:123], v192 offset:28672\n"
      "v_mfma_f32_16x16x32_bf16 v[24:27], v[56:59], v[76:79], v[24:27]\n"
      "ds_read_b128 v[124:127], v192 offset:30720\n"
      "v_mfma_f32_16x16x32_bf16 v[28:31], v[60:63], v[76:79], v[28:31]\n"
      "s_waitcnt lgkmcnt(6)\n"
      "buffer_load_dwordx4 v254, s[8:11], s94 offen offset:3072 sc1 lds\n"
      "s_waitcnt vmcnt(10)\n"
      "s_barrier\n"
      "v_mfma_f32_16x16x32_bf16 v[0:3], v[80:83], v[88:91], v[0:3]\n"
      "ds_read_b128 v[56:59], v194 offset:24576\n"
      "v_mfma_f32_16x16x32_bf16 v[4:7], v[84:87], v[88:91], v[4:7]\n"
      "ds_read_b128 v[60:63], v194 offset:25600\n"
      "v_mfma_f32_16x16x32_bf16 v[8:11], v[80:83], v[92:95], v[8:11]\n"
      "ds_read_b128 v[64:67], v192 offset:32768\n"
      "v_mfma_f32_16x16x32_bf16 v[12:15], v[84:87], v[92:95], v[12:15]\n"
      "s_add_u32 m0, s40, 0\n"
      "s_add_u32 s94, s92, 172032\n"
      "buffer_load_dwordx4 v254, s[8:11], s94 offen offset:0 sc1 lds\n"
      "v_mfma_f32_16x16x32_bf16 v[16:19], v[80:83], v[96:99], v[16:19]\n"
      "ds_read_b128 v[68:71], v192 offset:34816\n"
      "v_mfma_f32_16x16x32_bf16 v[20:23], v[84:87], v[96:99], v[20:23]\n"
      "ds_read_b128 v[72:75], v192 offset:36864\n"
      "v_mfma_f32_16x16x32_bf16 v[24:27], v[80:83], v[100:103], v[24:27]\n"
      "ds_read_b128 v[76:79], v192 offset:38912\n"
      "v_mfma_f32_16x16x32_bf16 v[28:31], v[84:87], v[100:103], v[28:31]\n"
      "s_waitcnt lgkmcnt(6)\n"
      "buffer_load_dwordx4 v254, s[8:11], s94 offen offset:1024 sc1 lds\n"
      "v_mfma_f32_16x16x32_bf16 v[0:3], v[104:107], v[112:115], v[0:3]\n"
      "ds_read_b128 v[80:83], v194 offset:28672\n"
      "v_mfma_f32_16x16x32_bf16 v[4:7], v[108:111], v[112:115], v[4:7]\n"
      "ds_read_b128 v[84:87], v194 offset:29696\n"
      "v_mfma_f32_16x16x32_bf16 v[8:11], v[104:107], v[116:119], v[8:11]\n"
      "ds_read_b128 v[88:91], v192 offset:40960\n"
      "v_mfma_f32_16x16x32_bf16 v[12:15], v[108:111], v[116:119], v[12:15]\n"
      "buffer_load_dwordx4 v254, s[8:11], s94 offen offset:2048 sc1 lds\n"
      "v_mfma_f32_16x16x32_bf16 v[16:19], v[104:107], v[120:123], v[16:19]\n"
      "ds_read_b128 v[92:95], v192 offset:43008\n"
      "v_mfma_f32_16x16x32_bf16 v[20:23], v[108:111], v[120:123], v[20:23]\n"
      "ds_read_b128 v[96:99], v192 offset:45056\n"
      "v_mfma_f32_16x16x32_bf16 v[24:27], v[104:107], v[124:127], v[24:27]\n"
      "ds_read_b128 v[100:103], v192 offset:47104\n"
      "v_mfma_f32_16x16x32_bf16 v[28:31], v[108:111], v[124:127], v[28:31]\n"
      "s_waitcnt lgkmcnt(6)\n"
      "buffer_load_dwordx4 v254, s[8:11], s94 offen offset:3072 sc1 lds\n"
      "s_barrier\n"
      "v_mfma_f32_16x16x32_bf16 v[0:3], v[56:59], v[64:67], v[0:3]\n"
      "ds_read_b128 v[104:107], v194 offset:32768\n"
      "v_mfma_f32_16x16x32_bf16 v[4:7], v[60:63], v[64:67], v[4:7]\n"
      "ds_read_b128 v[108:111], v194 offset:33792\n"
      "v_mfma_f32_16x16x32_bf16 v[8:11], v[56:59], v[68:71], v[8:11]\n"
      "ds_read_b128 v[112:115], v192 offset:49152\n"
      "v_mfma_f32_16x16x32_bf16 v[12:15], v[60:63], v[68:71], v[12:15]\n"
      "s_add_u32 m0, s40, 16384\n"
      "s_add_u32 s94, s92, 200704\n"
      "buffer_load_dwordx4 v254, s[8:11], s94 offen offset:0 sc1 lds\n"
      "v_mfma_f32_16x16x32_bf16 v[16:19], v[56:59], v[72:75], v[16:19]\n"
      "ds_read_b128 v[116:119], v192 offset:51200\n"
      "v_mfma_f32_16x16x32_bf16 v[20:23], v[60:63], v[72:75], v[20:23]\n"
      "ds_read_b128 v[120:123], v192 offset:53248\n"
      "v_mfma_f32_16x16x32_bf16 v[24:27], v[56:59], v[76:79], v[24:27]\n"
      "ds_read_b128 v[124:127], v192 offset:55296\n"
      "v_mfma_f32_16x16x32_bf16 v[28:31], v[60:63], v[76:79], v[28:31]\n"
      "s_waitcnt lgkmcnt(6)\n"
      "buffer_load_dwordx4 v254, s[8:11], s94 offen offset:1024 sc1 lds\n"
      "v_mfma_f32_16x16x32_bf16 v[0:3], v[80:83], v[88:91], v[0:3]\n"
      "ds_read_b128 v[56:59], v194 offset:36864\n"
      "v_mfma_f32_16x16x32_bf16 v[4:7], v[84:87], v[88:91], v[4:7]\n"
      "ds_read_b128 v[60:63], v194 offset:37888\n"
      "v_mfma_f32_16x16x32_bf16 v[8:11], v[80:83], v[92:95], v[8:11]\n"
      "ds_read_b128 v[64:67], v192 offset:57344\n"
      "v_mfma_f32_16x16x32_bf16 v[12:15], v[84:87], v[92:95], v[12:15]\n"
      "buffer_load_dwordx4 v254, s[8:11], s94 offen offset:2048 sc1 lds\n"
      "v_mfma_f32_16x16x32_bf16 v[16:19], v[80:83], v[96:99], v[16:19]\n"
      "ds_read_b128 v[68:71], v192 offset:59392\n"
      "v_mfma_f32_16x16x32_bf16 v[20:23], v[84:87], v[96:99], v[20:23]\n"
      "ds_read_b128 v[72:75], v192 offset:61440\n"
      "v_mfma_f32_16x16x32_bf16 v[24:27], v[80:83], v[100:103], v[24:27]\n"
      "ds_read_b128 v[76:79], v192 offset:63488\n"
      "v_mfma_f32_16x16x32_bf16 v[28:31], v[84:87], v[100:103], v[28:31]\n"
      "s_waitcnt lgkmcnt(6)\n"
      "buffer_load_dwordx4 v254, s[8:11], s94 offen offset:3072 sc1 lds\n"
      "s_waitcnt vmcnt(8)\n"
      "s_barrier\n"
      "v_mfma_f32_16x16x32_bf16 v[0:3], v[104:107], v[112:115], v[0:3]\n"
      "ds_read_b128 v[80:83], v194 offset:40960\n"
      "v_mfma_f32_16x16x32_bf16 v[4:7], v[108:111], v[112:115], v[4:7]\n"
      "ds_read_b128 v[84:87], v194 offset:41984\n"
      "v_mfma_f32_16x16x32_bf16 v[8:11], v[104:107], v[116:119], v[8:11]\n"
      "ds_read_b128 v[88:91], v193 offset:0\n"
      "v_mfma_f32_16x16x32_bf16 v[12:15], v[108:111], v[116:119], v[12:15]\n"
      "buffer_load_dwordx4 v[172:175], v199, s[8:11], s79 offen offset:0 sc1\n"
      "v_mfma_f32_16x16x32_bf16 v[16:19], v[104:107], v[120:123], v[16:19]\n"
      "ds_read_b128 v[92:95], v193 offset:2048\n"
      "v_mfma_f32_16x16x32_bf16 v[20:23], v[108:111], v[120:123], v[20:23]\n"
      "ds_read_b128 v[96:99], v193 offset:4096\n"
      "v_mfma_f32_16x16x32_bf16 v[24:27], v[104:107], v[124:127], v[24:27]\n"
      "ds_read_b128 v[100:103], v193 offset:6144\n"
      "v_mfma_f32_16x16x32_bf16 v[28:31], v[108:111], v[124:127], v[28:31]\n"
      "s_waitcnt lgkmcnt(6)\n"
      "buffer_load_dwordx4 v[176:179], v199, s[8:11], s79 offen offset:2048 sc1\n"
      "v_mfma_f32_16x16x32_bf16 v[0:3], v[56:59], v[64:67], v[0:3]\n"
      "ds_read_b128 v[104:107], v194 offset:45056\n"
      "v_mfma_f32_16x16x32_bf16 v[4:7], v[60:63], v[64:67], v[4:7]\n"
      "ds_read_b128 v[108:111], v194 offset:46080\n"
      "v_mfma_f32_16x16x32_bf16 v[8:11], v[56:59], v[68:71], v[8:11]\n"
      "ds_read_b128 v[112:115], v193 offset:8192\n"
      "v_mfma_f32_16x16x32_bf16 v[12:15], v[60:63], v[68:71], v[12:15]\n"
      "s_add_u32 s79, s79, 4096\n"
      "buffer_load_dwordx4 v[180:183], v199, s[8:11], s79 offen offset:0 sc1\n"
      "v_mfma_f32_16x16x32_bf16 v[16:19], v[56:59], v[72:75], v[16:19]\n"
      "ds_read_b128 v[116:119], v193 offset:10240\n"
      "v_mfma_f32_16x16x32_bf16 v[20:23], v[60:63], v[72:75], v[20:23]\n"
      "ds_read_b128 v[120:123], v193 offset:12288\n"
      "v_mfma_f32_16x16x32_bf16 v[24:27], v[56:59], v[76:79], v[24:27]\n"
      "ds_read_b128 v[124:127], v193 offset:14336\n"
      "v_mfma_f32_16x16x32_bf16 v[28:31], v[60:63], v[76:79], v[28:31]\n"
      "s_waitcnt lgkmcnt(6)\n"
      "buffer_load_dwordx4 v[184:187], v199, s[8:11], s79 offen offset:2048 sc1\n"
      "s_waitcnt vmcnt(8)\n"
      "s_barrier\n"
      "v_mfma_f32_16x16x32_bf16 v[0:3], v[80:83], v[88:91], v[0:3]\n"
      "ds_read_b128 v[56:59], v194 offset:49152\n"
      "v_mfma_f32_16x16x32_bf16 v[4:7], v[84:87], v[88:91], v[4:7]\n"
      "ds_read_b128 v[60:63], v194 offset:50176\n"
      "v_mfma_f32_16x16x32_bf16 v[8:11], v[80:83], v[92:95], v[8:11]\n"
      "ds_read_b128 v[64:67], v192 offset:0\n"
      "v_mfma_f32_16x16x32_bf16 v[12:15], v[84:87], v[92:95], v[12:15]\n"
      "v_mfma_f32_16x16x32_bf16 v[16:19], v[80:83], v[96:99], v[16:19]\n"
      "ds_read_b128 v[68:71], v192 offset:2048\n"
      "v_mfma_f32_16x16x32_bf16 v[20:23], v[84:87], v[96:99], v[20:23]\n"
      "ds_read_b128 v[72:75], v192 offset:4096\n"
      "v_mfma_f32_16x16x32_bf16 v[24:27], v[80:83], v[100:103], v[24:27]\n"
      "ds_read_b128 v[76:79], v192 offset:6144\n"
      "v_mfma_f32_16x16x32_bf16 v[28:31], v[84:87], v[100:103], v[28:31]\n"
      "s_waitcnt lgkmcnt(6)\n"
      "v_mfma_f32_16x16x32_bf16 v[0:3], v[104:107], v[112:115], v[0:3]\n"
      "ds_read_b128 v[80:83], v194 offset:53248\n"
      "v_mfma_f32_16x16x32_bf16 v[4:7], v[108:111], v[112:115], v[4:7]\n"
      "ds_read_b128 v[84:87], v194 offset:54272\n"
      "v_mfma_f32_16x16x32_bf16 v[8:11], v[104:107], v[116:119], v[8:11]\n"
      "ds_read_b128 v[88:91], v192 offset:8192\n"
      "v_mfma_f32_16x16x32_bf16 v[12:15], v[108:111], v[116:119], v[12:15]\n"
      "v_mfma_f32_16x16x32_bf16 v[16:19], v[104:107], v[120:123], v[16:19]\n"
      "ds_read_b128 v[92:95], v192 offset:10240\n"
      "v_mfma_f32_16x16x32_bf16 v[20:23], v[108:111], v[120:123], v[20:23]\n"
      "ds_read_b128 v[96:99], v192 offset:12288\n"
      "v_mfma_f32_16x16x32_bf16 v[24:27], v[104:107], v[124:127], v[24:27]\n"
      "ds_read_b128 v[100:103], v192 offset:14336\n"
      "v_mfma_f32_16x16x32_bf16 v[28:31], v[108:111], v[124:127], v[28:31]\n"
      "s_waitcnt lgkmcnt(6)\n"
      "s_waitcnt vmcnt(4)\n"
      "s_barrier\n"
      "v_mfma_f32_16x16x32_bf16 v[0:3], v[56:59], v[64:67], v[0:3]\n"
      "ds_read_b128 v[104:107], v194 offset:57344\n"
      "v_mfma_f32_16x16x32_bf16 v[4:7], v[60:63], v[64:67], v[4:7]\n"
      "ds_read_b128 v[108:111], v194 offset:58368\n"
      "v_mfma_f32_16x16x32_bf16 v[8:11], v[56:59], v[68:71], v[8:11]\n"
      "ds_read_b128 v[112:115], v192 offset:16384\n"
      "v_mfma_f32_16x16x32_bf16 v[12:15], v[60:63], v[68:71], v[12:15]\n"
      "v_mfma_f32_16x16x32_bf16 v[16:19], v[56:59], v[72:75], v[16:19]\n"
      "ds_read_b128 v[116:119], v192 offset:18432\n"
      "v_mfma_f32_16x16x32_bf16 v[20:23], v[60:63], v[72:75], v[20:23]\n"
      "ds_read_b128 v[120:123], v192 offset:20480\n"
      "v_mfma_f32_16x16x32_bf16 v[24:27], v[56:59], v[76:79], v[24:27]\n"
      "ds_read_b128 v[124:127], v192 offset:22528\n"
      "v_mfma_f32_16x16x32_bf16 v[28:31], v[60:63], v[76:79], v[28:31]\n"
      "s_waitcnt lgkmcnt(6)\n"
      "v_mfma_f32_16x16x32_bf16 v[0:3], v[80:83], v[88:91], v[0:3]\n"
      "ds_read_b128 v[56:59], v194 offset:61440\n"
      "v_mfma_f32_16x16x32_bf16 v[4:7], v[84:87], v[88:91], v[4:7]\n"
      "ds_read_b128 v[60:63], v194 offset:62464\n"
      "v_mfma_f32_16x16x32_bf16 v[8:11], v[80:83], v[92:95], v[8:11]\n"
      "ds_read_b128 v[64:67], v192 offset:24576\n"
      "v_mfma_f32_16x16x32_bf16 v[12:15], v[84:87], v[92:95], v[12:15]\n"
      "s_waitcnt vmcnt(0)\n"
      "v_pk_add_f32 v[172:173], v[172:173], v[176:177]\n"
      "v_pk_add_f32 v[174:175], v[174:175], v[178:179]\n"
      "v_mfma_f32_16x16x32_bf16 v[16:19], v[80:83], v[96:99], v[16:19]\n"
      "ds_read_b128 v[68:71], v192 offset:26624\n"
      "v_mfma_f32_16x16x32_bf16 v[20:23], v[84:87], v[96:99], v[20:23]\n"
      "ds_read_b128 v[72:75], v192 offset:28672\n"
      "v_mfma_f32_16x16x32_bf16 v[24:27], v[80:83], v[100:103], v[24:27]\n"
      "ds_read_b128 v[76:79], v192 offset:30720\n"
      "v_mfma_f32_16x16x32_bf16 v[28:31], v[84:87], v[100:103], v[28:31]\n"
      "s_waitcnt lgkmcnt(6)\n"
      "v_pk_add_f32 v[180:181], v[180:181], v[184:185]\n"
      "v_pk_add_f32 v[182:183], v[182:183], v[186:187]\n"
      "v_pk_add_f32 v[172:173], v[172:173], v[180:181]\n"
      "v_mfma_f32_16x16x32_bf16 v[0:3], v[104:107], v[112:115], v[0:3]\n"
      "v_pk_add_f32 v[174:175], v[174:175], v[182:183]\n"
      "v_mov_b64 v[176:177], v[172:173]\n"
      "v_mov_b64 v[178:179], v[174:175]\n"
      "v_mfma_f32_16x16x32_bf16 v[4:7], v[108:111], v[112:115], v[4:7]\n"
      "v_permlane16_swap_b32 v172, v176\n"
      "v_permlane16_swap_b32 v173, v177\n"
      "v_permlane16_swap_b32 v174, v178\n"
      "v_mfma_f32_16x16x32_bf16 v[8:11], v[104:107], v[116:119], v[8:11]\n"
      "v_permlane16_swap_b32 v175, v179\n"
      "v_pk_add_f32 v[172:173], v[172:173], v[176:177]\n"
      "v_pk_add_f32 v[174:175], v[174:175], v[178:179]\n"
      "v_mfma_f32_16x16x32_bf16 v[12:15], v[108:111], v[116:119], v[12:15]\n"
      "v_mov_b64 v[176:177], v[172:173]\n"
      "v_mov_b64 v[178:179], v[174:175]\n"
      "s_nop 0\n"
      "v_permlane32_swap_b32 v172, v176\n"
      "v_mfma_f32_16x16x32_bf16 v[16:19], v[104:107], v[120:123], v[16:19]\n"
      "v_permlane32_swap_b32 v173, v177\n"
      "v_permlane32_swap_b32 v174, v178\n"
      "v_permlane32_swap_b32 v175, v179\n"
      "v_mfma_f32_16x16x32_bf16 v[20:23], v[108:111], v[120:123], v[20:23]\n"
      "v_pk_add_f32 v[172:173], v[172:173], v[176:177]\n"
      "v_pk_add_f32 v[174:175], v[174:175], v[178:179]\n"
      "v_mul_f32 v172, 0x3b000000, v172\n"
      "v_mfma_f32_16x16x32_bf16 v[24:27], v[104:107], v[124:127], v[24:27]\n"
      "Lhs2_0_%=:\n"
      "s_cbranch_execz Lhs2_4_%=\n"
      "v_mul_f32 v173, 0x3b000000, v173\n"
      "v_mul_f32 v174, 0x3b000000, v174\n"
      "v_mul_f32 v175, 0x3b000000, v175\n"
      "v_mfma_f32_16x16x32_bf16 v[28:31], v[108:111], v[124:127], v[28:31]\n"
      "s_waitcnt lgkmcnt(0)\n"
      "v_fma_f32 v173, -v172, v172, v173\n"
      "v_fma_f32 v175, -v174, v174, v175\n"
      "v_max_f32 v173, 0, v173\n"
      "v_mfma_f32_16x16x32_bf16 v[0:3], v[56:59], v[64:67], v[0:3]\n"
      "v_max_f32 v175, 0, v175\n"
      "v_add_f32 v173, 0x3727c5ac, v173\n"
      "v_add_f32 v175, 0x3727c5ac, v175\n"
      "v_mfma_f32_16x16x32_bf16 v[4:7], v[60:63], v[64:67], v[4:7]\n"
      "v_rsq_f32 v176, v173\n"
      "v_rsq_f32 v177, v175\n"
      "v_mul_f32 v178, -v172, v176\n"
      "v_mfma_f32_16x16x32_bf16 v[8:11], v[56:59], v[68:71], v[8:11]\n"
      "v_mul_f32 v179, -v174, v177\n"
      "ds_write_b64 v202, v[176:177]\n"
      "ds_write_b64 v202, v[178:179] offset:128\n"
      "v_mfma_f32_16x16x32_bf16 v[12:15], v[60:63], v[68:71], v[12:15]\n"
      "ds_read_b128 v[156:159], v196\n"
      "ds_read_b128 v[160:163], v196 offset:16\n"
      "ds_read_b128 v[164:167], v196 offset:128\n"
      "v_mfma_f32_16x16x32_bf16 v[16:19], v[56:59], v[72:75], v[16:19]\n"
      "ds_read_b128 v[168:171], v196 offset:144\n"
      "v_mfma_f32_16x16x32_bf16 v[20:23], v[60:63], v[72:75], v[20:23]\n"
      "v_mfma_f32_16x16x32_bf16 v[24:27], v[56:59], v[76:79], v[24:27]\n"
      "v_mfma_f32_16x16x32_bf16 v[28:31], v[60:63], v[76:79], v[28:31]\n"
      "s_barrier\n"
      "s_waitcnt lgkmcnt(0)\n"
      "v_pk_fma_f32 v[116:117], v[164:165], v[140:141], v[140:141] op_sel:[0,0,1] op_sel_hi:[1,0,1]\n"
      "v_pk_fma_f32 v[118:119], v[166:167], v[140:141], v[140:141] op_sel:[0,0,1] op_sel_hi:[1,0,1]\n"
      "v_pk_fma_f32 v[120:121], v[168:169], v[140:141], v[140:141] op_sel:[0,0,1] op_sel_hi:[1,0,1]\n"
      "s_add_u32 m0, s41, 28672\n"
      "s_add_u32 s94, s93, 28672\n"
      "buffer_load_dwordx4 v254, s[8:11], s94 offen offset:0 sc1 lds\n"
      "s_nop 0\n"
      "v_pk_fma_f32 v[122:123], v[170:171], v[140:141], v[140:141] op_sel:[0,0,1] op_sel_hi:[1,0,1]\n"
      "v_pk_fma_f32 v[0:1], v[0:1], v[156:157], v[116:117]\n"
      "v_pk_fma_f32 v[2:3], v[2:3], v[158:159], v[118:119]\n"
      "v_pk_fma_f32 v[4:5], v[4:5], v[160:161], v[120:121]\n"
      "v_pk_fma_f32 v[6:7], v[6:7], v[162:163], v[122:123]\n"
      "v_pk_mul_f32 v[116:117], v[0:1], v[0:1]\n"
      "v_pk_mul_f32 v[118:119], v[2:3], v[2:3]\n"
      "buffer_load_dwordx4 v254, s[8:11], s94 offen offset:1024 sc1 lds\n"
      "v_pk_mul_f32 v[120:121], v[4:5], v[4:5]\n"
      "v_pk_mul_f32 v[122:123], v[6:7], v[6:7]\n"
      "v_pk_fma_f32 v[116:117], v[116:117], s[88:89], s[88:89] op_sel:[0,1,0] op_sel_hi:[1,1,0]\n"
      "v_pk_fma_f32 v[118:119], v[118:119], s[88:89], s[88:89] op_sel:[0,1,0] op_sel_hi:[1,1,0]\n"
      "v_pk_fma_f32 v[120:121], v[120:121], s[88:89], s[88:89] op_sel:[0,1,0] op_sel_hi:[1,1,0]\n"
      "v_pk_fma_f32 v[122:123], v[122:123], s[88:89], s[88:89] op_sel:[0,1,0] op_sel_hi:[1,1,0]\n"
      "v_pk_mul_f32 v[116:117], v[116:117], v[0:1]\n"
      "buffer_load_dwordx4 v254, s[8:11], s94 offen offset:2048 sc1 lds\n"
      "v_pk_mul_f32 v[118:119], v[118:119], v[2:3]\n"
      "v_pk_mul_f32 v[120:121], v[120:121], v[4:5]\n"
      "v_pk_mul_f32 v[122:123], v[122:123], v[6:7]\n"
      "v_exp_f32 v116, v116\n"
      "v_exp_f32 v117, v117\n"
      "v_exp_f32 v118, v118\n"
      "v_exp_f32 v119, v119\n"
      "s_add_u32 m0, s41, 40960\n"
      "s_add_u32 s94, s93, 57344\n"
      "buffer_load_dwordx4 v254, s[8:11], s94 offen offset:0 sc1 lds\n"
      "v_exp_f32 v120, v120\n"
      "v_exp_f32 v121, v121\n"
      "v_exp_f32 v122, v122\n"
      "v_exp_f32 v123, v123\n"
      "v_pk_add_f32 v[116:117], v[116:117], 1.0 op_sel_hi:[1,0]\n"
      "v_pk_add_f32 v[118:119], v[118:119], 1.0 op_sel_hi:[1,0]\n"
      "v_pk_add_f32 v[120:121], v[120:121], 1.0 op_sel_hi:[1,0]\n"
      "buffer_load_dwordx4 v254, s[8:11], s94 offen offset:1024 sc1 lds\n"
      "v_pk_add_f32 v[122:123], v[122:123], 1.0 op_sel_hi:[1,0]\n"
      "v_rcp_f32 v116, v116\n"
      "v_rcp_f32 v117, v117\n"
      "v_rcp_f32 v118, v118\n"
      "v_rcp_f32 v119, v119\n"
      "v_rcp_f32 v120, v120\n"
      "v_rcp_f32 v121, v121\n"
      "buffer_load_dwordx4 v254, s[8:11], s94 offen offset:2048 sc1 lds\n"
      "v_rcp_f32 v122, v122\n"
      "v_rcp_f32 v123, v123\n"
      "v_pk_mul_f32 v[0:1], v[0:1], v[116:117]\n"
      "v_pk_mul_f32 v[2:3], v[2:3], v[118:119]\n"
      "v_pk_mul_f32 v[4:5], v[4:5], v[120:121]\n"
      "v_pk_mul_f32 v[6:7], v[6:7], v[122:123]\n"
      "v_pk_mul_f32 v[180:181], v[0:1], v[0:1]\n"
      "v_pk_mul_f32 v[182:183], v[2:3], v[2:3]\n"
      "v_pk_mul_f32 v[184:185], v[4:5], v[4:5]\n"
      "v_pk_mul_f32 v[186:187], v[6:7], v[6:7]\n"
      "v_pk_fma_f32 v[116:117], v[164:165], v[142:143], v[142:143] op_sel:[0,0,1] op_sel_hi:[1,0,1]\n"
      "v_pk_fma_f32 v[118:119], v[166:167], v[142:143], v[142:143] op_sel:[0,0,1] op_sel_hi:[1,0,1]\n"
      "v_pk_fma_f32 v[120:121], v[168:169], v[142:143], v[142:143] op_sel:[0,0,1] op_sel_hi:[1,0,1]\n"
      "s_add_u32 m0, s41, 53248\n"
      "s_add_u32 s94, s93, 86016\n"
      "buffer_load_dwordx4 v254, s[8:11], s94 offen offset:0 sc1 lds\n"
      "v_pk_fma_f32 v[122:123], v[170:171], v[142:143], v[142:143] op_sel:[0,0,1] op_sel_hi:[1,0,1]\n"
      "v_pk_fma_f32 v[8:9], v[8:9], v[156:157], v[116:117]\n"
      "v_pk_fma_f32 v[10:11], v[10:11], v[158:159], v[118:119]\n"
      "v_pk_fma_f32 v[12:13], v[12:13], v[160:161], v[120:121]\n"
      "v_pk_fma_f32 v[14:15], v[14:15], v[162:163], v[122:123]\n"
      "v_pk_mul_f32 v[116:117], v[8:9], v[8:9]\n"
      "v_pk_mul_f32 v[118:119], v[10:11], v[10:11]\n"
      "buffer_load_dwordx4 v254, s[8:11], s94 offen offset:1024 sc1 lds\n"
      "v_pk_mul_f32 v[120:121], v[12:13], v[12:13]\n"
      "v_pk_mul_f32 v[122:123], v[14:15], v[14:15]\n"
      "v_pk_fma_f32 v[116:117], v[116:117], s[88:89], s[88:89] op_sel:[0,1,0] op_sel_hi:[1,1,0]\n"
      "v_pk_fma_f32 v[118:119], v[118:119], s[88:89], s[88:89] op_sel:[0,1,0] op_sel_hi:[1,1,0]\n"
      "v_pk_fma_f32 v[120:121], v[120:121], s[88:89], s[88:89] op_sel:[0,1,0] op_sel_hi:[1,1,0]\n"
      "v_pk_fma_f32 v[122:123], v[122:123], s[88:89], s[88:89] op_sel:[0,1,0] op_sel_hi:[1,1,0]\n"
      "v_pk_mul_f32 v[116:117], v[116:117], v[8:9]\n"
      "buffer_load_dwordx4 v254, s[8:11], s94 offen offset:2048 sc1 lds\n"
      "v_pk_mul_f32 v[118:119], v[118:119], v[10:11]\n"
      "v_pk_mul_f32 v[120:121], v[120:121], v[12:13]\n"
      "v_pk_mul_f32 v[122:123], v[122:123], v[14:15]\n"
      "v_exp_f32 v116, v116\n"
      "v_exp_f32 v117, v117\n"
      "v_exp_f32 v118, v118\n"
      "v_exp_f32 v119, v119\n"
      "s_add_u32 m0, s41, 135168\n"
      "s_add_u32 s94, s93, 114688\n"
      "buffer_load_dwordx4 v254, s[8:11], s94 offen offset:0 sc1 lds\n"
      "v_exp_f32 v120, v120\n"
      "v_exp_f32 v121, v121\n"
      "v_exp_f32 v122, v122\n"
      "v_exp_f32 v123, v123\n"
      "v_pk_add_f32 v[116:117], v[116:117], 1.0 op_sel_hi:[1,0]\n"
      "v_pk_add_f32 v[118:119], v[118:119], 1.0 op_sel_hi:[1,0]\n"
      "v_pk_add_f32 v[120:121], v[120:121], 1.0 op_sel_hi:[1,0]\n"
      "buffer_load_dwordx4 v254, s[8:11], s94 offen offset:1024 sc1 lds\n"
      "v_pk_add_f32 v[122:123], v[122:123], 1.0 op_sel_hi:[1,0]\n"
      "v_rcp_f32 v116, v116\n"
      "v_rcp_f32 v117, v117\n"
      "v_rcp_f32 v118, v118\n"
      "v_rcp_f32 v119, v119\n"
      "v_rcp_f32 v120, v120\n"
      "v_rcp_f32 v121, v121\n"
      "buffer_load_dwordx4 v254, s[8:11], s94 offen offset:2048 sc1 lds\n"
      "v_rcp_f32 v122, v122\n"
      "v_rcp_f32 v123, v123\n"
      "v_pk_mul_f32 v[8:9], v[8:9], v[116:117]\n"
      "v_pk_mul_f32 v[10:11], v[10:11], v[118:119]\n"
      "v_pk_mul_f32 v[12:13], v[12:13], v[120:121]\n"
      "v_pk_mul_f32 v[14:15], v[14:15], v[122:123]\n"
      "v_pk_add_f32 v[172:173], v[0:1], v[8:9]\n"
      "v_pk_add_f32 v[174:175], v[2:3], v[10:11]\n"
      "v_pk_add_f32 v[176:177], v[4:5], v[12:13]\n"
      "v_pk_add_f32 v[178:179], v[6:7], v[14:15]\n"
      "v_pk_fma_f32 v[180:181], v[8:9], v[8:9], v[180:181]\n"
      "v_pk_fma_f32 v[182:183], v[10:11], v[10:11], v[182:183]\n"
      "v_pk_fma_f32 v[184:185], v[12:13], v[12:13], v[184:185]\n"
      "v_pk_fma_f32 v[186:187], v[14:15], v[14:15], v[186:187]\n"
      "v_pk_fma_f32 v[116:117], v[164:165], v[144:145], v[144:145] op_sel:[0,0,1] op_sel_hi:[1,0,1]\n"
      "v_pk_fma_f32 v[118:119], v[166:167], v[144:145], v[144:145] op_sel:[0,0,1] op_sel_hi:[1,0,1]\n"
      "v_pk_fma_f32 v[120:121], v[168:169], v[144:145], v[144:145] op_sel:[0,0,1] op_sel_hi:[1,0,1]\n"
      "s_add_u32 m0, s41, 0\n"
      "s_add_u32 s94, s93, 143360\n"
      "buffer_load_dwordx4 v254, s[8:11], s94 offen offset:0 sc1 lds\n"
      "s_nop 0\n"
      "v_pk_fma_f32 v[122:123], v[170:171], v[144:145], v[144:145] op_sel:[0,0,1] op_sel_hi:[1,0,1]\n"
      "v_pk_fma_f32 v[16:17], v[16:17], v[156:157], v[116:117]\n"
      "v_pk_fma_f32 v[18:19], v[18:19], v[158:159], v[118:119]\n"
      "v_pk_fma_f32 v[20:21], v[20:21], v[160:161], v[120:121]\n"
      "v_pk_fma_f32 v[22:23], v[22:23], v[162:163], v[122:123]\n"
      "v_pk_mul_f32 v[116:117], v[16:17], v[16:17]\n"
      "v_pk_mul_f32 v[118:119], v[18:19], v[18:19]\n"
      "buffer_load_dwordx4 v254, s[8:11], s94 offen offset:1024 sc1 lds\n"
      "v_pk_mul_f32 v[120:121], v[20:21], v[20:21]\n"
      "v_pk_mul_f32 v[122:123], v[22:23], v[22:23]\n"
      "v_pk_fma_f32 v[116:117], v[116:117], s[88:89], s[88:89] op_sel:[0,1,0] op_sel_hi:[1,1,0]\n"
      "v_pk_fma_f32 v[118:119], v[118:119], s[88:89], s[88:89] op_sel:[0,1,0] op_sel_hi:[1,1,0]\n"
      "v_pk_fma_f32 v[120:121], v[120:121], s[88:89], s[88:89] op_sel:[0,1,0] op_sel_hi:[1,1,0]\n"
      "v_pk_fma_f32 v[122:123], v[122:123], s[88:89], s[88:89] op_sel:[0,1,0] op_sel_hi:[1,1,0]\n"
      "v_pk_mul_f32 v[116:117], v[116:117], v[16:17]\n"
      "buffer_load_dwordx4 v254, s[8:11], s94 offen offset:2048 sc1 lds\n"
      "v_pk_mul_f32 v[118:119], v[118:119], v[18:19]\n"
      "v_pk_mul_f32 v[120:121], v[120:121], v[20:21]\n"
      "v_pk_mul_f32 v[122:123], v[122:123], v[22:23]\n"
      "v_exp_f32 v116, v116\n"
      "v_exp_f32 v117, v117\n"
      "v_exp_f32 v118, v118\n"
      "v_exp_f32 v119, v119\n"
      "s_add_u32 m0, s41, 12288\n"
      "s_add_u32 s94, s93, 172032\n"
      "buffer_load_dwordx4 v254, s[8:11], s94 offen offset:0 sc1 lds\n"
      "v_exp_f32 v120, v120\n"
      "v_exp_f32 v121, v121\n"
      "v_exp_f32 v122, v122\n"
      "v_exp_f32 v123, v123\n"
      "v_pk_add_f32 v[116:117], v[116:117], 1.0 op_sel_hi:[1,0]\n"
      "v_pk_add_f32 v[118:119], v[118:119], 1.0 op_sel_hi:[1,0]\n"
      "v_pk_add_f32 v[120:121], v[120:121], 1.0 op_sel_hi:[1,0]\n"
      "buffer_load_dwordx4 v254, s[8:11], s94 offen offset:1024 sc1 lds\n"
      "v_pk_add_f32 v[122:123], v[122:123], 1.0 op_sel_hi:[1,0]\n"
      "v_rcp_f32 v116, v116\n"
      "v_rcp_f32 v117, v117\n"
      "v_rcp_f32 v118, v118\n"
      "v_rcp_f32 v119, v119\n"
      "v_rcp_f32 v120, v120\n"
      "v_rcp_f32 v121, v121\n"
      "buffer_load_dwordx4 v254, s[8:11], s94 offen offset:2048 sc1 lds\n"
      "v_rcp_f32 v122, v122\n"
      "v_rcp_f32 v123, v123\n"
      "v_pk_mul_f32 v[16:17], v[16:17], v[116:117]\n"
      "v_pk_mul_f32 v[18:19], v[18:19], v[118:119]\n"
      "v_pk_mul_f32 v[20:21], v[20:21], v[120:121]\n"
      "v_pk_mul_f32 v[22:23], v[22:23], v[122:123]\n"
      "v_pk_add_f32 v[172:173], v[172:173], v[16:17]\n"
      "v_pk_add_f32 v[174:175], v[174:175], v[18:19]\n"
      "v_pk_add_f32 v[176:177], v[176:177], v[20:21]\n"
      "v_pk_add_f32 v[178:179], v[178:179], v[22:23]\n"
      "v_pk_fma_f32 v[180:181], v[16:17], v[16:17], v[180:181]\n"
      "v_pk_fma_f32 v[182:183], v[18:19], v[18:19], v[182:183]\n"
      "v_pk_fma_f32 v[184:185], v[20:21], v[20:21], v[184:185]\n"
      "v_pk_fma_f32 v[186:187], v[22:23], v[22:23], v[186:187]\n"
      "v_pk_fma_f32 v[116:117], v[164:165], v[146:147], v[146:147] op_sel:[0,0,1] op_sel_hi:[1,0,1]\n"
      "v_pk_fma_f32 v[118:119], v[166:167], v[146:147], v[146:147] op_sel:[0,0,1] op_sel_hi:[1,0,1]\n"
      "v_pk_fma_f32 v[120:121], v[168:169], v[146:147], v[146:147] op_sel:[0,0,1] op_sel_hi:[1,0,1]\n"
      "v_pk_fma_f32 v[122:123], v[170:171], v[146:147], v[146:147] op_sel:[0,0,1] op_sel_hi:[1,0,1]\n"
      "v_pk_fma_f32 v[24:25], v[24:25], v[156:157], v[116:117]\n"
      "v_pk_fma_f32 v[26:27], v[26:27], v[158:159], v[118:119]\n"
      "v_pk_fma_f32 v[28:29], v[28:29], v[160:161], v[120:121]\n"
      "v_pk_fma_f32 v[30:31], v[30:31], v[162:163], v[122:123]\n"
      "v_pk_mul_f32 v[116:117], v[24:25], v[24:25]\n"
      "v_pk_mul_f32 v[118:119], v[26:27], v[26:27]\n"
      "v_pk_mul_f32 v[120:121], v[28:29], v[28:29]\n"
      "v_pk_mul_f32 v[122:123], v[30:31], v[30:31]\n"
      "v_pk_fma_f32 v[116:117], v[116:117], s[88:89], s[88:89] op_sel:[0,1,0] op_sel_hi:[1,1,0]\n"
      "v_pk_fma_f32 v[118:119], v[118:119], s[88:89], s[88:89] op_sel:[0,1,0] op_sel_hi:[1,1,0]\n"
      "v_pk_fma_f32 v[120:121], v[120:121], s[88:89], s[88:89] op_sel:[0,1,0] op_sel_hi:[1,1,0]\n"
      "v_pk_fma_f32 v[122:123], v[122:123], s[88:89], s[88:89] op_sel:[0,1,0] op_sel_hi:[1,1,0]\n"
      "v_pk_mul_f32 v[116:117], v[116:117], v[24:25]\n"
      "v_pk_mul_f32 v[118:119], v[118:119], v[26:27]\n"
      "v_pk_mul_f32 v[120:121], v[120:121], v[28:29]\n"
      "v_pk_mul_f32 v[122:123], v[122:123], v[30:31]\n"
      "v_exp_f32 v116, v116\n"
      "v_exp_f32 v117, v117\n"
      "v_exp_f32 v118, v118\n"
      "v_exp_f32 v119, v119\n"
      "v_exp_f32 v120, v120\n"
      "ds_read_b128 v[56:59], v194 offset:0\n"
      "v_exp_f32 v121, v121\n"
      "v_exp_f32 v122, v122\n"
      "ds_read_b128 v[60:63], v194 offset:1024\n"
      "v_exp_f32 v123, v123\n"
      "v_pk_add_f32 v[116:117], v[116:117], 1.0 op_sel_hi:[1,0]\n"
      "ds_read_b128 v[64:67], v193 offset:16384\n"
      "v_pk_add_f32 v[118:119], v[118:119], 1.0 op_sel_hi:[1,0]\n"
      "v_pk_add_f32 v[120:121], v[120:121], 1.0 op_sel_hi:[1,0]\n"
      "ds_read_b128 v[68:71], v193 offset:18432\n"
      "v_pk_add_f32 v[122:123], v[122:123], 1.0 op_sel_hi:[1,0]\n"
      "v_rcp_f32 v116, v116\n"
      "ds_read_b128 v[72:75], v193 offset:20480\n"
      "v_rcp_f32 v117, v117\n"
      "v_rcp_f32 v118, v118\n"
      "ds_read_b128 v[76:79], v194 offset:4096\n"
      "v_rcp_f32 v119, v119\n"
      "v_rcp_f32 v120, v120\n"
      "ds_read_b128 v[80:83], v194 offset:5120\n"
      "v_rcp_f32 v121, v121\n"
      "v_rcp_f32 v122, v122\n"
      "ds_read_b128 v[84:87], v193 offset:22528\n"
      "v_rcp_f32 v123, v123\n"
      "v_pk_mul_f32 v[24:25], v[24:25], v[116:117]\n"
      "ds_read_b128 v[88:91], v193 offset:24576\n"
      "v_pk_mul_f32 v[26:27], v[26:27], v[118:119]\n"
      "v_pk_mul_f32 v[28:29], v[28:29], v[120:121]\n"
      "ds_read_b128 v[92:95], v193 offset:26624\n"
      "v_pk_mul_f32 v[30:31], v[30:31], v[122:123]\n"
      "s_cmp_lg_u32 s61, 0\n"
      "s_cbranch_scc1 Lq39_%=\n"
      "v_pk_add_f32 v[172:173], v[172:173], v[24:25]\n"
      "v_pk_add_f32 v[174:175], v[174:175], v[26:27]\n"
      "v_pk_add_f32 v[176:177], v[176:177], v[28:29]\n"
      "v_pk_add_f32 v[178:179], v[178:179], v[30:31]\n"
      "v_pk_fma_f32 v[180:181], v[24:25], v[24:25], v[180:181]\n"
      "v_pk_fma_f32 v[182:183], v[26:27], v[26:27], v[182:183]\n"
      "v_pk_fma_f32 v[184:185], v[28:29], v[28:29], v[184:185]\n"
      "v_pk_fma_f32 v[186:187], v[30:31], v[30:31], v[186:187]\n"
      "Lq39_%=:\n"
      "v_add_f32_dpp v172, v172, v172 row_ror:8 row_mask:0xf bank_mask:0x3\n"
      "v_add_f32_dpp v173, v173, v173 row_ror:8 row_mask:0xf bank_mask:0x3\n"
      "v_add_f32_dpp v174, v174, v174 row_ror:8 row_mask:0xf bank_mask:0x3\n"
      "v_add_f32_dpp v175, v175, v175 row_ror:8 row_mask:0xf bank_mask:0x3\n"
      "v_add_f32_dpp v172, v176, v176 row_ror:8 row_mask:0xf bank_mask:0xc\n"
      "v_add_f32_dpp v173, v177, v177 row_ror:8 row_mask:0xf bank_mask:0xc\n"
      "v_add_f32_dpp v174, v178, v178 row_ror:8 row_mask:0xf bank_mask:0xc\n"
      "v_add_f32_dpp v175, v179, v179 row_ror:8 row_mask:0xf bank_mask:0xc\n"
      "v_add_f32_dpp v180, v180, v180 row_ror:8 row_mask:0xf bank_mask:0x3\n"
      "v_add_f32_dpp v181, v181, v181 row_ror:8 row_mask:0xf bank_mask:0x3\n"
      "v_add_f32_dpp v182, v182, v182 row_ror:8 row_mask:0xf bank_mask:0x3\n"
      "v_add_f32_dpp v183, v183, v183 row_ror:8 row_mask:0xf bank_mask:0x3\n"
      "v_add_f32_dpp v180, v184, v184 row_ror:8 row_mask:0xf bank_mask:0xc\n"
      "v_add_f32_dpp v181, v185, v185 row_ror:8 row_mask:0xf bank_mask:0xc\n"
      "v_add_f32_dpp v182, v186, v186 row_ror:8 row_mask:0xf bank_mask:0xc\n"
      "v_add_f32_dpp v183, v187, v187 row_ror:8 row_mask:0xf bank_mask:0xc\n"
      "v_add_f32_dpp v172, v172, v172 row_half_mirror row_mask:0xf bank_mask:0x5\n"
      "v_add_f32_dpp v173, v173, v173 row_half_mirror row_mask:0xf bank_mask:0x5\n"
      "v_add_f32_dpp v180, v180, v180 row_half_mirror row_mask:0xf bank_mask:0x5\n"
      "v_add_f32_dpp v181, v181, v181 row_half_mirror row_mask:0xf bank_mask:0x5\n"
      "v_add_f32_dpp v172, v174, v174 row_half_mirror row_mask:0xf bank_mask:0xa\n"
      "v_add_f32_dpp v173, v175, v175 row_half_mirror row_mask:0xf bank_mask:0xa\n"
      "v_add_f32_dpp v180, v182, v182 row_half_mirror row_mask:0xf bank_mask:0xa\n"
      "v_add_f32_dpp v181, v183, v183 row_half_mirror row_mask:0xf bank_mask:0xa\n"
      "v_add_f32_dpp v172, v172, v172 quad_perm:[1,0,3,2] row_mask:0xf bank_mask:0xf\n"
      "v_add_f32_dpp v173, v173, v173 quad_perm:[1,0,3,2] row_mask:0xf bank_mask:0xf\n"
      "v_add_f32_dpp v180, v180, v180 quad_perm:[1,0,3,2] row_mask:0xf bank_mask:0xf\n"
      "v_add_f32_dpp v181, v181, v181 quad_perm:[1,0,3,2] row_mask:0xf bank_mask:0xf\n"
      "v_add_f32_dpp v172, v172, v172 quad_perm:[2,3,0,1] row_mask:0xf bank_mask:0xf\n"
      "v_add_f32_dpp v173, v173, v173 quad_perm:[2,3,0,1] row_mask:0xf bank_mask:0xf\n"
      "v_add_f32_dpp v180, v180, v180 quad_perm:[2,3,0,1] row_mask:0xf bank_mask:0xf\n"
      "v_add_f32_dpp v181, v181, v181 quad_perm:[2,3,0,1] row_mask:0xf bank_mask:0xf\n"
      "s_mov_b32 vcc_lo, 0xaaaaaaaa\n"
      "s_mov_b32 vcc_hi, 0xaaaaaaaa\n"
      "v_cndmask_b32 v124, v172, v173, vcc\n"
      "v_mov_b32 v125, s56\n"
      "v_cndmask_b32 v126, v180, v181, vcc\n"
      "v_mov_b32 v127, s57\n"
      "s_mov_b32 exec_lo, 0x33333333\n"
      "s_mov_b32 exec_hi, 0x33333333\n"
      "buffer_store_dwordx4 v[124:127], v198, s[8:11], s82 offen sc1\n"
      "s_mov_b64 exec, -1\n"
      "s_waitcnt vmcnt(1)\n"
      "s_barrier\n"
      "s_waitcnt lgkmcnt(5)\n"
      "v_mfma_f32_16x16x32_bf16 v[32:35], v[56:59], v[64:67], 0\n"
      "ds_read_b128 v[96:99], v194 offset:8192\n"
      "v_mfma_f32_16x16x32_bf16 v[36:39], v[60:63], v[64:67], 0\n"
      "ds_read_b128 v[100:103], v194 offset:9216\n"
      "v_mfma_f32_16x16x32_bf16 v[40:43], v[56:59], v[68:71], 0\n"
      "ds_read_b128 v[104:107], v192 offset:28672\n"
      "v_mfma_f32_16x16x32_bf16 v[44:47], v[60:63], v[68:71], 0\n"
      "ds_read_b128 v[108:111], v192 offset:30720\n"
      "v_mfma_f32_16x16x32_bf16 v[48:51], v[56:59], v[72:75], 0\n"
      "ds_read_b128 v[112:115], v192 offset:32768\n"
      "v_mfma_f32_16x16x32_bf16 v[52:55], v[60:63], v[72:75], 0\n"
      "s_waitcnt lgkmcnt(5)\n"
      "v_mfma_f32_16x16x32_bf16 v[32:35], v[76:79], v[84:87], v[32:35]\n"
      "ds_read_b128 v[56:59], v194 offset:12288\n"
      "v_mfma_f32_16x16x32_bf16 v[36:39], v[80:83], v[84:87], v[36:39]\n"
      "ds_read_b128 v[60:63], v194 offset:13312\n"
      "v_mfma_f32_16x16x32_bf16 v[40:43], v[76:79], v[88:91], v[40:43]\n"
      "ds_read_b128 v[64:67], v192 offset:34816\n"
      "v_mfma_f32_16x16x32_bf16 v[44:47], v[80:83], v[88:91], v[44:47]\n"
      "ds_read_b128 v[68:71], v192 offset:36864\n"
      "v_mfma_f32_16x16x32_bf16 v[48:51], v[76:79], v[92:95], v[48:51]\n"
      "ds_read_b128 v[72:75], v192 offset:38912\n"
      "v_mfma_f32_16x16x32_bf16 v[52:55], v[80:83], v[92:95], v[52:55]\n"
      "s_waitcnt lgkmcnt(5)\n"
      "s_barrier\n"
      "v_mfma_f32_16x16x32_bf16 v[32:35], v[96:99], v[104:107], v[32:35]\n"
      "ds_read_b128 v[76:79], v194 offset:16384\n"
      "v_mfma_f32_16x16x32_bf16 v[36:39], v[100:103], v[104:107], v[36:39]\n"
      "ds_read_b128 v[80:83], v194 offset:17408\n"
      "v_mfma_f32_16x16x32_bf16 v[40:43], v[96:99], v[108:111], v[40:43]\n"
      "ds_read_b128 v[84:87], v192 offset:40960\n"
      "v_mfma_f32_16x16x32_bf16 v[44:47], v[100:103], v[108:111], v[44:47]\n"
      "ds_read_b128 v[88:91], v192 offset:43008\n"
      "v_mfma_f32_16x16x32_bf16 v[48:51], v[96:99], v[112:115], v[48:51]\n"
      "ds_read_b128 v[92:95], v192 offset:45056\n"
      "v_mfma_f32_16x16x32_bf16 v[52:55], v[100:103], v[112:115], v[52:55]\n"
      "s_waitcnt lgkmcnt(5)\n"
      "s_add_u32 m0, s41, 151552\n"
      "s_add_u32 s94, s93, 200704\n"
      "buffer_load_dwordx4 v254, s[8:11], s94 offen offset:0 sc1 lds\n"
      "v_mfma_f32_16x16x32_bf16 v[32:35], v[56:59], v[64:67], v[32:35]\n"
      "ds_read_b128 v[96:99], v194 offset:20480\n"
      "v_mfma_f32_16x16x32_bf16 v[36:39], v[60:63], v[64:67], v[36:39]\n"
      "ds_read_b128 v[100:103], v194 offset:21504\n"
      "v_mfma_f32_16x16x32_bf16 v[40:43], v[56:59], v[68:71], v[40:43]\n"
      "ds_read_b128 v[104:107], v192 offset:47104\n"
      "v_mfma_f32_16x16x32_bf16 v[44:47], v[60:63], v[68:71], v[44:47]\n"
      "ds_read_b128 v[108:111], v192 offset:49152\n"
      "v_mfma_f32_16x16x32_bf16 v[48:51], v[56:59], v[72:75], v[48:51]\n"
      "ds_read_b128 v[112:115], v192 offset:51200\n"
      "v_mfma_f32_16x16x32_bf16 v[52:55], v[60:63], v[72:75], v[52:55]\n"
      "s_waitcnt lgkmcnt(5)\n"
      "buffer_load_dwordx4 v254, s[8:11], s94 offen offset:1024 sc1 lds\n"
      "v_mfma_f32_16x16x32_bf16 v[32:35], v[76:79], v[84:87], v[32:35]\n"
      "ds_read_b128 v[56:59], v194 offset:24576\n"
      "v_mfma_f32_16x16x32_bf16 v[36:39], v[80:83], v[84:87], v[36:39]\n"
      "ds_read_b128 v[60:63], v194 offset:25600\n"
      "v_mfma_f32_16x16x32_bf16 v[40:43], v[76:79], v[88:91], v[40:43]\n"
      "ds_read_b128 v[64:67], v192 offset:53248\n"
      "v_mfma_f32_16x16x32_bf16 v[44:47], v[80:83], v[88:91], v[44:47]\n"
      "ds_read_b128 v[68:71], v192 offset:55296\n"
      "v_mfma_f32_16x16x32_bf16 v[48:51], v[76:79], v[92:95], v[48:51]\n"
      "ds_read_b128 v[72:75], v192 offset:57344\n"
      "v_mfma_f32_16x16x32_bf16 v[52:55], v[80:83], v[92:95], v[52:55]\n"
      "s_waitcnt lgkmcnt(5)\n"
      "buffer_load_dwordx4 v254, s[8:11], s94 offen offset:2048 sc1 lds\n"
      "v_mfma_f32_16x16x32_bf16 v[32:35], v[96:99], v[104:107], v[32:35]\n"
      "ds_read_b128 v[76:79], v194 offset:28672\n"
      "v_mfma_f32_16x16x32_bf16 v[36:39], v[100:103], v[104:107], v[36:39]\n"
      "ds_read_b128 v[80:83], v194 offset:29696\n"
      "v_mfma_f32_16x16x32_bf16 v[40:43], v[96:99], v[108:111], v[40:43]\n"
      "ds_read_b128 v[84:87], v192 offset:59392\n"
      "v_mfma_f32_16x16x32_bf16 v[44:47], v[100:103], v[108:111], v[44:47]\n"
      "ds_read_b128 v[88:91], v192 offset:61440\n"
      "v_mfma_f32_16x16x32_bf16 v[48:51], v[96:99], v[112:115], v[48:51]\n"
      "ds_read_b128 v[92:95], v192 offset:63488\n"
      "v_mfma_f32_16x16x32_bf16 v[52:55], v[100:103], v[112:115], v[52:55]\n"
      "s_waitcnt lgkmcnt(5)\n"
      "v_mfma_f32_16x16x32_bf16 v[32:35], v[56:59], v[64:67], v[32:35]\n"
      "ds_read_b128 v[96:99], v194 offset:32768\n"
      "v_mfma_f32_16x16x32_bf16 v[36:39], v[60:63], v[64:67], v[36:39]\n"
      "ds_read_b128 v[100:103], v194 offset:33792\n"
      "v_mfma_f32_16x16x32_bf16 v[40:43], v[56:59], v[68:71], v[40:43]\n"
      "ds_read_b128 v[104:107], v193 offset:0\n"
      "v_mfma_f32_16x16x32_bf16 v[44:47], v[60:63], v[68:71], v[44:47]\n"
      "ds_read_b128 v[108:111], v193 offset:2048\n"
      "v_mfma_f32_16x16x32_bf16 v[48:51], v[56:59], v[72:75], v[48:51]\n"
      "ds_read_b128 v[112:115], v193 offset:4096\n"
      "v_mfma_f32_16x16x32_bf16 v[52:55], v[60:63], v[72:75], v[52:55]\n"
      "s_waitcnt lgkmcnt(5)\n"
      "buffer_load_dwordx4 v[124:127], v197, s[8:11], s80 offen offset:0 sc1\n"
      "buffer_load_dwordx4 v[128:131], v197, s[8:11], s80 offen offset:1024 sc1\n"
      "v_mfma_f32_16x16x32_bf16 v[32:35], v[76:79], v[84:87], v[32:35]\n"
      "ds_read_b128 v[56:59], v194 offset:36864\n"
      "v_mfma_f32_16x16x32_bf16 v[36:39], v[80:83], v[84:87], v[36:39]\n"
      "ds_read_b128 v[60:63], v194 offset:37888\n"
      "v_mfma_f32_16x16x32_bf16 v[40:43], v[76:79], v[88:91], v[40:43]\n"
      "ds_read_b128 v[64:67], v193 offset:6144\n"
      "v_mfma_f32_16x16x32_bf16 v[44:47], v[80:83], v[88:91], v[44:47]\n"
      "ds_read_b128 v[68:71], v193 offset:8192\n"
      "v_mfma_f32_16x16x32_bf16 v[48:51], v[76:79], v[92:95], v[48:51]\n"
      "ds_read_b128 v[72:75], v193 offset:10240\n"
      "v_mfma_f32_16x16x32_bf16 v[52:55], v[80:83], v[92:95], v[52:55]\n"
      "s_waitcnt lgkmcnt(5)\n"
      "buffer_load_dwordx4 v[132:135], v197, s[8:11], s80 offen offset:2048 sc1\n"
      "buffer_load_dwordx4 v[136:139], v197, s[8:11], s80 offen offset:3072 sc1\n"
      "v_mfma_f32_16x16x32_bf16 v[32:35], v[96:99], v[104:107], v[32:35]\n"
      "ds_read_b128 v[76:79], v194 offset:40960\n"
      "v_mfma_f32_16x16x32_bf16 v[36:39], v[100:103], v[104:107], v[36:39]\n"
      "ds_read_b128 v[80:83], v194 offset:41984\n"
      "v_mfma_f32_16x16x32_bf16 v[40:43], v[96:99], v[108:111], v[40:43]\n"
      "ds_read_b128 v[84:87], v192 offset:0\n"
      "v_mfma_f32_16x16x32_bf16 v[44:47], v[100:103], v[108:111], v[44:47]\n"
      "ds_read_b128 v[88:91], v192 offset:2048\n"
      "v_mfma_f32_16x16x32_bf16 v[48:51], v[96:99], v[112:115], v[48:51]\n"
      "ds_read_b128 v[92:95], v192 offset:4096\n"
      "v_mfma_f32_16x16x32_bf16 v[52:55], v[100:103], v[112:115], v[52:55]\n"
      "s_waitcnt lgkmcnt(5)\n"
      "buffer_load_dwordx4 v[172:175], v197, s[8:11], s81 offen offset:0 sc1\n"
      "buffer_load_dwordx4 v[176:179], v197, s[8:11], s81 offen offset:1024 sc1\n"
      "v_mfma_f32_16x16x32_bf16 v[32:35], v[56:59], v[64:67], v[32:35]\n"
      "ds_read_b128 v[96:99], v194 offset:45056\n"
      "v_mfma_f32_16x16x32_bf16 v[36:39], v[60:63], v[64:67], v[36:39]\n"
      "ds_read_b128 v[100:103], v194 offset:46080\n"
      "v_mfma_f32_16x16x32_bf16 v[40:43], v[56:59], v[68:71], v[40:43]\n"
      "ds_read_b128 v[104:107], v192 offset:6144\n"
      "v_mfma_f32_16x16x32_bf16 v[44:47], v[60:63], v[68:71], v[44:47]\n"
      "ds_read_b128 v[108:111], v192 offset:8192\n"
      "v_mfma_f32_16x16x32_bf16 v[48:51], v[56:59], v[72:75], v[48:51]\n"
      "ds_read_b128 v[112:115], v192 offset:10240\n"
      "v_mfma_f32_16x16x32_bf16 v[52:55], v[60:63], v[72:75], v[52:55]\n"
      "s_waitcnt lgkmcnt(5)\n"
      "buffer_load_dwordx4 v[180:183], v197, s[8:11], s81 offen offset:2048 sc1\n"
      "buffer_load_dwordx4 v[184:187], v197, s[8:11], s81 offen offset:3072 sc1\n"
      "s_waitcnt vmcnt(8)\n"
      "s_barrier\n"
      "v_mfma_f32_16x16x32_bf16 v[32:35], v[76:79], v[84:87], v[32:35]\n"
      "s_add_u32 m0, s85, 57344\n"
      "ds_read_b128 v[56:59], v194 offset:49152\n"
      "v_mfma_f32_16x16x32_bf16 v[36:39], v[80:83], v[84:87], v[36:39]\n"
      "s_add_u32 s67, s98, 0\n"
      "ds_read_b128 v[60:63], v194 offset:50176\n"
      "v_mfma_f32_16x16x32_bf16 v[40:43], v[76:79], v[88:91], v[40:43]\n"
      "ds_read_b128 v[64:67], v192 offset:12288\n"
      "buffer_load_dwordx4 v249, s[32:35], s67 offen lds\n"
      "v_mfma_f32_16x16x32_bf16 v[44:47], v[80:83], v[88:91], v[44:47]\n"
      "ds_read_b128 v[68:71], v192 offset:14336\n"
      "v_mfma_f32_16x16x32_bf16 v[48:51], v[76:79], v[92:95], v[48:51]\n"
      "s_add_u32 m0, s85, 59392\n"
      "ds_read_b128 v[72:75], v192 offset:16384\n"
      "v_mfma_f32_16x16x32_bf16 v[52:55], v[80:83], v[92:95], v[52:55]\n"
      "s_waitcnt lgkmcnt(5)\n"
      "v_mfma_f32_16x16x32_bf16 v[32:35], v[96:99], v[104:107], v[32:35]\n"
      "s_add_u32 s67, s98, 16384\n"
      "ds_read_b128 v[76:79], v194 offset:53248\n"
      "v_mfma_f32_16x16x32_bf16 v[36:39], v[100:103], v[104:107], v[36:39]\n"
      "ds_read_b128 v[80:83], v194 offset:54272\n"
      "buffer_load_dwordx4 v249, s[32:35], s67 offen lds\n"
      "v_mfma_f32_16x16x32_bf16 v[40:43], v[96:99], v[108:111], v[40:43]\n"
      "ds_read_b128 v[84:87], v192 offset:18432\n"
      "v_mfma_f32_16x16x32_bf16 v[44:47], v[100:103], v[108:111], v[44:47]\n"
      "s_add_u32 m0, s85, 61440\n"
      "ds_read_b128 v[88:91], v192 offset:20480\n"
      "v_mfma_f32_16x16x32_bf16 v[48:51], v[96:99], v[112:115], v[48:51]\n"
      "s_add_u32 s67, s98, 32768\n"
      "ds_read_b128 v[92:95], v192 offset:22528\n"
      "v_mfma_f32_16x16x32_bf16 v[52:55], v[100:103], v[112:115], v[52:55]\n"
      "s_waitcnt lgkmcnt(5)\n"
      "buffer_load_dwordx4 v249, s[32:35], s67 offen lds\n"
      "v_mfma_f32_16x16x32_bf16 v[32:35], v[56:59], v[64:67], v[32:35]\n"
      "ds_read_b128 v[96:99], v194 offset:57344\n"
      "v_mfma_f32_16x16x32_bf16 v[36:39], v[60:63], v[64:67], v[36:39]\n"
      "s_add_u32 m0, s85, 63488\n"
      "ds_read_b128 v[100:103], v194 offset:58368\n"
      "v_mfma_f32_16x16x32_bf16 v[40:43], v[56:59], v[68:71], v[40:43]\n"
      "s_add_u32 s67, s98, 49152\n"
      "ds_read_b128 v[104:107], v193 offset:16384\n"
      "v_mfma_f32_16x16x32_bf16 v[44:47], v[60:63], v[68:71], v[44:47]\n"
      "ds_read_b128 v[108:111], v193 offset:18432\n"
      "buffer_load_dwordx4 v249, s[32:35], s67 offen lds\n"
      "v_mfma_f32_16x16x32_bf16 v[48:51], v[56:59], v[72:75], v[48:51]\n"
      "ds_read_b128 v[112:115], v193 offset:20480\n"
      "v_mfma_f32_16x16x32_bf16 v[52:55], v[60:63], v[72:75], v[52:55]\n"
      "s_waitcnt lgkmcnt(5)\n"
      "v_mfma_f32_16x16x32_bf16 v[32:35], v[76:79], v[84:87], v[32:35]\n"
      "s_add_u32 m0, s85, 65536\n"
      "ds_read_b128 v[56:59], v194 offset:61440\n"
      "v_mfma_f32_16x16x32_bf16 v[36:39], v[80:83], v[84:87], v[36:39]\n"
      "s_add_u32 s67, s98, 65536\n"
      "ds_read_b128 v[60:63], v194 offset:62464\n"
      "v_mfma_f32_16x16x32_bf16 v[40:43], v[76:79], v[88:91], v[40:43]\n"
      "ds_read_b128 v[64:67], v193 offset:22528\n"
      "buffer_load_dwordx4 v249, s[32:35], s67 offen lds\n"
      "v_mfma_f32_16x16x32_bf16 v[44:47], v[80:83], v[88:91], v[44:47]\n"
      "ds_read_b128 v[68:71], v193 offset:24576\n"
      "v_mfma_f32_16x16x32_bf16 v[48:51], v[76:79], v[92:95], v[48:51]\n"
      "ds_read_b128 v[72:75], v193 offset:26624\n"
      "v_mfma_f32_16x16x32_bf16 v[52:55], v[80:83], v[92:95], v[52:55]\n"
      "s_waitcnt lgkmcnt(5)\n"
      "v_mfma_f32_16x16x32_bf16 v[32:35], v[96:99], v[104:107], v[32:35]\n"
      "s_waitcnt vmcnt(5)\n"
      "v_xor_b32 v200, s56, v125\n"
      "v_bitop3_b32 v200, s56, v129, v200 bitop3:0xbe\n"
      "v_mfma_f32_16x16x32_bf16 v[36:39], v[100:103], v[104:107], v[36:39]\n"
      "v_bitop3_b32 v200, s56, v133, v200 bitop3:0xbe\n"
      "v_bitop3_b32 v200, s56, v137, v200 bitop3:0xbe\n"
      "v_bitop3_b32 v200, s56, v173, v200 bitop3:0xbe\n"
      "v_mfma_f32_16x16x32_bf16 v[40:43], v[96:99], v[108:111], v[40:43]\n"
      "v_bitop3_b32 v200, s56, v177, v200 bitop3:0xbe\n"
      "v_bitop3_b32 v200, s56, v181, v200 bitop3:0xbe\n"
      "v_bitop3_b32 v200, s56, v185, v200 bitop3:0xbe\n"
      "v_mfma_f32_16x16x32_bf16 v[44:47], v[100:103], v[108:111], v[44:47]\n"
      "v_bitop3_b32 v200, s57, v127, v200 bitop3:0xbe\n"
      "v_bitop3_b32 v200, s57, v131, v200 bitop3:0xbe\n"
      "v_bitop3_b32 v200, s57, v135, v200 bitop3:0xbe\n"
      "v_mfma_f32_16x16x32_bf16 v[48:51], v[96:99], v[112:115], v[48:51]\n"
      "v_bitop3_b32 v200, s57, v139, v200 bitop3:0xbe\n"
      "v_bitop3_b32 v200, s57, v175, v200 bitop3:0xbe\n"
      "v_bitop3_b32 v200, s57, v179, v200 bitop3:0xbe\n"
      "v_mfma_f32_16x16x32_bf16 v[52:55], v[100:103], v[112:115], v[52:55]\n"
      "s_waitcnt lgkmcnt(0)\n"
      "v_bitop3_b32 v200, s57, v183, v200 bitop3:0xbe\n"
      "v_bitop3_b32 v200, s57, v187, v200 bitop3:0xbe\n"
      "v_cmp_ne_u32 vcc, 0, v200\n"
      "v_mfma_f32_16x16x32_bf16 v[32:35], v[56:59], v[64:67], v[32:35]\n"
      "v_add_f32 v116, v124, v128\n"
      "v_add_f32 v118, v126, v130\n"
      "v_add_f32 v116, v116, v132\n"
      "v_mfma_f32_16x16x32_bf16 v[36:39], v[60:63], v[64:67], v[36:39]\n"
      "v_add_f32 v118, v118, v134\n"
      "v_add_f32 v116, v116, v136\n"
      "v_add_f32 v118, v118, v138\n"
      "v_mfma_f32_16x16x32_bf16 v[40:43], v[56:59], v[68:71], v[40:43]\n"
      "v_add_f32 v116, v116, v172\n"
      "v_add_f32 v118, v118, v174\n"
      "v_add_f32 v116, v116, v176\n"
      "v_mfma_f32_16x16x32_bf16 v[44:47], v[60:63], v[68:71], v[44:47]\n"
      "v_add_f32 v118, v118, v178\n"
      "v_add_f32 v116, v116, v180\n"
      "v_add_f32 v118, v118, v182\n"
      "v_mfma_f32_16x16x32_bf16 v[48:51], v[56:59], v[72:75], v[48:51]\n"
      "v_add_f32 v116, v116, v184\n"
      "v_add_f32 v118, v118, v186\n"
      "v_mfma_f32_16x16x32_bf16 v[52:55], v[60:63], v[72:75], v[52:55]\n"
      "s_cmp_eq_u64 vcc, 0\n"
      "s_cbranch_scc1 Lg40_%=\n"
      "buffer_load_dwordx4 v[124:127], v197, s[8:11], s80 offen offset:0 sc1\n"
      "buffer_load_dwordx4 v[128:131], v197, s[8:11], s80 offen offset:1024 sc1\n"
      "Lhs2_1_%=:\n"
      "s_cbranch_execz Lhr2_%=\n"
      "buffer_load_dwordx4 v[132:135], v197, s[8:11], s80 offen offset:2048 sc1\n"
      "buffer_load_dwordx4 v[136:139], v197, s[8:11], s80 offen offset:3072 sc1\n"
      "buffer_load_dwordx4 v[172:175], v197, s[8:11], s81 offen offset:0 sc1\n"
      "buffer_load_dwordx4 v[176:179], v197, s[8:11], s81 offen offset:1024 sc1\n"
      "buffer_load_dwordx4 v[180:183], v197, s[8:11], s81 offen offset:2048 sc1\n"
      "buffer_load_dwordx4 v[184:187], v197, s[8:11], s81 offen offset:3072 sc1\n"
      "Lp42_%=:\n"
      "s_waitcnt vmcnt(0)\n"
      "v_xor_b32_e64 v200, s56, v125\n"
      "v_bitop3_b32 v200, s56, v129, v200 bitop3:0xbe\n"
      "v_bitop3_b32 v200, s56, v133, v200 bitop3:0xbe\n"
      "v_bitop3_b32 v200, s56, v137, v200 bitop3:0xbe\n"
      "v_bitop3_b32 v200, s56, v173, v200 bitop3:0xbe\n"
      "v_bitop3_b32 v200, s56, v177, v200 bitop3:0xbe\n"
      "v_bitop3_b32 v200, s56, v181, v200 bitop3:0xbe\n"
      "v_bitop3_b32 v200, s56, v185, v200 bitop3:0xbe\n"
      "v_bitop3_b32 v200, s57, v127, v200 bitop3:0xbe\n"
      "v_bitop3_b32 v200, s57, v131, v200 bitop3:0xbe\n"
      "v_bitop3_b32 v200, s57, v135, v200 bitop3:0xbe\n"
      "v_bitop3_b32 v200, s57, v139, v200 bitop3:0xbe\n"
      "v_bitop3_b32 v200, s57, v175, v200 bitop3:0xbe\n"
      "v_bitop3_b32 v200, s57, v179, v200 bitop3:0xbe\n"
      "v_bitop3_b32 v200, s57, v183, v200 bitop3:0xbe\n"
      "v_bitop3_b32 v200, s57, v187, v200 bitop3:0xbe\n"
      "v_cmp_ne_u32 vcc, 0, v200\n"
      "s_nop 0\n"
      "s_cmp_eq_u64 vcc, 0\n"
      "s_cbranch_scc1 Lpd43_%=\n"
      "s_cmp_eq_u32 s62, 0\n"
      "s_cbranch_scc1 Lpd43_%=\n"
      "s_sub_u32 s62, s62, 1\n"
      "s_sleep 1\n"
      "buffer_load_dwordx4 v[124:127], v197, s[8:11], s80 offen offset:0 sc1\n"
      "buffer_load_dwordx4 v[128:131], v197, s[8:11], s80 offen offset:1024 sc1\n"
      "buffer_load_dwordx4 v[132:135], v197, s[8:11], s80 offen offset:2048 sc1\n"
      "buffer_load_dwordx4 v[136:139], v197, s[8:11], s80 offen offset:3072 sc1\n"
      "buffer_load_dwordx4 v[172:175], v197, s[8:11], s81 offen offset:0 sc1\n"
      "buffer_load_dwordx4 v[176:179], v197, s[8:11], s81 offen offset:1024 sc1\n"
      "buffer_load_dwordx4 v[180:183], v197, s[8:11], s81 offen offset:2048 sc1\n"
      "buffer_load_dwordx4 v[184:187], v197, s[8:11], s81 offen offset:3072 sc1\n"
      "s_branch Lp42_%=\n"
      "Lpd43_%=:\n"
      "v_add_f32 v116, v124, v128\n"
      "v_add_f32 v118, v126, v130\n"
      "v_add_f32 v116, v116, v132\n"
      "v_add_f32 v118, v118, v134\n"
      "v_add_f32 v116, v116, v136\n"
      "v_add_f32 v118, v118, v138\n"
      "v_add_f32 v116, v116, v172\n"
      "v_add_f32 v118, v118, v174\n"
      "v_add_f32 v116, v116, v176\n"
      "v_add_f32 v118, v118, v178\n"
      "v_add_f32 v116, v116, v180\n"
      "v_add_f32 v118, v118, v182\n"
      "v_add_f32 v116, v116, v184\n"
      "v_add_f32 v118, v118, v186\n"
      "Lg40_%=:\n"
      "v_mov_b32 v117, v116\n"
      "v_mov_b32 v119, v118\n"
      "s_nop 0\n"
      "v_permlane32_swap_b32 v116, v117\n"
      "v_permlane32_swap_b32 v118, v119\n"
      "v_add_f32 v116, v116, v117\n"
      "v_add_f32 v118, v118, v119\n"
      "v_mul_f32 v116, 0x3a924925, v116\n"
      "v_mul_f32 v118, 0x3a924925, v118\n"
      "v_fma_f32 v118, -v116, v116, v118\n"
      "v_max_f32 v118, 0, v118\n"
      "v_add_f32 v118, 0x3727c5ac, v118\n"
      "v_rsq_f32 v118, v118\n"
      "s_nop 0\n"
      "v_mul_f32 v116, -v116, v118\n"
      "ds_write_b32 v195, v118\n"
      "ds_write_b32 v195, v116 offset:128\n"
      "ds_read_b128 v[56:59], v196\n"
      "ds_read_b128 v[60:63], v196 offset:16\n"
      "ds_read_b128 v[64:67], v196 offset:128\n"
      "ds_read_b128 v[68:71], v196 offset:144\n"
      "s_nop 0\n"
      "v_cvt_pk_bf16_f32 v236, v24, v25\n"
      "v_cvt_pk_bf16_f32 v237, v26, v27\n"
      "v_cvt_pk_bf16_f32 v238, v28, v29\n"
      "v_cvt_pk_bf16_f32 v239, v30, v31\n"
      "v_pk_fma_f32 v[116:117], v[164:165], v[148:149], v[148:149] op_sel:[0,0,1] op_sel_hi:[1,0,1]\n"
      "v_pk_fma_f32 v[118:119], v[166:167], v[148:149], v[148:149] op_sel:[0,0,1] op_sel_hi:[1,0,1]\n"
      "v_pk_fma_f32 v[120:121], v[168:169], v[148:149], v[148:149] op_sel:[0,0,1] op_sel_hi:[1,0,1]\n"
      "v_pk_fma_f32 v[122:123], v[170:171], v[148:149], v[148:149] op_sel:[0,0,1] op_sel_hi:[1,0,1]\n"
      "v_pk_fma_f32 v[32:33], v[32:33], v[156:157], v[116:117]\n"
      "v_pk_fma_f32 v[34:35], v[34:35], v[158:159], v[118:119]\n"
      "v_pk_fma_f32 v[36:37], v[36:37], v[160:161], v[120:121]\n"
      "v_pk_fma_f32 v[38:39], v[38:39], v[162:163], v[122:123]\n"
      "v_pk_mul_f32 v[116:117], v[32:33], v[32:33]\n"
      "v_pk_mul_f32 v[118:119], v[34:35], v[34:35]\n"
      "v_pk_mul_f32 v[120:121], v[36:37], v[36:37]\n"
      "v_pk_mul_f32 v[122:123], v[38:39], v[38:39]\n"
      "v_pk_fma_f32 v[116:117], v[116:117], s[88:89], s[88:89] op_sel:[0,1,0] op_sel_hi:[1,1,0]\n"
      "v_pk_fma_f32 v[118:119], v[118:119], s[88:89], s[88:89] op_sel:[0,1,0] op_sel_hi:[1,1,0]\n"
      "v_pk_fma_f32 v[120:121], v[120:121], s[88:89], s[88:89] op_sel:[0,1,0] op_sel_hi:[1,1,0]\n"
      "v_pk_fma_f32 v[122:123], v[122:123], s[88:89], s[88:89] op_sel:[0,1,0] op_sel_hi:[1,1,0]\n"
      "s_waitcnt lgkmcnt(0)\n"
      "s_nop 0\n"
      "v_pk_fma_f32 v[0:1], v[0:1], v[56:57], v[64:65]\n"
      "v_pk_fma_f32 v[2:3], v[2:3], v[58:59], v[66:67]\n"
      "v_pk_fma_f32 v[4:5], v[4:5], v[60:61], v[68:69]\n"
      "v_pk_fma_f32 v[6:7], v[6:7], v[62:63], v[70:71]\n"
      "v_cvt_pk_bf16_f32 v72, v0, v1\n"
      "v_cvt_pk_bf16_f32 v73, v2, v3\n"
      "v_cvt_pk_bf16_f32 v74, v4, v5\n"
      "v_cvt_pk_bf16_f32 v75, v6, v7\n"
      "buffer_store_dwordx4 v[72:75], v254, s[8:11], s83 offen offset:0 sc1\n"
      "v_pk_fma_f32 v[8:9], v[8:9], v[56:57], v[64:65]\n"
      "v_pk_fma_f32 v[10:11], v[10:11], v[58:59], v[66:67]\n"
      "v_pk_fma_f32 v[12:13], v[12:13], v[60:61], v[68:69]\n"
      "v_pk_fma_f32 v[14:15], v[14:15], v[62:63], v[70:71]\n"
      "v_cvt_pk_bf16_f32 v76, v8, v9\n"
      "v_cvt_pk_bf16_f32 v77, v10, v11\n"
      "v_cvt_pk_bf16_f32 v78, v12, v13\n"
      "v_cvt_pk_bf16_f32 v79, v14, v15\n"
      "buffer_store_dwordx4 v[76:79], v254, s[8:11], s83 offen offset:2048 sc1\n"
      "v_pk_fma_f32 v[16:17], v[16:17], v[56:57], v[64:65]\n"
      "v_pk_fma_f32 v[18:19], v[18:19], v[58:59], v[66:67]\n"
      "v_pk_fma_f32 v[20:21], v[20:21], v[60:61], v[68:69]\n"
      "v_pk_fma_f32 v[22:23], v[22:23], v[62:63], v[70:71]\n"
      "v_cvt_pk_bf16_f32 v80, v16, v17\n"
      "v_cvt_pk_bf16_f32 v81, v18, v19\n"
      "v_cvt_pk_bf16_f32 v82, v20, v21\n"
      "v_cvt_pk_bf16_f32 v83, v22, v23\n"
      "buffer_store_dwordx4 v[80:83], v254, s[8:11], s84 offen offset:0 sc1\n"
      "s_cmp_lg_u32 s61, 0\n"
      "s_cbranch_scc1 Lq44_%=\n"
      "v_pk_fma_f32 v[24:25], v[24:25], v[56:57], v[64:65]\n"
      "v_pk_fma_f32 v[26:27], v[26:27], v[58:59], v[66:67]\n"
      "v_pk_fma_f32 v[28:29], v[28:29], v[60:61], v[68:69]\n"
      "v_pk_fma_f32 v[30:31], v[30:31], v[62:63], v[70:71]\n"
      "v_cvt_pk_bf16_f32 v84, v24, v25\n"
      "v_cvt_pk_bf16_f32 v85, v26, v27\n"
      "v_cvt_pk_bf16_f32 v86, v28, v29\n"
      "v_cvt_pk_bf16_f32 v87, v30, v31\n"
      "buffer_store_dwordx4 v[84:87], v254, s[8:11], s84 offen offset:2048 sc1\n"
      "Lq44_%=:\n"
      "v_pk_mul_f32 v[116:117], v[116:117], v[32:33]\n"
      "v_pk_mul_f32 v[118:119], v[118:119], v[34:35]\n"
      "v_pk_mul_f32 v[120:121], v[120:121], v[36:37]\n"
      "v_pk_mul_f32 v[122:123], v[122:123], v[38:39]\n"
      "v_exp_f32 v116, v116\n"
      "v_exp_f32 v117, v117\n"
      "v_exp_f32 v118, v118\n"
      "v_exp_f32 v119, v119\n"
      "v_exp_f32 v120, v120\n"
      "v_exp_f32 v121, v121\n"
      "v_exp_f32 v122, v122\n"
      "v_exp_f32 v123, v123\n"
      "v_pk_add_f32 v[116:117], v[116:117], 1.0 op_sel_hi:[1,0]\n"
      "v_pk_add_f32 v[118:119], v[118:119], 1.0 op_sel_hi:[1,0]\n"
      "v_pk_add_f32 v[120:121], v[120:121], 1.0 op_sel_hi:[1,0]\n"
      "v_pk_add_f32 v[122:123], v[122:123], 1.0 op_sel_hi:[1,0]\n"
      "v_rcp_f32 v116, v116\n"
      "v_rcp_f32 v117, v117\n"
      "v_rcp_f32 v118, v118\n"
      "v_rcp_f32 v119, v119\n"
      "v_rcp_f32 v120, v120\n"
      "v_rcp_f32 v121, v121\n"
      "v_rcp_f32 v122, v122\n"
      "v_rcp_f32 v123, v123\n"
      "v_pk_mul_f32 v[32:33], v[32:33], v[116:117]\n"
      "v_pk_mul_f32 v[34:35], v[34:35], v[118:119]\n"
      "v_pk_mul_f32 v[36:37], v[36:37], v[120:121]\n"
      "v_pk_mul_f32 v[38:39], v[38:39], v[122:123]\n"
      "v_cvt_pk_bf16_f32 v224, v32, v33\n"
      "v_cvt_pk_bf16_f32 v225, v34, v35\n"
      "v_cvt_pk_bf16_f32 v226, v36, v37\n"
      "v_cvt_pk_bf16_f32 v227, v38, v39\n"
      "v_pk_fma_f32 v[116:117], v[164:165], v[150:151], v[150:151] op_sel:[0,0,1] op_sel_hi:[1,0,1]\n"
      "v_pk_fma_f32 v[118:119], v[166:167], v[150:151], v[150:151] op_sel:[0,0,1] op_sel_hi:[1,0,1]\n"
      "v_pk_fma_f32 v[120:121], v[168:169], v[150:151], v[150:151] op_sel:[0,0,1] op_sel_hi:[1,0,1]\n"
      "v_pk_fma_f32 v[122:123], v[170:171], v[150:151], v[150:151] op_sel:[0,0,1] op_sel_hi:[1,0,1]\n"
      "v_pk_fma_f32 v[40:41], v[40:41], v[156:157], v[116:117]\n"
      "v_pk_fma_f32 v[42:43], v[42:43], v[158:159], v[118:119]\n"
      "v_pk_fma_f32 v[44:45], v[44:45], v[160:161], v[120:121]\n"
      "v_pk_fma_f32 v[46:47], v[46:47], v[162:163], v[122:123]\n"
      "v_pk_mul_f32 v[116:117], v[40:41], v[40:41]\n"
      "v_pk_mul_f32 v[118:119], v[42:43], v[42:43]\n"
      "v_pk_mul_f32 v[120:121], v[44:45], v[44:45]\n"
      "v_pk_mul_f32 v[122:123], v[46:47], v[46:47]\n"
      "v_pk_fma_f32 v[116:117], v[116:117], s[88:89], s[88:89] op_sel:[0,1,0] op_sel_hi:[1,1,0]\n"
      "v_pk_fma_f32 v[118:119], v[118:119], s[88:89], s[88:89] op_sel:[0,1,0] op_sel_hi:[1,1,0]\n"
      "v_pk_fma_f32 v[120:121], v[120:121], s[88:89], s[88:89] op_sel:[0,1,0] op_sel_hi:[1,1,0]\n"
      "v_pk_fma_f32 v[122:123], v[122:123], s[88:89], s[88:89] op_sel:[0,1,0] op_sel_hi:[1,1,0]\n"
      "v_pk_mul_f32 v[116:117], v[116:117], v[40:41]\n"
      "v_pk_mul_f32 v[118:119], v[118:119], v[42:43]\n"
      "v_pk_mul_f32 v[120:121], v[120:121], v[44:45]\n"
      "v_pk_mul_f32 v[122:123], v[122:123], v[46:47]\n"
      "v_exp_f32 v116, v116\n"
      "v_exp_f32 v117, v117\n"
      "v_exp_f32 v118, v118\n"
      "v_exp_f32 v119, v119\n"
      "v_exp_f32 v120, v120\n"
      "v_exp_f32 v121, v121\n"
      "v_exp_f32 v122, v122\n"
      "v_exp_f32 v123, v123\n"
      "v_pk_add_f32 v[116:117], v[116:117], 1.0 op_sel_hi:[1,0]\n"
      "v_pk_add_f32 v[118:119], v[118:119], 1.0 op_sel_hi:[1,0]\n"
      "v_pk_add_f32 v[120:121], v[120:121], 1.0 op_sel_hi:[1,0]\n"
      "v_pk_add_f32 v[122:123], v[122:123], 1.0 op_sel_hi:[1,0]\n"
      "v_rcp_f32 v116, v116\n"
      "v_rcp_f32 v117, v117\n"
      "v_rcp_f32 v118, v118\n"
      "v_rcp_f32 v119, v119\n"
      "v_rcp_f32 v120, v120\n"
      "v_rcp_f32 v121, v121\n"
      "v_rcp_f32 v122, v122\n"
      "v_rcp_f32 v123, v123\n"
      "v_pk_mul_f32 v[40:41], v[40:41], v[116:117]\n"
      "v_pk_mul_f32 v[42:43], v[42:43], v[118:119]\n"
      "v_pk_mul_f32 v[44:45], v[44:45], v[120:121]\n"
      "v_pk_mul_f32 v[46:47], v[46:47], v[122:123]\n"
      "v_cvt_pk_bf16_f32 v228, v40, v41\n"
      "v_cvt_pk_bf16_f32 v229, v42, v43\n"
      "v_cvt_pk_bf16_f32 v230, v44, v45\n"
      "v_cvt_pk_bf16_f32 v231, v46, v47\n"
      "s_cmp_lg_u32 s53, 0\n"
      "s_waitcnt vmcnt(0) lgkmcnt(0)\n"
      "s_barrier\n"
      "s_cbranch_scc1 Lf45_%=\n"
      "s_mov_b64 exec, 1\n"
      "buffer_store_dwordx2 v[252:253], off, s[8:11], s90 sc1\n"
      "s_mov_b64 exec, -1\n"
      "Lf45_%=:\n"
      "s_barrier\n"
      "v_pk_fma_f32 v[116:117], v[164:165], v[152:153], v[152:153] op_sel:[0,0,1] op_sel_hi:[1,0,1]\n"
      "s_add_u32 m0, s85, 67584\n"
      "s_add_u32 s67, s98, 81920\n"
      "buffer_load_dwordx4 v249, s[32:35], s67 offen lds\n"
      "s_nop 0\n"
      "v_pk_fma_f32 v[118:119], v[166:167], v[152:153], v[152:153] op_sel:[0,0,1] op_sel_hi:[1,0,1]\n"
      "v_pk_fma_f32 v[120:121], v[168:169], v[152:153], v[152:153] op_sel:[0,0,1] op_sel_hi:[1,0,1]\n"
      "v_pk_fma_f32 v[122:123], v[170:171], v[152:153], v[152:153] op_sel:[0,0,1] op_sel_hi:[1,0,1]\n"
      "v_pk_fma_f32 v[48:49], v[48:49], v[156:157], v[116:117]\n"
      "v_pk_fma_f32 v[50:51], v[50:51], v[158:159], v[118:119]\n"
      "s_add_u32 m0, s85, 69632\n"
      "s_add_u32 s67, s98, 98304\n"
      "buffer_load_dwordx4 v249, s[32:35], s67 offen lds\n"
      "v_pk_fma_f32 v[52:53], v[52:53], v[160:161], v[120:121]\n"
      "v_pk_fma_f32 v[54:55], v[54:55], v[162:163], v[122:123]\n"
      "v_pk_mul_f32 v[116:117], v[48:49], v[48:49]\n"
      "v_pk_mul_f32 v[118:119], v[50:51], v[50:51]\n"
      "v_pk_mul_f32 v[120:121], v[52:53], v[52:53]\n"
      "s_add_u32 m0, s85, 28672\n"
      "s_add_u32 s67, s97, 0\n"
      "buffer_load_dwordx4 v249, s[32:35], s67 offen lds\n"
      "s_nop 0\n"
      "v_pk_mul_f32 v[122:123], v[54:55], v[54:55]\n"
      "v_pk_fma_f32 v[116:117], v[116:117], s[88:89], s[88:89] op_sel:[0,1,0] op_sel_hi:[1,1,0]\n"
      "v_pk_fma_f32 v[118:119], v[118:119], s[88:89], s[88:89] op_sel:[0,1,0] op_sel_hi:[1,1,0]\n"
      "v_pk_fma_f32 v[120:121], v[120:121], s[88:89], s[88:89] op_sel:[0,1,0] op_sel_hi:[1,1,0]\n"
      "v_pk_fma_f32 v[122:123], v[122:123], s[88:89], s[88:89] op_sel:[0,1,0] op_sel_hi:[1,1,0]\n"
      "s_add_u32 m0, s85, 30720\n"
      "s_add_u32 s67, s97, 16384\n"
      "buffer_load_dwordx4 v249, s[32:35], s67 offen lds\n"
      "v_pk_mul_f32 v[116:117], v[116:117], v[48:49]\n"
      "v_pk_mul_f32 v[118:119], v[118:119], v[50:51]\n"
      "v_pk_mul_f32 v[120:121], v[120:121], v[52:53]\n"
      "v_pk_mul_f32 v[122:123], v[122:123], v[54:55]\n"
      "v_exp_f32 v116, v116\n"
      "s_add_u32 m0, s85, 32768\n"
      "s_add_u32 s67, s97, 32768\n"
      "buffer_load_dwordx4 v249, s[32:35], s67 offen lds\n"
      "v_exp_f32 v117, v117\n"
      "v_exp_f32 v118, v118\n"
      "v_exp_f32 v119, v119\n"
      "v_exp_f32 v120, v120\n"
      "v_exp_f32 v121, v121\n"
      "s_add_u32 m0, s85, 34816\n"
      "s_add_u32 s67, s97, 49152\n"
      "buffer_load_dwordx4 v249, s[32:35], s67 offen lds\n"
      "v_exp_f32 v122, v122\n"
      "v_exp_f32 v123, v123\n"
      "v_pk_add_f32 v[116:117], v[116:117], 1.0 op_sel_hi:[1,0]\n"
      "v_pk_add_f32 v[118:119], v[118:119], 1.0 op_sel_hi:[1,0]\n"
      "v_pk_add_f32 v[120:121], v[120:121], 1.0 op_sel_hi:[1,0]\n"
      "s_add_u32 m0, s85, 36864\n"
      "s_add_u32 s67, s97, 65536\n"
      "buffer_load_dwordx4 v249, s[32:35], s67 offen lds\n"
      "v_pk_add_f32 v[122:123], v[122:123], 1.0 op_sel_hi:[1,0]\n"
      "v_rcp_f32 v116, v116\n"
      "v_rcp_f32 v117, v117\n"
      "v_rcp_f32 v118, v118\n"
      "v_rcp_f32 v119, v119\n"
      "s_add_u32 m0, s85, 38912\n"
      "s_add_u32 s67, s97, 81920\n"
      "buffer_load_dwordx4 v249, s[32:35], s67 offen lds\n"
      "v_rcp_f32 v120, v120\n"
      "v_rcp_f32 v121, v121\n"
      "v_rcp_f32 v122, v122\n"
      "v_rcp_f32 v123, v123\n"
      "v_pk_mul_f32 v[48:49], v[48:49], v[116:117]\n"
      "s_add_u32 m0, s85, 40960\n"
      "s_add_u32 s67, s97, 98304\n"
      "buffer_load_dwordx4 v249, s[32:35], s67 offen lds\n"
      "v_pk_mul_f32 v[50:51], v[50:51], v[118:119]\n"
      "v_pk_mul_f32 v[52:53], v[52:53], v[120:121]\n"
      "v_pk_mul_f32 v[54:55], v[54:55], v[122:123]\n"
      "v_cvt_pk_bf16_f32 v232, v48, v49\n"
      "v_cvt_pk_bf16_f32 v233, v50, v51\n"
      "s_add_u32 m0, s85, 0\n"
      "s_add_u32 s67, s96, 0\n"
      "buffer_load_dwordx4 v249, s[32:35], s67 offen lds\n"
      "v_cvt_pk_bf16_f32 v234, v52, v53\n"
      "v_cvt_pk_bf16_f32 v235, v54, v55\n"
      "s_lshl_b32 s63, s61, 10\n"
      "v_add_u32 v196, s63, v254\n"
      "v_xor_b32 v197, 1024, v196\n"
      "s_add_u32 m0, s85, 2048\n"
      "s_add_u32 s67, s96, 16384\n"
      "buffer_load_dwordx4 v249, s[32:35], s67 offen lds\n"
      "v_lshl_add_u32 v212, v211, 3, v209\n"
      "v_lshlrev_b32 v212, 7, v212\n"
      "v_lshl_add_u32 v212, v210, 5, v212\n"
      "v_add_u32 v212, v212, v208\n"
      "v_lshrrev_b32 v207, 1, v209\n"
      "s_add_u32 m0, s85, 4096\n"
      "s_add_u32 s67, s96, 32768\n"
      "buffer_load_dwordx4 v249, s[32:35], s67 offen lds\n"
      "v_xor_b32 v207, s61, v207\n"
      "v_lshl_add_u32 v198, v207, 6, v212\n"
      "v_add_u32 v199, 57344, v198\n"
      "s_xor_b32 s63, s53, 1\n"
      "s_mul_i32 s63, s63, 24576\n"
      "s_add_u32 m0, s85, 6144\n"
      "s_add_u32 s67, s96, 49152\n"
      "buffer_load_dwordx4 v249, s[32:35], s67 offen lds\n"
      "s_add_u32 s63, s63, 0\n"
      "v_add_u32 v200, s63, v254\n"
      "s_xor_b32 s63, s53, 2\n"
      "s_mul_i32 s63, s63, 24576\n"
      "s_add_u32 s63, s63, 8192\n"
      "s_add_u32 m0, s85, 8192\n"
      "s_add_u32 s67, s96, 65536\n"
      "buffer_load_dwordx4 v249, s[32:35], s67 offen lds\n"
      "v_add_u32 v201, s63, v254\n"
      "s_xor_b32 s63, s53, 3\n"
      "s_mul_i32 s63, s63, 24576\n"
      "s_add_u32 s63, s63, 16384\n"
      "v_add_u32 v202, s63, v254\n"
      "s_add_u32 m0, s85, 10240\n"
      "s_add_u32 s67, s96, 81920\n"
      "buffer_load_dwordx4 v249, s[32:35], s67 offen lds\n"
      "s_or_b32 s63, s53, 1\n"
      "s_xor_b32 s63, s63, 0\n"
      "s_mul_i32 s65, s63, 3\n"
      "s_xor_b32 s64, s63, s53\n"
      "s_add_u32 s65, s65, s64\n"
      "s_add_u32 m0, s85, 12288\n"
      "s_add_u32 s67, s96, 98304\n"
      "buffer_load_dwordx4 v249, s[32:35], s67 offen lds\n"
      "s_sub_u32 s65, s65, 1\n"
      "s_cmp_eq_u32 s64, 0\n"
      "s_cselect_b32 s65, 14, s65\n"
      "s_lshl_b32 s65, s65, 13\n"
      "v_add_u32 v203, s65, v254\n"
      "s_or_b32 s63, s53, 1\n"
      "s_xor_b32 s63, s63, 2\n"
      "s_mul_i32 s65, s63, 3\n"
      "s_xor_b32 s64, s63, s53\n"
      "s_add_u32 s65, s65, s64\n"
      "s_sub_u32 s65, s65, 1\n"
      "s_cmp_eq_u32 s64, 0\n"
      "s_cselect_b32 s65, 14, s65\n"
      "s_lshl_b32 s65, s65, 13\n"
      "v_add_u32 v204, s65, v254\n"
      "s_mul_i32 s63, s53, 24576\n"
      "v_add_u32 v205, s63, v254\n"
      "v_lshrrev_b32 v212, 4, v255\n"
      "v_lshl_add_u32 v207, v212, 2, v255\n"
      "v_and_b32 v207, 15, v207\n"
      "v_lshlrev_b32 v207, 4, v207\n"
      "v_lshl_add_u32 v206, v212, 8, v207\n"
      "v_lshlrev_b32 v212, 4, v212\n"
      "s_cmp_eq_u32 s61, 0\n"
      "s_cselect_b32 s63, 0x40000000, 0\n"
      "v_or_b32 v207, s63, v206\n"
      "s_lshl1_add_u32 s63, s58, s60\n"
      "s_lshl_b32 s74, s63, 6\n"
      "s_mul_i32 s71, s63, 57344\n"
      "s_mul_i32 s64, s59, 7168\n"
      "s_add_u32 s71, s71, s64\n"
      "s_add_u32 s71, s71, 15728640\n"
      "s_and_b32 s66, s59, 1\n"
      "s_cselect_b32 s73, 0, 6144\n"
      "s_add_u32 s73, s73, s71\n"
      "s_add_u32 s66, s66, s61\n"
      "s_lshl_b32 s66, s66, 10\n"
      "s_add_u32 s71, s71, s66\n"
      "s_add_u32 s72, s71, 4096\n"
      "s_mul_i32 s68, s59, 458752\n"
      "s_mul_i32 s63, s53, 114688\n"
      "s_add_u32 s68, s68, s63\n"
      "s_add_u32 s68, s68, 11534336\n"
      "s_and_b32 s63, s59, 0\n"
      "s_xor_b32 s92, s63, 0\n"
      "s_mul_i32 s92, s92, 28672\n"
      "s_add_u32 s92, s92, s68\n"
      "s_xor_b32 s93, s63, 1\n"
      "s_mul_i32 s93, s93, 28672\n"
      "s_add_u32 s93, s93, s68\n"
      "s_xor_b32 s94, s63, 2\n"
      "s_mul_i32 s94, s94, 28672\n"
      "s_add_u32 s94, s94, s68\n"
      "s_xor_b32 s95, s63, 3\n"
      "s_mul_i32 s95, s95, 28672\n"
      "s_add_u32 s95, s95, s68\n"
      "s_lshl2_add_u32 s70, s59, s53\n"
      "s_mul_i32 s70, s70, 256\n"
      "s_add_u32 s70, s70, 21585920\n"
      "s_mul_i32 s90, s58, 256\n"
      "s_lshl3_add_u32 s90, s59, s90\n"
      "s_add_u32 s90, s90, 21594112\n"
      "buffer_load_dwordx2 v[220:221], v248, s[8:11], s70 offen sc1\n"
      "Lp46_%=:\n"
      "s_waitcnt vmcnt(0)\n"
      "v_cmp_ne_u64 vcc, s[56:57], v[220:221]\n"
      "s_nop 0\n"
      "s_cmp_eq_u64 vcc, 0\n"
      "s_cbranch_scc1 Lpd47_%=\n"
      "s_cmp_eq_u32 s62, 0\n"
      "s_cbranch_scc1 Lpd47_%=\n"
      "s_sub_u32 s62, s62, 1\n"
      "s_sleep 1\n"
      "buffer_load_dwordx2 v[220:221], v248, s[8:11], s70 offen sc1\n"
      "s_branch Lp46_%=\n"
      "Lpd47_%=:\n"
      "s_add_u32 s69, s92, 0\n"
      "buffer_load_dwordx4 v[112:115], v196, s[8:11], s69 offen offset:0 sc1\n"
      "s_add_u32 s69, s92, 7168\n"
      "buffer_load_dwordx4 v[116:119], v196, s[8:11], s69 offen offset:0 sc1\n"
      "s_add_u32 s69, s92, 0\n"
      "buffer_load_dwordx4 v[120:123], v197, s[8:11], s69 offen offset:0 sc1\n"
      "s_add_u32 s69, s92, 7168\n"
      "buffer_load_dwordx4 v[124:127], v197, s[8:11], s69 offen offset:0 sc1\n"
      "s_add_u32 s69, s92, 0\n"
      "buffer_load_dwordx4 v[128:131], v196, s[8:11], s69 offen offset:2048 sc1\n"
      "s_add_u32 s69, s92, 7168\n"
      "buffer_load_dwordx4 v[132:135], v196, s[8:11], s69 offen offset:2048 sc1\n"
      "s_add_u32 s69, s92, 0\n"
      "buffer_load_dwordx4 v[136:139], v197, s[8:11], s69 offen offset:2048 sc1\n"
      "s_add_u32 s69, s92, 7168\n"
      "buffer_load_dwordx4 v[140:143], v197, s[8:11], s69 offen offset:2048 sc1\n"
      "s_add_u32 s69, s92, 4096\n"
      "buffer_load_dwordx4 v[144:147], v196, s[8:11], s69 offen offset:0 sc1\n"
      "s_add_u32 s69, s92, 11264\n"
      "buffer_load_dwordx4 v[148:151], v196, s[8:11], s69 offen offset:0 sc1\n"
      "s_add_u32 s69, s92, 4096\n"
      "buffer_load_dwordx4 v[152:155], v197, s[8:11], s69 offen offset:0 sc1\n"
      "s_add_u32 s69, s92, 11264\n"
      "buffer_load_dwordx4 v[156:159], v197, s[8:11], s69 offen offset:0 sc1\n"
      "s_waitcnt vmcnt(11)\n"
      "v_mfma_f32_16x16x32_bf16 v[56:59], a[8:11], v[112:115], 0\n"
      "s_waitcnt vmcnt(10)\n"
      "v_mfma_f32_16x16x32_bf16 v[56:59], a[24:27], v[116:119], v[56:59]\n"
      "v_mfma_f32_16x16x32_bf16 v[60:63], a[12:15], v[112:115], 0\n"
      "v_mfma_f32_16x16x32_bf16 v[60:63], a[28:31], v[116:119], v[60:63]\n"
      "s_add_u32 s69, s92, 4096\n"
      "buffer_load_dwordx4 v[160:163], v254, s[8:11], s69 offen offset:2048 sc1\n"
      "v_mfma_f32_16x16x32_bf16 v[0:3], a[0:3], v[112:115], 0\n"
      "v_mfma_f32_16x16x32_bf16 v[0:3], a[16:19], v[116:119], v[0:3]\n"
      "v_mfma_f32_16x16x32_bf16 v[4:7], a[4:7], v[112:115], 0\n"
      "v_mfma_f32_16x16x32_bf16 v[4:7], a[20:23], v[116:119], v[4:7]\n"
      "s_add_u32 s69, s92, 11264\n"
      "buffer_load_dwordx4 v[164:167], v254, s[8:11], s69 offen offset:2048 sc1\n"
      "s_waitcnt vmcnt(11)\n"
      "v_mfma_f32_16x16x32_bf16 v[64:67], a[8:11], v[120:123], 0\n"
      "s_waitcnt vmcnt(10)\n"
      "v_mfma_f32_16x16x32_bf16 v[64:67], a[24:27], v[124:127], v[64:67]\n"
      "v_mfma_f32_16x16x32_bf16 v[68:71], a[12:15], v[120:123], 0\n"
      "v_mfma_f32_16x16x32_bf16 v[68:71], a[28:31], v[124:127], v[68:71]\n"
      "s_add_u32 s69, s92, 14336\n"
      "buffer_load_dwordx4 v[112:115], v196, s[8:11], s69 offen offset:0 sc1\n"
      "v_mfma_f32_16x16x32_bf16 v[8:11], a[0:3], v[120:123], 0\n"
      "v_mfma_f32_16x16x32_bf16 v[8:11], a[16:19], v[124:127], v[8:11]\n"
      "v_mfma_f32_16x16x32_bf16 v[12:15], a[4:7], v[120:123], 0\n"
      "v_mfma_f32_16x16x32_bf16 v[12:15], a[20:23], v[124:127], v[12:15]\n"
      "s_add_u32 s69, s92, 21504\n"
      "buffer_load_dwordx4 v[116:119], v196, s[8:11], s69 offen offset:0 sc1\n"
      "s_waitcnt vmcnt(11)\n"
      "v_mfma_f32_16x16x32_bf16 v[72:75], a[8:11], v[128:131], 0\n"
      "s_waitcnt vmcnt(10)\n"
      "v_mfma_f32_16x16x32_bf16 v[72:75], a[24:27], v[132:135], v[72:75]\n"
      "v_mfma_f32_16x16x32_bf16 v[76:79], a[12:15], v[128:131], 0\n"
      "v_mfma_f32_16x16x32_bf16 v[76:79], a[28:31], v[132:135], v[76:79]\n"
      "s_add_u32 s69, s92, 14336\n"
      "buffer_load_dwordx4 v[120:123], v197, s[8:11], s69 offen offset:0 sc1\n"
      "v_mfma_f32_16x16x32_bf16 v[16:19], a[0:3], v[128:131], 0\n"
      "v_mfma_f32_16x16x32_bf16 v[16:19], a[16:19], v[132:135], v[16:19]\n"
      "v_mfma_f32_16x16x32_bf16 v[20:23], a[4:7], v[128:131], 0\n"
      "v_mfma_f32_16x16x32_bf16 v[20:23], a[20:23], v[132:135], v[20:23]\n"
      "s_add_u32 s69, s92, 21504\n"
      "buffer_load_dwordx4 v[124:127], v197, s[8:11], s69 offen offset:0 sc1\n"
      "s_waitcnt vmcnt(11)\n"
      "v_mfma_f32_16x16x32_bf16 v[80:83], a[8:11], v[136:139], 0\n"
      "s_waitcnt vmcnt(10)\n"
      "v_mfma_f32_16x16x32_bf16 v[80:83], a[24:27], v[140:143], v[80:83]\n"
      "v_mfma_f32_16x16x32_bf16 v[84:87], a[12:15], v[136:139], 0\n"
      "v_mfma_f32_16x16x32_bf16 v[84:87], a[28:31], v[140:143], v[84:87]\n"
      "s_add_u32 s69, s92, 14336\n"
      "buffer_load_dwordx4 v[128:131], v196, s[8:11], s69 offen offset:2048 sc1\n"
      "v_mfma_f32_16x16x32_bf16 v[24:27], a[0:3], v[136:139], 0\n"
      "v_mfma_f32_16x16x32_bf16 v[24:27], a[16:19], v[140:143], v[24:27]\n"
      "v_mfma_f32_16x16x32_bf16 v[28:31], a[4:7], v[136:139], 0\n"
      "v_mfma_f32_16x16x32_bf16 v[28:31], a[20:23], v[140:143], v[28:31]\n"
      "s_add_u32 s69, s92, 21504\n"
      "buffer_load_dwordx4 v[132:135], v196, s[8:11], s69 offen offset:2048 sc1\n"
      "s_waitcnt vmcnt(11)\n"
      "v_mfma_f32_16x16x32_bf16 v[88:91], a[8:11], v[144:147], 0\n"
      "s_waitcnt vmcnt(10)\n"
      "v_mfma_f32_16x16x32_bf16 v[88:91], a[24:27], v[148:151], v[88:91]\n"
      "v_mfma_f32_16x16x32_bf16 v[92:95], a[12:15], v[144:147], 0\n"
      "v_mfma_f32_16x16x32_bf16 v[92:95], a[28:31], v[148:151], v[92:95]\n"
      "s_add_u32 s69, s92, 14336\n"
      "buffer_load_dwordx4 v[136:139], v197, s[8:11], s69 offen offset:2048 sc1\n"
      "v_mfma_f32_16x16x32_bf16 v[32:35], a[0:3], v[144:147], 0\n"
      "v_mfma_f32_16x16x32_bf16 v[32:35], a[16:19], v[148:151], v[32:35]\n"
      "v_mfma_f32_16x16x32_bf16 v[36:39], a[4:7], v[144:147], 0\n"
      "v_mfma_f32_16x16x32_bf16 v[36:39], a[20:23], v[148:151], v[36:39]\n"
      "s_add_u32 s69, s92, 21504\n"
      "buffer_load_dwordx4 v[140:143], v197, s[8:11], s69 offen offset:2048 sc1\n"
      "s_waitcnt vmcnt(11)\n"
      "v_mfma_f32_16x16x32_bf16 v[96:99], a[8:11], v[152:155], 0\n"
      "s_waitcnt vmcnt(10)\n"
      "v_mfma_f32_16x16x32_bf16 v[96:99], a[24:27], v[156:159], v[96:99]\n"
      "v_mfma_f32_16x16x32_bf16 v[100:103], a[12:15], v[152:155], 0\n"
      "v_mfma_f32_16x16x32_bf16 v[100:103], a[28:31], v[156:159], v[100:103]\n"
      "s_add_u32 s69, s92, 18432\n"
      "buffer_load_dwordx4 v[144:147], v196, s[8:11], s69 offen offset:0 sc1\n"
      "v_mfma_f32_16x16x32_bf16 v[40:43], a[0:3], v[152:155], 0\n"
      "v_mfma_f32_16x16x32_bf16 v[40:43], a[16:19], v[156:159], v[40:43]\n"
      "v_mfma_f32_16x16x32_bf16 v[44:47], a[4:7], v[152:155], 0\n"
      "v_mfma_f32_16x16x32_bf16 v[44:47], a[20:23], v[156:159], v[44:47]\n"
      "s_add_u32 s69, s92, 25600\n"
      "buffer_load_dwordx4 v[148:151], v196, s[8:11], s69 offen offset:0 sc1\n"
      "s_waitcnt vmcnt(11)\n"
      "v_mfma_f32_16x16x32_bf16 v[104:107], a[8:11], v[160:163], 0\n"
      "s_waitcnt vmcnt(10)\n"
      "v_mfma_f32_16x16x32_bf16 v[104:107], a[24:27], v[164:167], v[104:107]\n"
      "v_mfma_f32_16x16x32_bf16 v[108:111], a[12:15], v[160:163], 0\n"
      "v_mfma_f32_16x16x32_bf16 v[108:111], a[28:31], v[164:167], v[108:111]\n"
      "s_add_u32 s69, s92, 18432\n"
      "buffer_load_dwordx4 v[152:155], v197, s[8:11], s69 offen offset:0 sc1\n"
      "v_mfma_f32_16x16x32_bf16 v[48:51], a[0:3], v[160:163], 0\n"
      "v_mfma_f32_16x16x32_bf16 v[48:51], a[16:19], v[164:167], v[48:51]\n"
      "v_mfma_f32_16x16x32_bf16 v[52:55], a[4:7], v[160:163], 0\n"
      "v_mfma_f32_16x16x32_bf16 v[52:55], a[20:23], v[164:167], v[52:55]\n"
      "s_add_u32 s69, s92, 25600\n"
      "buffer_load_dwordx4 v[156:159], v197, s[8:11], s69 offen offset:0 sc1\n"
      "s_waitcnt vmcnt(11)\n"
      "v_mfma_f32_16x16x32_bf16 v[56:59], a[40:43], v[112:115], v[56:59]\n"
      "s_waitcnt vmcnt(10)\n"
      "v_mfma_f32_16x16x32_bf16 v[56:59], a[56:59], v[116:119], v[56:59]\n"
      "v_mfma_f32_16x16x32_bf16 v[60:63], a[44:47], v[112:115], v[60:63]\n"
      "v_mfma_f32_16x16x32_bf16 v[60:63], a[60:63], v[116:119], v[60:63]\n"
      "s_add_u32 s69, s92, 18432\n"
      "buffer_load_dwordx4 v[160:163], v254, s[8:11], s69 offen offset:2048 sc1\n"
      "v_mfma_f32_16x16x32_bf16 v[0:3], a[32:35], v[112:115], v[0:3]\n"
      "v_mfma_f32_16x16x32_bf16 v[0:3], a[48:51], v[116:119], v[0:3]\n"
      "v_mfma_f32_16x16x32_bf16 v[4:7], a[36:39], v[112:115], v[4:7]\n"
      "v_mfma_f32_16x16x32_bf16 v[4:7], a[52:55], v[116:119], v[4:7]\n"
      "s_add_u32 s69, s92, 25600\n"
      "buffer_load_dwordx4 v[164:167], v254, s[8:11], s69 offen offset:2048 sc1\n"
      "s_waitcnt vmcnt(11)\n"
      "v_mfma_f32_16x16x32_bf16 v[64:67], a[40:43], v[120:123], v[64:67]\n"
      "s_waitcnt vmcnt(10)\n"
      "v_mfma_f32_16x16x32_bf16 v[64:67], a[56:59], v[124:127], v[64:67]\n"
      "v_mfma_f32_16x16x32_bf16 v[68:71], a[44:47], v[120:123], v[68:71]\n"
      "v_mfma_f32_16x16x32_bf16 v[68:71], a[60:63], v[124:127], v[68:71]\n"
      "s_add_u32 s69, s93, 0\n"
      "buffer_load_dwordx4 v[112:115], v196, s[8:11], s69 offen offset:0 sc1\n"
      "v_mfma_f32_16x16x32_bf16 v[8:11], a[32:35], v[120:123], v[8:11]\n"
      "v_mfma_f32_16x16x32_bf16 v[8:11], a[48:51], v[124:127], v[8:11]\n"
      "v_mfma_f32_16x16x32_bf16 v[12:15], a[36:39], v[120:123], v[12:15]\n"
      "v_mfma_f32_16x16x32_bf16 v[12:15], a[52:55], v[124:127], v[12:15]\n"
      "s_add_u32 s69, s93, 7168\n"
      "buffer_load_dwordx4 v[116:119], v196, s[8:11], s69 offen offset:0 sc1\n"
      "s_waitcnt vmcnt(11)\n"
      "v_mfma_f32_16x16x32_bf16 v[72:75], a[40:43], v[128:131], v[72:75]\n"
      "s_waitcnt vmcnt(10)\n"
      "v_mfma_f32_16x16x32_bf16 v[72:75], a[56:59], v[132:135], v[72:75]\n"
      "v_mfma_f32_16x16x32_bf16 v[76:79], a[44:47], v[128:131], v[76:79]\n"
      "v_mfma_f32_16x16x32_bf16 v[76:79], a[60:63], v[132:135], v[76:79]\n"
      "s_add_u32 s69, s93, 0\n"
      "buffer_load_dwordx4 v[120:123], v197, s[8:11], s69 offen offset:0 sc1\n"
      "v_mfma_f32_16x16x32_bf16 v[16:19], a[32:35], v[128:131], v[16:19]\n"
      "v_mfma_f32_16x16x32_bf16 v[16:19], a[48:51], v[132:135], v[16:19]\n"
      "v_mfma_f32_16x16x32_bf16 v[20:23], a[36:39], v[128:131], v[20:23]\n"
      "v_mfma_f32_16x16x32_bf16 v[20:23], a[52:55], v[132:135], v[20:23]\n"
      "s_add_u32 s69, s93, 7168\n"
      "buffer_load_dwordx4 v[124:127], v197, s[8:11], s69 offen offset:0 sc1\n"
      "s_waitcnt vmcnt(11)\n"
      "v_mfma_f32_16x16x32_bf16 v[80:83], a[40:43], v[136:139], v[80:83]\n"
      "s_waitcnt vmcnt(10)\n"
      "v_mfma_f32_16x16x32_bf16 v[80:83], a[56:59], v[140:143], v[80:83]\n"
      "v_mfma_f32_16x16x32_bf16 v[84:87], a[44:47], v[136:139], v[84:87]\n"
      "v_mfma_f32_16x16x32_bf16 v[84:87], a[60:63], v[140:143], v[84:87]\n"
      "s_add_u32 s69, s93, 0\n"
      "buffer_load_dwordx4 v[128:131], v196, s[8:11], s69 offen offset:2048 sc1\n"
      "v_mfma_f32_16x16x32_bf16 v[24:27], a[32:35], v[136:139], v[24:27]\n"
      "v_mfma_f32_16x16x32_bf16 v[24:27], a[48:51], v[140:143], v[24:27]\n"
      "v_mfma_f32_16x16x32_bf16 v[28:31], a[36:39], v[136:139], v[28:31]\n"
      "v_mfma_f32_16x16x32_bf16 v[28:31], a[52:55], v[140:143], v[28:31]\n"
      "s_add_u32 s69, s93, 7168\n"
      "buffer_load_dwordx4 v[132:135], v196, s[8:11], s69 offen offset:2048 sc1\n"
      "s_waitcnt vmcnt(11)\n"
      "v_mfma_f32_16x16x32_bf16 v[88:91], a[40:43], v[144:147], v[88:91]\n"
      "s_waitcnt vmcnt(10)\n"
      "v_mfma_f32_16x16x32_bf16 v[88:91], a[56:59], v[148:151], v[88:91]\n"
      "v_mfma_f32_16x16x32_bf16 v[92:95], a[44:47], v[144:147], v[92:95]\n"
      "v_mfma_f32_16x16x32_bf16 v[92:95], a[60:63], v[148:151], v[92:95]\n"
      "s_add_u32 s69, s93, 0\n"
      "buffer_load_dwordx4 v[136:139], v197, s[8:11], s69 offen offset:2048 sc1\n"
      "v_mfma_f32_16x16x32_bf16 v[32:35], a[32:35], v[144:147], v[32:35]\n"
      "v_mfma_f32_16x16x32_bf16 v[32:35], a[48:51], v[148:151], v[32:35]\n"
      "v_mfma_f32_16x16x32_bf16 v[36:39], a[36:39], v[144:147], v[36:39]\n"
      "Lhs2_2_%=:\n"
      "s_cbranch_execz Lhr2_%=\n"
      "v_mfma_f32_16x16x32_bf16 v[36:39], a[52:55], v[148:151], v[36:39]\n"
      "s_add_u32 s69, s93, 7168\n"
      "buffer_load_dwordx4 v[140:143], v197, s[8:11], s69 offen offset:2048 sc1\n"
      "s_waitcnt vmcnt(11)\n"
      "v_mfma_f32_16x16x32_bf16 v[96:99], a[40:43], v[152:155], v[96:99]\n"
      "s_waitcnt vmcnt(10)\n"
      "v_mfma_f32_16x16x32_bf16 v[96:99], a[56:59], v[156:159], v[96:99]\n"
      "v_mfma_f32_16x16x32_bf16 v[100:103], a[44:47], v[152:155], v[100:103]\n"
      "v_mfma_f32_16x16x32_bf16 v[100:103], a[60:63], v[156:159], v[100:103]\n"
      "s_add_u32 s69, s93, 4096\n"
      "buffer_load_dwordx4 v[144:147], v196, s[8:11], s69 offen offset:0 sc1\n"
      "v_mfma_f32_16x16x32_bf16 v[40:43], a[32:35], v[152:155], v[40:43]\n"
      "v_mfma_f32_16x16x32_bf16 v[40:43], a[48:51], v[156:159], v[40:43]\n"
      "v_mfma_f32_16x16x32_bf16 v[44:47], a[36:39], v[152:155], v[44:47]\n"
      "v_mfma_f32_16x16x32_bf16 v[44:47], a[52:55], v[156:159], v[44:47]\n"
      "s_add_u32 s69, s93, 11264\n"
      "buffer_load_dwordx4 v[148:151], v196, s[8:11], s69 offen offset:0 sc1\n"
      "s_waitcnt vmcnt(11)\n"
      "v_mfma_f32_16x16x32_bf16 v[104:107], a[40:43], v[160:163], v[104:107]\n"
      "s_waitcnt vmcnt(10)\n"
      "v_mfma_f32_16x16x32_bf16 v[104:107], a[56:59], v[164:167], v[104:107]\n"
      "v_mfma_f32_16x16x32_bf16 v[108:111], a[44:47], v[160:163], v[108:111]\n"
      "v_mfma_f32_16x16x32_bf16 v[108:111], a[60:63], v[164:167], v[108:111]\n"
      "s_add_u32 s69, s93, 4096\n"
      "buffer_load_dwordx4 v[152:155], v197, s[8:11], s69 offen offset:0 sc1\n"
      "v_mfma_f32_16x16x32_bf16 v[48:51], a[32:35], v[160:163], v[48:51]\n"
      "v_mfma_f32_16x16x32_bf16 v[48:51], a[48:51], v[164:167], v[48:51]\n"
      "v_mfma_f32_16x16x32_bf16 v[52:55], a[36:39], v[160:163], v[52:55]\n"
      "v_mfma_f32_16x16x32_bf16 v[52:55], a[52:55], v[164:167], v[52:55]\n"
      "s_add_u32 s69, s93, 11264\n"
      "buffer_load_dwordx4 v[156:159], v197, s[8:11], s69 offen offset:0 sc1\n"
      "s_waitcnt vmcnt(11)\n"
      "s_barrier\n"
      "v_mfma_f32_16x16x32_bf16 v[56:59], a[72:75], v[112:115], v[56:59]\n"
      "s_waitcnt vmcnt(10)\n"
      "ds_read_b64_tr_b16 a[0:1], v198 offset:0\n"
      "v_mfma_f32_16x16x32_bf16 v[56:59], a[88:91], v[116:119], v[56:59]\n"
      "ds_read_b64_tr_b16 a[2:3], v198 offset:512\n"
      "v_mfma_f32_16x16x32_bf16 v[60:63], a[76:79], v[112:115], v[60:63]\n"
      "ds_read_b64_tr_b16 a[4:5], v198 offset:2048\n"
      "v_mfma_f32_16x16x32_bf16 v[60:63], a[92:95], v[116:119], v[60:63]\n"
      "s_add_u32 s69, s93, 4096\n"
      "buffer_load_dwordx4 v[160:163], v254, s[8:11], s69 offen offset:2048 sc1\n"
      "v_mfma_f32_16x16x32_bf16 v[0:3], a[64:67], v[112:115], v[0:3]\n"
      "ds_read_b64_tr_b16 a[6:7], v198 offset:2560\n"
      "v_mfma_f32_16x16x32_bf16 v[0:3], a[80:83], v[116:119], v[0:3]\n"
      "ds_read_b64_tr_b16 a[8:9], v198 offset:4096\n"
      "v_mfma_f32_16x16x32_bf16 v[4:7], a[68:71], v[112:115], v[4:7]\n"
      "ds_read_b64_tr_b16 a[10:11], v198 offset:4608\n"
      "v_mfma_f32_16x16x32_bf16 v[4:7], a[84:87], v[116:119], v[4:7]\n"
      "s_add_u32 s69, s93, 11264\n"
      "buffer_load_dwordx4 v[164:167], v254, s[8:11], s69 offen offset:2048 sc1\n"
      "s_waitcnt vmcnt(11)\n"
      "v_mfma_f32_16x16x32_bf16 v[64:67], a[72:75], v[120:123], v[64:67]\n"
      "s_waitcnt vmcnt(10)\n"
      "ds_read_b64_tr_b16 a[12:13], v198 offset:6144\n"
      "v_mfma_f32_16x16x32_bf16 v[64:67], a[88:91], v[124:127], v[64:67]\n"
      "ds_read_b64_tr_b16 a[14:15], v198 offset:6656\n"
      "v_mfma_f32_16x16x32_bf16 v[68:71], a[76:79], v[120:123], v[68:71]\n"
      "ds_read_b64_tr_b16 a[16:17], v198 offset:8192\n"
      "v_mfma_f32_16x16x32_bf16 v[68:71], a[92:95], v[124:127], v[68:71]\n"
      "s_add_u32 s69, s93, 14336\n"
      "buffer_load_dwordx4 v[112:115], v196, s[8:11], s69 offen offset:0 sc1\n"
      "v_mfma_f32_16x16x32_bf16 v[8:11], a[64:67], v[120:123], v[8:11]\n"
      "ds_read_b64_tr_b16 a[18:19], v198 offset:8704\n"
      "v_mfma_f32_16x16x32_bf16 v[8:11], a[80:83], v[124:127], v[8:11]\n"
      "ds_read_b64_tr_b16 a[20:21], v198 offset:10240\n"
      "v_mfma_f32_16x16x32_bf16 v[12:15], a[68:71], v[120:123], v[12:15]\n"
      "ds_read_b64_tr_b16 a[22:23], v198 offset:10752\n"
      "v_mfma_f32_16x16x32_bf16 v[12:15], a[84:87], v[124:127], v[12:15]\n"
      "s_add_u32 s69, s93, 21504\n"
      "buffer_load_dwordx4 v[116:119], v196, s[8:11], s69 offen offset:0 sc1\n"
      "s_waitcnt vmcnt(11)\n"
      "v_mfma_f32_16x16x32_bf16 v[72:75], a[72:75], v[128:131], v[72:75]\n"
      "s_waitcnt vmcnt(10)\n"
      "ds_read_b64_tr_b16 a[24:25], v198 offset:12288\n"
      "v_mfma_f32_16x16x32_bf16 v[72:75], a[88:91], v[132:135], v[72:75]\n"
      "ds_read_b64_tr_b16 a[26:27], v198 offset:12800\n"
      "v_mfma_f32_16x16x32_bf16 v[76:79], a[76:79], v[128:131], v[76:79]\n"
      "ds_read_b64_tr_b16 a[28:29], v198 offset:14336\n"
      "v_mfma_f32_16x16x32_bf16 v[76:79], a[92:95], v[132:135], v[76:79]\n"
      "s_add_u32 s69, s93, 14336\n"
      "buffer_load_dwordx4 v[120:123], v197, s[8:11], s69 offen offset:0 sc1\n"
      "v_mfma_f32_16x16x32_bf16 v[16:19], a[64:67], v[128:131], v[16:19]\n"
      "ds_read_b64_tr_b16 a[30:31], v198 offset:14848\n"
      "v_mfma_f32_16x16x32_bf16 v[16:19], a[80:83], v[132:135], v[16:19]\n"
      "ds_read_b64_tr_b16 a[32:33], v198 offset:16384\n"
      "v_mfma_f32_16x16x32_bf16 v[20:23], a[68:71], v[128:131], v[20:23]\n"
      "ds_read_b64_tr_b16 a[34:35], v198 offset:16896\n"
      "v_mfma_f32_16x16x32_bf16 v[20:23], a[84:87], v[132:135], v[20:23]\n"
      "s_add_u32 s69, s93, 21504\n"
      "buffer_load_dwordx4 v[124:127], v197, s[8:11], s69 offen offset:0 sc1\n"
      "s_waitcnt vmcnt(11)\n"
      "v_mfma_f32_16x16x32_bf16 v[80:83], a[72:75], v[136:139], v[80:83]\n"
      "s_waitcnt vmcnt(10)\n"
      "ds_read_b64_tr_b16 a[36:37], v198 offset:18432\n"
      "v_mfma_f32_16x16x32_bf16 v[80:83], a[88:91], v[140:143], v[80:83]\n"
      "ds_read_b64_tr_b16 a[38:39], v198 offset:18944\n"
      "v_mfma_f32_16x16x32_bf16 v[84:87], a[76:79], v[136:139], v[84:87]\n"
      "ds_read_b64_tr_b16 a[40:41], v198 offset:20480\n"
      "v_mfma_f32_16x16x32_bf16 v[84:87], a[92:95], v[140:143], v[84:87]\n"
      "s_add_u32 s69, s93, 14336\n"
      "buffer_load_dwordx4 v[128:131], v196, s[8:11], s69 offen offset:2048 sc1\n"
      "v_mfma_f32_16x16x32_bf16 v[24:27], a[64:67], v[136:139], v[24:27]\n"
      "ds_read_b64_tr_b16 a[42:43], v198 offset:20992\n"
      "v_mfma_f32_16x16x32_bf16 v[24:27], a[80:83], v[140:143], v[24:27]\n"
      "ds_read_b64_tr_b16 a[44:45], v198 offset:22528\n"
      "v_mfma_f32_16x16x32_bf16 v[28:31], a[68:71], v[136:139], v[28:31]\n"
      "ds_read_b64_tr_b16 a[46:47], v198 offset:23040\n"
      "v_mfma_f32_16x16x32_bf16 v[28:31], a[84:87], v[140:143], v[28:31]\n"
      "s_add_u32 s69, s93, 21504\n"
      "buffer_load_dwordx4 v[132:135], v196, s[8:11], s69 offen offset:2048 sc1\n"
      "s_waitcnt vmcnt(11)\n"
      "v_mfma_f32_16x16x32_bf16 v[88:91], a[72:75], v[144:147], v[88:91]\n"
      "s_waitcnt vmcnt(10)\n"
      "ds_read_b64_tr_b16 a[48:49], v198 offset:24576\n"
      "v_mfma_f32_16x16x32_bf16 v[88:91], a[88:91], v[148:151], v[88:91]\n"
      "ds_read_b64_tr_b16 a[50:51], v198 offset:25088\n"
      "v_mfma_f32_16x16x32_bf16 v[92:95], a[76:79], v[144:147], v[92:95]\n"
      "ds_read_b64_tr_b16 a[52:53], v198 offset:26624\n"
      "v_mfma_f32_16x16x32_bf16 v[92:95], a[92:95], v[148:151], v[92:95]\n"
      "s_add_u32 s69, s93, 14336\n"
      "buffer_load_dwordx4 v[136:139], v197, s[8:11], s69 offen offset:2048 sc1\n"
      "v_mfma_f32_16x16x32_bf16 v[32:35], a[64:67], v[144:147], v[32:35]\n"
      "ds_read_b64_tr_b16 a[54:55], v198 offset:27136\n"
      "v_mfma_f32_16x16x32_bf16 v[32:35], a[80:83], v[148:151], v[32:35]\n"
      "ds_read_b64_tr_b16 a[56:57], v198 offset:28672\n"
      "v_mfma_f32_16x16x32_bf16 v[36:39], a[68:71], v[144:147], v[36:39]\n"
      "ds_read_b64_tr_b16 a[58:59], v198 offset:29184\n"
      "v_mfma_f32_16x16x32_bf16 v[36:39], a[84:87], v[148:151], v[36:39]\n"
      "s_add_u32 s69, s93, 21504\n"
      "buffer_load_dwordx4 v[140:143], v197, s[8:11], s69 offen offset:2048 sc1\n"
      "s_waitcnt vmcnt(11)\n"
      "v_mfma_f32_16x16x32_bf16 v[96:99], a[72:75], v[152:155], v[96:99]\n"
      "s_waitcnt vmcnt(10)\n"
      "ds_read_b64_tr_b16 a[60:61], v198 offset:30720\n"
      "v_mfma_f32_16x16x32_bf16 v[96:99], a[88:91], v[156:159], v[96:99]\n"
      "ds_read_b64_tr_b16 a[62:63], v198 offset:31232\n"
      "v_mfma_f32_16x16x32_bf16 v[100:103], a[76:79], v[152:155], v[100:103]\n"
      "v_mfma_f32_16x16x32_bf16 v[100:103], a[92:95], v[156:159], v[100:103]\n"
      "s_add_u32 s69, s93, 18432\n"
      "buffer_load_dwordx4 v[144:147], v196, s[8:11], s69 offen offset:0 sc1\n"
      "v_mfma_f32_16x16x32_bf16 v[40:43], a[64:67], v[152:155], v[40:43]\n"
      "v_mfma_f32_16x16x32_bf16 v[40:43], a[80:83], v[156:159], v[40:43]\n"
      "v_mfma_f32_16x16x32_bf16 v[44:47], a[68:71], v[152:155], v[44:47]\n"
      "v_mfma_f32_16x16x32_bf16 v[44:47], a[84:87], v[156:159], v[44:47]\n"
      "s_add_u32 s69, s93, 25600\n"
      "buffer_load_dwordx4 v[148:151], v196, s[8:11], s69 offen offset:0 sc1\n"
      "s_waitcnt vmcnt(11)\n"
      "v_mfma_f32_16x16x32_bf16 v[104:107], a[72:75], v[160:163], v[104:107]\n"
      "s_waitcnt vmcnt(10)\n"
      "v_mfma_f32_16x16x32_bf16 v[104:107], a[88:91], v[164:167], v[104:107]\n"
      "v_mfma_f32_16x16x32_bf16 v[108:111], a[76:79], v[160:163], v[108:111]\n"
      "v_mfma_f32_16x16x32_bf16 v[108:111], a[92:95], v[164:167], v[108:111]\n"
      "s_add_u32 s69, s93, 18432\n"
      "buffer_load_dwordx4 v[152:155], v197, s[8:11], s69 offen offset:0 sc1\n"
      "v_mfma_f32_16x16x32_bf16 v[48:51], a[64:67], v[160:163], v[48:51]\n"
      "v_mfma_f32_16x16x32_bf16 v[48:51], a[80:83], v[164:167], v[48:51]\n"
      "v_mfma_f32_16x16x32_bf16 v[52:55], a[68:71], v[160:163], v[52:55]\n"
      "v_mfma_f32_16x16x32_bf16 v[52:55], a[84:87], v[164:167], v[52:55]\n"
      "s_add_u32 s69, s93, 25600\n"
      "buffer_load_dwordx4 v[156:159], v197, s[8:11], s69 offen offset:0 sc1\n"
      "s_waitcnt vmcnt(11)\n"
      "buffer_load_dwordx4 v[216:219], v212, s[48:51], s74 offen\n"
      "v_mfma_f32_16x16x32_bf16 v[56:59], a[104:107], v[112:115], v[56:59]\n"
      "s_waitcnt vmcnt(11)\n"
      "ds_read_b64_tr_b16 a[64:65], v198 offset:32768\n"
      "v_mfma_f32_16x16x32_bf16 v[56:59], a[120:123], v[116:119], v[56:59]\n"
      "ds_read_b64_tr_b16 a[66:67], v198 offset:33280\n"
      "v_mfma_f32_16x16x32_bf16 v[60:63], a[108:111], v[112:115], v[60:63]\n"
      "ds_read_b64_tr_b16 a[68:69], v198 offset:34816\n"
      "v_mfma_f32_16x16x32_bf16 v[60:63], a[124:127], v[116:119], v[60:63]\n"
      "s_add_u32 s69, s93, 18432\n"
      "buffer_load_dwordx4 v[160:163], v254, s[8:11], s69 offen offset:2048 sc1\n"
      "v_mfma_f32_16x16x32_bf16 v[0:3], a[96:99], v[112:115], v[0:3]\n"
      "ds_read_b64_tr_b16 a[70:71], v198 offset:35328\n"
      "v_mfma_f32_16x16x32_bf16 v[0:3], a[112:115], v[116:119], v[0:3]\n"
      "ds_read_b64_tr_b16 a[72:73], v198 offset:36864\n"
      "v_mfma_f32_16x16x32_bf16 v[4:7], a[100:103], v[112:115], v[4:7]\n"
      "ds_read_b64_tr_b16 a[74:75], v198 offset:37376\n"
      "v_mfma_f32_16x16x32_bf16 v[4:7], a[116:119], v[116:119], v[4:7]\n"
      "s_add_u32 s69, s93, 25600\n"
      "buffer_load_dwordx4 v[164:167], v254, s[8:11], s69 offen offset:2048 sc1\n"
      "s_waitcnt vmcnt(12)\n"
      "v_mfma_f32_16x16x32_bf16 v[64:67], a[104:107], v[120:123], v[64:67]\n"
      "s_waitcnt vmcnt(11)\n"
      "ds_read_b64_tr_b16 a[76:77], v198 offset:38912\n"
      "v_mfma_f32_16x16x32_bf16 v[64:67], a[120:123], v[124:127], v[64:67]\n"
      "ds_read_b64_tr_b16 a[78:79], v198 offset:39424\n"
      "v_mfma_f32_16x16x32_bf16 v[68:71], a[108:111], v[120:123], v[68:71]\n"
      "ds_read_b64_tr_b16 a[80:81], v198 offset:40960\n"
      "v_mfma_f32_16x16x32_bf16 v[68:71], a[124:127], v[124:127], v[68:71]\n"
      "s_add_u32 s69, s94, 0\n"
      "buffer_load_dwordx4 v[112:115], v196, s[8:11], s69 offen offset:0 sc1\n"
      "v_mfma_f32_16x16x32_bf16 v[8:11], a[96:99], v[120:123], v[8:11]\n"
      "ds_read_b64_tr_b16 a[82:83], v198 offset:41472\n"
      "v_mfma_f32_16x16x32_bf16 v[8:11], a[112:115], v[124:127], v[8:11]\n"
      "ds_read_b64_tr_b16 a[84:85], v198 offset:43008\n"
      "v_mfma_f32_16x16x32_bf16 v[12:15], a[100:103], v[120:123], v[12:15]\n"
      "ds_read_b64_tr_b16 a[86:87], v198 offset:43520\n"
      "v_mfma_f32_16x16x32_bf16 v[12:15], a[116:119], v[124:127], v[12:15]\n"
      "s_add_u32 s69, s94, 7168\n"
      "buffer_load_dwordx4 v[116:119], v196, s[8:11], s69 offen offset:0 sc1\n"
      "s_waitcnt vmcnt(12)\n"
      "v_mfma_f32_16x16x32_bf16 v[72:75], a[104:107], v[128:131], v[72:75]\n"
      "s_waitcnt vmcnt(11)\n"
      "ds_read_b64_tr_b16 a[88:89], v198 offset:45056\n"
      "v_mfma_f32_16x16x32_bf16 v[72:75], a[120:123], v[132:135], v[72:75]\n"
      "ds_read_b64_tr_b16 a[90:91], v198 offset:45568\n"
      "v_mfma_f32_16x16x32_bf16 v[76:79], a[108:111], v[128:131], v[76:79]\n"
      "ds_read_b64_tr_b16 a[92:93], v198 offset:47104\n"
      "v_mfma_f32_16x16x32_bf16 v[76:79], a[124:127], v[132:135], v[76:79]\n"
      "s_add_u32 s69, s94, 0\n"
      "buffer_load_dwordx4 v[120:123], v197, s[8:11], s69 offen offset:0 sc1\n"
      "v_mfma_f32_16x16x32_bf16 v[16:19], a[96:99], v[128:131], v[16:19]\n"
      "ds_read_b64_tr_b16 a[94:95], v198 offset:47616\n"
      "v_mfma_f32_16x16x32_bf16 v[16:19], a[112:115], v[132:135], v[16:19]\n"
      "v_mfma_f32_16x16x32_bf16 v[20:23], a[100:103], v[128:131], v[20:23]\n"
      "v_mfma_f32_16x16x32_bf16 v[20:23], a[116:119], v[132:135], v[20:23]\n"
      "s_add_u32 s69, s94, 7168\n"
      "buffer_load_dwordx4 v[124:127], v197, s[8:11], s69 offen offset:0 sc1\n"
      "s_waitcnt vmcnt(12)\n"
      "v_mfma_f32_16x16x32_bf16 v[80:83], a[104:107], v[136:139], v[80:83]\n"
      "s_waitcnt vmcnt(11)\n"
      "v_mfma_f32_16x16x32_bf16 v[80:83], a[120:123], v[140:143], v[80:83]\n"
      "v_mfma_f32_16x16x32_bf16 v[84:87], a[108:111], v[136:139], v[84:87]\n"
      "v_mfma_f32_16x16x32_bf16 v[84:87], a[124:127], v[140:143], v[84:87]\n"
      "s_add_u32 s69, s94, 0\n"
      "buffer_load_dwordx4 v[128:131], v196, s[8:11], s69 offen offset:2048 sc1\n"
      "v_mfma_f32_16x16x32_bf16 v[24:27], a[96:99], v[136:139], v[24:27]\n"
      "v_mfma_f32_16x16x32_bf16 v[24:27], a[112:115], v[140:143], v[24:27]\n"
      "v_mfma_f32_16x16x32_bf16 v[28:31], a[100:103], v[136:139], v[28:31]\n"
      "v_mfma_f32_16x16x32_bf16 v[28:31], a[116:119], v[140:143], v[28:31]\n"
      "s_add_u32 s69, s94, 7168\n"
      "buffer_load_dwordx4 v[132:135], v196, s[8:11], s69 offen offset:2048 sc1\n"
      "s_waitcnt vmcnt(12)\n"
      "v_mfma_f32_16x16x32_bf16 v[88:91], a[104:107], v[144:147], v[88:91]\n"
      "s_waitcnt vmcnt(11)\n"
      "v_mfma_f32_16x16x32_bf16 v[88:91], a[120:123], v[148:151], v[88:91]\n"
      "v_mfma_f32_16x16x32_bf16 v[92:95], a[108:111], v[144:147], v[92:95]\n"
      "v_mfma_f32_16x16x32_bf16 v[92:95], a[124:127], v[148:151], v[92:95]\n"
      "s_add_u32 s69, s94, 0\n"
      "buffer_load_dwordx4 v[136:139], v197, s[8:11], s69 offen offset:2048 sc1\n"
      "v_mfma_f32_16x16x32_bf16 v[32:35], a[96:99], v[144:147], v[32:35]\n"
      "v_mfma_f32_16x16x32_bf16 v[32:35], a[112:115], v[148:151], v[32:35]\n"
      "v_mfma_f32_16x16x32_bf16 v[36:39], a[100:103], v[144:147], v[36:39]\n"
      "v_mfma_f32_16x16x32_bf16 v[36:39], a[116:119], v[148:151], v[36:39]\n"
      "s_add_u32 s69, s94, 7168\n"
      "buffer_load_dwordx4 v[140:143], v197, s[8:11], s69 offen offset:2048 sc1\n"
      "s_waitcnt vmcnt(12)\n"
      "v_mfma_f32_16x16x32_bf16 v[96:99], a[104:107], v[152:155], v[96:99]\n"
      "s_waitcnt vmcnt(11)\n"
      "v_mfma_f32_16x16x32_bf16 v[96:99], a[120:123], v[156:159], v[96:99]\n"
      "v_mfma_f32_16x16x32_bf16 v[100:103], a[108:111], v[152:155], v[100:103]\n"
      "v_mfma_f32_16x16x32_bf16 v[100:103], a[124:127], v[156:159], v[100:103]\n"
      "s_add_u32 s69, s94, 4096\n"
      "buffer_load_dwordx4 v[144:147], v196, s[8:11], s69 offen offset:0 sc1\n"
      "v_mfma_f32_16x16x32_bf16 v[40:43], a[96:99], v[152:155], v[40:43]\n"
      "v_mfma_f32_16x16x32_bf16 v[40:43], a[112:115], v[156:159], v[40:43]\n"
      "v_mfma_f32_16x16x32_bf16 v[44:47], a[100:103], v[152:155], v[44:47]\n"
      "v_mfma_f32_16x16x32_bf16 v[44:47], a[116:119], v[156:159], v[44:47]\n"
      "s_add_u32 s69, s94, 11264\n"
      "buffer_load_dwordx4 v[148:151], v196, s[8:11], s69 offen offset:0 sc1\n"
      "s_waitcnt vmcnt(11)\n"
      "v_mfma_f32_16x16x32_bf16 v[104:107], a[104:107], v[160:163], v[104:107]\n"
      "s_waitcnt vmcnt(10)\n"
      "v_mfma_f32_16x16x32_bf16 v[104:107], a[120:123], v[164:167], v[104:107]\n"
      "v_mfma_f32_16x16x32_bf16 v[108:111], a[108:111], v[160:163], v[108:111]\n"
      "v_mfma_f32_16x16x32_bf16 v[108:111], a[124:127], v[164:167], v[108:111]\n"
      "s_add_u32 s69, s94, 4096\n"
      "buffer_load_dwordx4 v[152:155], v197, s[8:11], s69 offen offset:0 sc1\n"
      "v_mfma_f32_16x16x32_bf16 v[48:51], a[96:99], v[160:163], v[48:51]\n"
      "v_mfma_f32_16x16x32_bf16 v[48:51], a[112:115], v[164:167], v[48:51]\n"
      "v_mfma_f32_16x16x32_bf16 v[52:55], a[100:103], v[160:163], v[52:55]\n"
      "v_mfma_f32_16x16x32_bf16 v[52:55], a[116:119], v[164:167], v[52:55]\n"
      "s_add_u32 s69, s94, 11264\n"
      "buffer_load_dwordx4 v[156:159], v197, s[8:11], s69 offen offset:0 sc1\n"
      "s_waitcnt vmcnt(11)\n"
      "v_mfma_f32_16x16x32_bf16 v[56:59], a[136:139], v[112:115], v[56:59]\n"
      "s_waitcnt vmcnt(10)\n"
      "ds_read_b64_tr_b16 a[96:97], v198 offset:49152\n"
      "v_mfma_f32_16x16x32_bf16 v[56:59], a[152:155], v[116:119], v[56:59]\n"
      "ds_read_b64_tr_b16 a[98:99], v198 offset:49664\n"
      "v_mfma_f32_16x16x32_bf16 v[60:63], a[140:143], v[112:115], v[60:63]\n"
      "ds_read_b64_tr_b16 a[100:101], v198 offset:51200\n"
      "v_mfma_f32_16x16x32_bf16 v[60:63], a[156:159], v[116:119], v[60:63]\n"
      "s_add_u32 s69, s94, 4096\n"
      "buffer_load_dwordx4 v[160:163], v254, s[8:11], s69 offen offset:2048 sc1\n"
      "v_mfma_f32_16x16x32_bf16 v[0:3], a[128:131], v[112:115], v[0:3]\n"
      "ds_read_b64_tr_b16 a[102:103], v198 offset:51712\n"
      "v_mfma_f32_16x16x32_bf16 v[0:3], a[144:147], v[116:119], v[0:3]\n"
      "ds_read_b64_tr_b16 a[104:105], v198 offset:53248\n"
      "v_mfma_f32_16x16x32_bf16 v[4:7], a[132:135], v[112:115], v[4:7]\n"
      "ds_read_b64_tr_b16 a[106:107], v198 offset:53760\n"
      "v_mfma_f32_16x16x32_bf16 v[4:7], a[148:151], v[116:119], v[4:7]\n"
      "s_add_u32 s69, s94, 11264\n"
      "buffer_load_dwordx4 v[164:167], v254, s[8:11], s69 offen offset:2048 sc1\n"
      "s_waitcnt vmcnt(11)\n"
      "v_mfma_f32_16x16x32_bf16 v[64:67], a[136:139], v[120:123], v[64:67]\n"
      "s_waitcnt vmcnt(10)\n"
      "ds_read_b64_tr_b16 a[108:109], v198 offset:55296\n"
      "v_mfma_f32_16x16x32_bf16 v[64:67], a[152:155], v[124:127], v[64:67]\n"
      "ds_read_b64_tr_b16 a[110:111], v198 offset:55808\n"
      "v_mfma_f32_16x16x32_bf16 v[68:71], a[140:143], v[120:123], v[68:71]\n"
      "ds_read_b64_tr_b16 a[112:113], v199 offset:0\n"
      "v_mfma_f32_16x16x32_bf16 v[68:71], a[156:159], v[124:127], v[68:71]\n"
      "s_add_u32 s69, s94, 14336\n"
      "buffer_load_dwordx4 v[112:115], v196, s[8:11], s69 offen offset:0 sc1\n"
      "v_mfma_f32_16x16x32_bf16 v[8:11], a[128:131], v[120:123], v[8:11]\n"
      "ds_read_b64_tr_b16 a[114:115], v199 offset:512\n"
      "v_mfma_f32_16x16x32_bf16 v[8:11], a[144:147], v[124:127], v[8:11]\n"
      "ds_read_b64_tr_b16 a[116:117], v199 offset:2048\n"
      "v_mfma_f32_16x16x32_bf16 v[12:15], a[132:135], v[120:123], v[12:15]\n"
      "ds_read_b64_tr_b16 a[118:119], v199 offset:2560\n"
      "v_mfma_f32_16x16x32_bf16 v[12:15], a[148:151], v[124:127], v[12:15]\n"
      "s_add_u32 s69, s94, 21504\n"
      "buffer_load_dwordx4 v[116:119], v196, s[8:11], s69 offen offset:0 sc1\n"
      "s_waitcnt vmcnt(11)\n"
      "v_mfma_f32_16x16x32_bf16 v[72:75], a[136:139], v[128:131], v[72:75]\n"
      "s_waitcnt vmcnt(10)\n"
      "ds_read_b64_tr_b16 a[120:121], v199 offset:4096\n"
      "v_mfma_f32_16x16x32_bf16 v[72:75], a[152:155], v[132:135], v[72:75]\n"
      "ds_read_b64_tr_b16 a[122:123], v199 offset:4608\n"
      "v_mfma_f32_16x16x32_bf16 v[76:79], a[140:143], v[128:131], v[76:79]\n"
      "ds_read_b64_tr_b16 a[124:125], v199 offset:6144\n"
      "v_mfma_f32_16x16x32_bf16 v[76:79], a[156:159], v[132:135], v[76:79]\n"
      "s_add_u32 s69, s94, 14336\n"
      "buffer_load_dwordx4 v[120:123], v197, s[8:11], s69 offen offset:0 sc1\n"
      "v_mfma_f32_16x16x32_bf16 v[16:19], a[128:131], v[128:131], v[16:19]\n"
      "ds_read_b64_tr_b16 a[126:127], v199 offset:6656\n"
      "v_mfma_f32_16x16x32_bf16 v[16:19], a[144:147], v[132:135], v[16:19]\n"
      "v_mfma_f32_16x16x32_bf16 v[20:23], a[132:135], v[128:131], v[20:23]\n"
      "v_mfma_f32_16x16x32_bf16 v[20:23], a[148:151], v[132:135], v[20:23]\n"
      "s_add_u32 s69, s94, 21504\n"
      "buffer_load_dwordx4 v[124:127], v197, s[8:11], s69 offen offset:0 sc1\n"
      "s_waitcnt vmcnt(11)\n"
      "v_mfma_f32_16x16x32_bf16 v[80:83], a[136:139], v[136:139], v[80:83]\n"
      "s_waitcnt vmcnt(10)\n"
      "v_mfma_f32_16x16x32_bf16 v[80:83], a[152:155], v[140:143], v[80:83]\n"
      "v_mfma_f32_16x16x32_bf16 v[84:87], a[140:143], v[136:139], v[84:87]\n"
      "v_mfma_f32_16x16x32_bf16 v[84:87], a[156:159], v[140:143], v[84:87]\n"
      "s_add_u32 s69, s94, 14336\n"
      "buffer_load_dwordx4 v[128:131], v196, s[8:11], s69 offen offset:2048 sc1\n"
      "v_mfma_f32_16x16x32_bf16 v[24:27], a[128:131], v[136:139], v[24:27]\n"
      "v_mfma_f32_16x16x32_bf16 v[24:27], a[144:147], v[140:143], v[24:27]\n"
      "v_mfma_f32_16x16x32_bf16 v[28:31], a[132:135], v[136:139], v[28:31]\n"
      "v_mfma_f32_16x16x32_bf16 v[28:31], a[148:151], v[140:143], v[28:31]\n"
      "s_add_u32 s69, s94, 21504\n"
      "buffer_load_dwordx4 v[132:135], v196, s[8:11], s69 offen offset:2048 sc1\n"
      "s_waitcnt vmcnt(11)\n"
      "v_mfma_f32_16x16x32_bf16 v[88:91], a[136:139], v[144:147], v[88:91]\n"
      "s_waitcnt vmcnt(10)\n"
      "v_mfma_f32_16x16x32_bf16 v[88:91], a[152:155], v[148:151], v[88:91]\n"
      "v_mfma_f32_16x16x32_bf16 v[92:95], a[140:143], v[144:147], v[92:95]\n"
      "v_mfma_f32_16x16x32_bf16 v[92:95], a[156:159], v[148:151], v[92:95]\n"
      "s_add_u32 s69, s94, 14336\n"
      "buffer_load_dwordx4 v[136:139], v197, s[8:11], s69 offen offset:2048 sc1\n"
      "v_mfma_f32_16x16x32_bf16 v[32:35], a[128:131], v[144:147], v[32:35]\n"
      "v_mfma_f32_16x16x32_bf16 v[32:35], a[144:147], v[148:151], v[32:35]\n"
      "v_mfma_f32_16x16x32_bf16 v[36:39], a[132:135], v[144:147], v[36:39]\n"
      "v_mfma_f32_16x16x32_bf16 v[36:39], a[148:151], v[148:151], v[36:39]\n"
      "s_add_u32 s69, s94, 21504\n"
      "buffer_load_dwordx4 v[140:143], v197, s[8:11], s69 offen offset:2048 sc1\n"
      "s_waitcnt vmcnt(11)\n"
      "v_mfma_f32_16x16x32_bf16 v[96:99], a[136:139], v[152:155], v[96:99]\n"
      "s_waitcnt vmcnt(10)\n"
      "v_mfma_f32_16x16x32_bf16 v[96:99], a[152:155], v[156:159], v[96:99]\n"
      "v_mfma_f32_16x16x32_bf16 v[100:103], a[140:143], v[152:155], v[100:103]\n"
      "v_mfma_f32_16x16x32_bf16 v[100:103], a[156:159], v[156:159], v[100:103]\n"
      "s_add_u32 s69, s94, 18432\n"
      "buffer_load_dwordx4 v[144:147], v196, s[8:11], s69 offen offset:0 sc1\n"
      "v_mfma_f32_16x16x32_bf16 v[40:43], a[128:131], v[152:155], v[40:43]\n"
      "v_mfma_f32_16x16x32_bf16 v[40:43], a[144:147], v[156:159], v[40:43]\n"
      "v_mfma_f32_16x16x32_bf16 v[44:47], a[132:135], v[152:155], v[44:47]\n"
      "v_mfma_f32_16x16x32_bf16 v[44:47], a[148:151], v[156:159], v[44:47]\n"
      "s_add_u32 s69, s94, 25600\n"
      "buffer_load_dwordx4 v[148:151], v196, s[8:11], s69 offen offset:0 sc1\n"
      "s_waitcnt vmcnt(11)\n"
      "v_mfma_f32_16x16x32_bf16 v[104:107], a[136:139], v[160:163], v[104:107]\n"
      "s_waitcnt vmcnt(10)\n"
      "v_mfma_f32_16x16x32_bf16 v[104:107], a[152:155], v[164:167], v[104:107]\n"
      "v_mfma_f32_16x16x32_bf16 v[108:111], a[140:143], v[160:163], v[108:111]\n"
      "v_mfma_f32_16x16x32_bf16 v[108:111], a[156:159], v[164:167], v[108:111]\n"
      "s_add_u32 s69, s94, 18432\n"
      "buffer_load_dwordx4 v[152:155], v197, s[8:11], s69 offen offset:0 sc1\n"
      "v_mfma_f32_16x16x32_bf16 v[48:51], a[128:131], v[160:163], v[48:51]\n"
      "v_mfma_f32_16x16x32_bf16 v[48:51], a[144:147], v[164:167], v[48:51]\n"
      "v_mfma_f32_16x16x32_bf16 v[52:55], a[132:135], v[160:163], v[52:55]\n"
      "v_mfma_f32_16x16x32_bf16 v[52:55], a[148:151], v[164:167], v[52:55]\n"
      "s_add_u32 s69, s94, 25600\n"
      "buffer_load_dwordx4 v[156:159], v197, s[8:11], s69 offen offset:0 sc1\n"
      "s_waitcnt vmcnt(11)\n"
      "v_mfma_f32_16x16x32_bf16 v[56:59], a[168:171], v[112:115], v[56:59]\n"
      "s_waitcnt vmcnt(10)\n"
      "ds_read_b64_tr_b16 a[128:129], v199 offset:8192\n"
      "s_waitcnt vmcnt(28)\n"
      "v_lshlrev_b32 v168, 16, v216\n"
      "v_and_b32 v169, 0xffff0000, v216\n"
      "v_mfma_f32_16x16x32_bf16 v[56:59], a[184:187], v[116:119], v[56:59]\n"
      "ds_read_b64_tr_b16 a[130:131], v199 offset:8704\n"
      "v_lshlrev_b32 v170, 16, v217\n"
      "v_and_b32 v171, 0xffff0000, v217\n"
      "v_lshlrev_b32 v172, 16, v218\n"
      "v_mfma_f32_16x16x32_bf16 v[60:63], a[172:175], v[112:115], v[60:63]\n"
      "ds_read_b64_tr_b16 a[132:133], v199 offset:10240\n"
      "v_and_b32 v173, 0xffff0000, v218\n"
      "v_lshlrev_b32 v174, 16, v219\n"
      "v_and_b32 v175, 0xffff0000, v219\n"
      "v_mfma_f32_16x16x32_bf16 v[60:63], a[188:191], v[116:119], v[60:63]\n"
      "s_add_u32 s69, s94, 18432\n"
      "buffer_load_dwordx4 v[160:163], v254, s[8:11], s69 offen offset:2048 sc1\n"
      "v_mfma_f32_16x16x32_bf16 v[0:3], a[160:163], v[112:115], v[0:3]\n"
      "ds_read_b64_tr_b16 a[134:135], v199 offset:10752\n"
      "v_mfma_f32_16x16x32_bf16 v[0:3], a[176:179], v[116:119], v[0:3]\n"
      "ds_read_b64_tr_b16 a[136:137], v199 offset:12288\n"
      "v_mfma_f32_16x16x32_bf16 v[4:7], a[164:167], v[112:115], v[4:7]\n"
      "ds_read_b64_tr_b16 a[138:139], v199 offset:12800\n"
      "v_mfma_f32_16x16x32_bf16 v[4:7], a[180:183], v[116:119], v[4:7]\n"
      "s_add_u32 s69, s94, 25600\n"
      "buffer_load_dwordx4 v[164:167], v254, s[8:11], s69 offen offset:2048 sc1\n"
      "s_waitcnt vmcnt(11)\n"
      "v_mfma_f32_16x16x32_bf16 v[64:67], a[168:171], v[120:123], v[64:67]\n"
      "s_waitcnt vmcnt(10)\n"
      "ds_read_b64_tr_b16 a[140:141], v199 offset:14336\n"
      "v_mfma_f32_16x16x32_bf16 v[64:67], a[184:187], v[124:127], v[64:67]\n"
      "ds_read_b64_tr_b16 a[142:143], v199 offset:14848\n"
      "v_mfma_f32_16x16x32_bf16 v[68:71], a[172:175], v[120:123], v[68:71]\n"
      "ds_read_b64_tr_b16 a[144:145], v199 offset:16384\n"
      "v_mfma_f32_16x16x32_bf16 v[68:71], a[188:191], v[124:127], v[68:71]\n"
      "s_add_u32 s69, s95, 0\n"
      "buffer_load_dwordx4 v[112:115], v197, s[8:11], s69 offen offset:0 sc1\n"
      "v_mfma_f32_16x16x32_bf16 v[8:11], a[160:163], v[120:123], v[8:11]\n"
      "ds_read_b64_tr_b16 a[146:147], v199 offset:16896\n"
      "v_mfma_f32_16x16x32_bf16 v[8:11], a[176:179], v[124:127], v[8:11]\n"
      "ds_read_b64_tr_b16 a[148:149], v199 offset:18432\n"
      "v_mfma_f32_16x16x32_bf16 v[12:15], a[164:167], v[120:123], v[12:15]\n"
      "ds_read_b64_tr_b16 a[150:151], v199 offset:18944\n"
      "v_mfma_f32_16x16x32_bf16 v[12:15], a[180:183], v[124:127], v[12:15]\n"
      "s_add_u32 s69, s95, 7168\n"
      "buffer_load_dwordx4 v[116:119], v197, s[8:11], s69 offen offset:0 sc1\n"
      "s_waitcnt vmcnt(11)\n"
      "v_mfma_f32_16x16x32_bf16 v[72:75], a[168:171], v[128:131], v[72:75]\n"
      "s_waitcnt vmcnt(10)\n"
      "ds_read_b64_tr_b16 a[152:153], v199 offset:20480\n"
      "v_mfma_f32_16x16x32_bf16 v[72:75], a[184:187], v[132:135], v[72:75]\n"
      "ds_read_b64_tr_b16 a[154:155], v199 offset:20992\n"
      "v_mfma_f32_16x16x32_bf16 v[76:79], a[172:175], v[128:131], v[76:79]\n"
      "ds_read_b64_tr_b16 a[156:157], v199 offset:22528\n"
      "v_mfma_f32_16x16x32_bf16 v[76:79], a[188:191], v[132:135], v[76:79]\n"
      "s_add_u32 s69, s95, 14336\n"
      "buffer_load_dwordx4 v[120:123], v197, s[8:11], s69 offen offset:0 sc1\n"
      "v_mfma_f32_16x16x32_bf16 v[16:19], a[160:163], v[128:131], v[16:19]\n"
      "ds_read_b64_tr_b16 a[158:159], v199 offset:23040\n"
      "v_mfma_f32_16x16x32_bf16 v[16:19], a[176:179], v[132:135], v[16:19]\n"
      "v_mfma_f32_16x16x32_bf16 v[20:23], a[164:167], v[128:131], v[20:23]\n"
      "v_mfma_f32_16x16x32_bf16 v[20:23], a[180:183], v[132:135], v[20:23]\n"
      "s_add_u32 s69, s95, 21504\n"
      "buffer_load_dwordx4 v[124:127], v197, s[8:11], s69 offen offset:0 sc1\n"
      "s_waitcnt vmcnt(11)\n"
      "v_mfma_f32_16x16x32_bf16 v[80:83], a[168:171], v[136:139], v[80:83]\n"
      "s_waitcnt vmcnt(10)\n"
      "v_mfma_f32_16x16x32_bf16 v[80:83], a[184:187], v[140:143], v[80:83]\n"
      "v_mfma_f32_16x16x32_bf16 v[84:87], a[172:175], v[136:139], v[84:87]\n"
      "v_mfma_f32_16x16x32_bf16 v[84:87], a[188:191], v[140:143], v[84:87]\n"
      "s_add_u32 s69, s95, 0\n"
      "buffer_load_dwordx4 v[128:131], v196, s[8:11], s69 offen offset:0 sc1\n"
      "v_mfma_f32_16x16x32_bf16 v[24:27], a[160:163], v[136:139], v[24:27]\n"
      "v_mfma_f32_16x16x32_bf16 v[24:27], a[176:179], v[140:143], v[24:27]\n"
      "v_mfma_f32_16x16x32_bf16 v[28:31], a[164:167], v[136:139], v[28:31]\n"
      "v_mfma_f32_16x16x32_bf16 v[28:31], a[180:183], v[140:143], v[28:31]\n"
      "s_add_u32 s69, s95, 7168\n"
      "buffer_load_dwordx4 v[132:135], v196, s[8:11], s69 offen offset:0 sc1\n"
      "s_waitcnt vmcnt(11)\n"
      "v_mfma_f32_16x16x32_bf16 v[88:91], a[168:171], v[144:147], v[88:91]\n"
      "s_waitcnt vmcnt(10)\n"
      "v_mfma_f32_16x16x32_bf16 v[88:91], a[184:187], v[148:151], v[88:91]\n"
      "v_mfma_f32_16x16x32_bf16 v[92:95], a[172:175], v[144:147], v[92:95]\n"
      "v_mfma_f32_16x16x32_bf16 v[92:95], a[188:191], v[148:151], v[92:95]\n"
      "s_add_u32 s69, s95, 14336\n"
      "buffer_load_dwordx4 v[136:139], v196, s[8:11], s69 offen offset:0 sc1\n"
      "v_mfma_f32_16x16x32_bf16 v[32:35], a[160:163], v[144:147], v[32:35]\n"
      "v_mfma_f32_16x16x32_bf16 v[32:35], a[176:179], v[148:151], v[32:35]\n"
      "v_mfma_f32_16x16x32_bf16 v[36:39], a[164:167], v[144:147], v[36:39]\n"
      "v_mfma_f32_16x16x32_bf16 v[36:39], a[180:183], v[148:151], v[36:39]\n"
      "s_add_u32 s69, s95, 21504\n"
      "buffer_load_dwordx4 v[140:143], v196, s[8:11], s69 offen offset:0 sc1\n"
      "s_waitcnt vmcnt(11)\n"
      "v_mfma_f32_16x16x32_bf16 v[96:99], a[168:171], v[152:155], v[96:99]\n"
      "s_waitcnt vmcnt(10)\n"
      "v_mfma_f32_16x16x32_bf16 v[96:99], a[184:187], v[156:159], v[96:99]\n"
      "v_mfma_f32_16x16x32_bf16 v[100:103], a[172:175], v[152:155], v[100:103]\n"
      "v_mfma_f32_16x16x32_bf16 v[100:103], a[188:191], v[156:159], v[100:103]\n"
      "s_add_u32 s69, s95, 0\n"
      "buffer_load_dwordx4 v[144:147], v197, s[8:11], s69 offen offset:2048 sc1\n"
      "v_mfma_f32_16x16x32_bf16 v[40:43], a[160:163], v[152:155], v[40:43]\n"
      "v_mfma_f32_16x16x32_bf16 v[40:43], a[176:179], v[156:159], v[40:43]\n"
      "v_mfma_f32_16x16x32_bf16 v[44:47], a[164:167], v[152:155], v[44:47]\n"
      "v_mfma_f32_16x16x32_bf16 v[44:47], a[180:183], v[156:159], v[44:47]\n"
      "s_add_u32 s69, s95, 7168\n"
      "buffer_load_dwordx4 v[148:151], v197, s[8:11], s69 offen offset:2048 sc1\n"
      "s_waitcnt vmcnt(11)\n"
      "v_mfma_f32_16x16x32_bf16 v[104:107], a[168:171], v[160:163], v[104:107]\n"
      "s_waitcnt vmcnt(10)\n"
      "v_mfma_f32_16x16x32_bf16 v[104:107], a[184:187], v[164:167], v[104:107]\n"
      "v_mfma_f32_16x16x32_bf16 v[108:111], a[172:175], v[160:163], v[108:111]\n"
      "v_mfma_f32_16x16x32_bf16 v[108:111], a[188:191], v[164:167], v[108:111]\n"
      "s_add_u32 s69, s95, 14336\n"
      "buffer_load_dwordx4 v[152:155], v197, s[8:11], s69 offen offset:2048 sc1\n"
      "v_mfma_f32_16x16x32_bf16 v[48:51], a[160:163], v[160:163], v[48:51]\n"
      "v_mfma_f32_16x16x32_bf16 v[48:51], a[176:179], v[164:167], v[48:51]\n"
      "v_mfma_f32_16x16x32_bf16 v[52:55], a[164:167], v[160:163], v[52:55]\n"
      "v_mfma_f32_16x16x32_bf16 v[52:55], a[180:183], v[164:167], v[52:55]\n"
      "s_add_u32 s69, s95, 21504\n"
      "buffer_load_dwordx4 v[156:159], v197, s[8:11], s69 offen offset:2048 sc1\n"
      "s_waitcnt vmcnt(11)\n"
      "v_mfma_f32_16x16x32_bf16 v[64:67], a[200:203], v[112:115], v[64:67]\n"
      "s_waitcnt vmcnt(10)\n"
      "ds_read_b64_tr_b16 a[160:161], v199 offset:24576\n"
      "v_mfma_f32_16x16x32_bf16 v[64:67], a[216:219], v[116:119], v[64:67]\n"
      "ds_read_b64_tr_b16 a[162:163], v199 offset:25088\n"
      "v_mfma_f32_16x16x32_bf16 v[68:71], a[204:207], v[112:115], v[68:71]\n"
      "ds_read_b64_tr_b16 a[164:165], v199 offset:26624\n"
      "v_mfma_f32_16x16x32_bf16 v[68:71], a[220:223], v[116:119], v[68:71]\n"
      "s_add_u32 s69, s95, 0\n"
      "buffer_load_dwordx4 v[160:163], v196, s[8:11], s69 offen offset:2048 sc1\n"
      "v_mfma_f32_16x16x32_bf16 v[8:11], a[192:195], v[112:115], v[8:11]\n"
      "ds_read_b64_tr_b16 a[166:167], v199 offset:27136\n"
      "v_mfma_f32_16x16x32_bf16 v[8:11], a[208:211], v[116:119], v[8:11]\n"
      "v_mfma_f32_16x16x32_bf16 v[12:15], a[196:199], v[112:115], v[12:15]\n"
      "v_mfma_f32_16x16x32_bf16 v[12:15], a[212:215], v[116:119], v[12:15]\n"
      "s_add_u32 s69, s95, 7168\n"
      "buffer_load_dwordx4 v[164:167], v196, s[8:11], s69 offen offset:2048 sc1\n"
      "s_waitcnt vmcnt(11)\n"
      "v_mfma_f32_16x16x32_bf16 v[64:67], a[232:235], v[120:123], v[64:67]\n"
      "s_waitcnt vmcnt(10)\n"
      "v_mfma_f32_16x16x32_bf16 v[64:67], a[248:251], v[124:127], v[64:67]\n"
      "v_mfma_f32_16x16x32_bf16 v[68:71], a[236:239], v[120:123], v[68:71]\n"
      "v_mfma_f32_16x16x32_bf16 v[68:71], a[252:255], v[124:127], v[68:71]\n"
      "s_add_u32 s69, s95, 14336\n"
      "buffer_load_dwordx4 v[112:115], v196, s[8:11], s69 offen offset:2048 sc1\n"
      "v_mfma_f32_16x16x32_bf16 v[8:11], a[224:227], v[120:123], v[8:11]\n"
      "v_mfma_f32_16x16x32_bf16 v[8:11], a[240:243], v[124:127], v[8:11]\n"
      "v_mfma_f32_16x16x32_bf16 v[12:15], a[228:231], v[120:123], v[12:15]\n"
      "v_mfma_f32_16x16x32_bf16 v[12:15], a[244:247], v[124:127], v[12:15]\n"
      "s_add_u32 s69, s95, 21504\n"
      "buffer_load_dwordx4 v[116:119], v196, s[8:11], s69 offen offset:2048 sc1\n"
      "s_waitcnt vmcnt(11)\n"
      "s_waitcnt lgkmcnt(0)\n"
      "s_barrier\n"
      "v_mfma_f32_16x16x32_bf16 v[56:59], a[200:203], v[128:131], v[56:59]\n"
      "s_waitcnt vmcnt(10)\n"
      "v_mfma_f32_16x16x32_bf16 v[56:59], a[216:219], v[132:135], v[56:59]\n"
      "Lhs2_3_%=:\n"
      "s_cbranch_execz Lhr2_%=\n"
      "v_mfma_f32_16x16x32_bf16 v[60:63], a[204:207], v[128:131], v[60:63]\n"
      "v_mfma_f32_16x16x32_bf16 v[60:63], a[220:223], v[132:135], v[60:63]\n"
      "s_add_u32 s69, s95, 4096\n"
      "buffer_load_dwordx4 v[120:123], v254, s[8:11], s69 offen offset:2048 sc1\n"
      "ds_write_b128 v202, v[64:67] offset:0\n"
      "v_mfma_f32_16x16x32_bf16 v[0:3], a[192:195], v[128:131], v[0:3]\n"
      "ds_write_b128 v202, v[68:71] offset:1024\n"
      "v_mfma_f32_16x16x32_bf16 v[0:3], a[208:211], v[132:135], v[0:3]\n"
      "ds_write_b128 v200, v[8:11] offset:0\n"
      "v_mfma_f32_16x16x32_bf16 v[4:7], a[196:199], v[128:131], v[4:7]\n"
      "ds_write_b128 v200, v[12:15] offset:1024\n"
      "v_mfma_f32_16x16x32_bf16 v[4:7], a[212:215], v[132:135], v[4:7]\n"
      "s_add_u32 s69, s95, 11264\n"
      "buffer_load_dwordx4 v[124:127], v254, s[8:11], s69 offen offset:2048 sc1\n"
      "s_waitcnt vmcnt(11)\n"
      "v_mfma_f32_16x16x32_bf16 v[56:59], a[232:235], v[136:139], v[56:59]\n"
      "s_waitcnt vmcnt(10)\n"
      "v_mfma_f32_16x16x32_bf16 v[56:59], a[248:251], v[140:143], v[56:59]\n"
      "v_mfma_f32_16x16x32_bf16 v[60:63], a[236:239], v[136:139], v[60:63]\n"
      "v_mfma_f32_16x16x32_bf16 v[60:63], a[252:255], v[140:143], v[60:63]\n"
      "s_add_u32 s69, s95, 18432\n"
      "buffer_load_dwordx4 v[128:131], v254, s[8:11], s69 offen offset:2048 sc1\n"
      "v_mfma_f32_16x16x32_bf16 v[0:3], a[224:227], v[136:139], v[0:3]\n"
      "v_mfma_f32_16x16x32_bf16 v[0:3], a[240:243], v[140:143], v[0:3]\n"
      "v_mfma_f32_16x16x32_bf16 v[4:7], a[228:231], v[136:139], v[4:7]\n"
      "v_mfma_f32_16x16x32_bf16 v[4:7], a[244:247], v[140:143], v[4:7]\n"
      "s_add_u32 s69, s95, 25600\n"
      "buffer_load_dwordx4 v[132:135], v254, s[8:11], s69 offen offset:2048 sc1\n"
      "s_waitcnt vmcnt(11)\n"
      "v_mfma_f32_16x16x32_bf16 v[80:83], a[200:203], v[144:147], v[80:83]\n"
      "s_waitcnt vmcnt(10)\n"
      "v_mfma_f32_16x16x32_bf16 v[80:83], a[216:219], v[148:151], v[80:83]\n"
      "v_mfma_f32_16x16x32_bf16 v[84:87], a[204:207], v[144:147], v[84:87]\n"
      "v_mfma_f32_16x16x32_bf16 v[84:87], a[220:223], v[148:151], v[84:87]\n"
      "s_add_u32 s69, s95, 4096\n"
      "buffer_load_dwordx4 v[136:139], v197, s[8:11], s69 offen offset:0 sc1\n"
      "ds_write_b128 v201, v[56:59] offset:0\n"
      "v_mfma_f32_16x16x32_bf16 v[24:27], a[192:195], v[144:147], v[24:27]\n"
      "ds_write_b128 v201, v[60:63] offset:1024\n"
      "v_mfma_f32_16x16x32_bf16 v[24:27], a[208:211], v[148:151], v[24:27]\n"
      "v_mfma_f32_16x16x32_bf16 v[28:31], a[196:199], v[144:147], v[28:31]\n"
      "v_mfma_f32_16x16x32_bf16 v[28:31], a[212:215], v[148:151], v[28:31]\n"
      "s_add_u32 s69, s95, 11264\n"
      "buffer_load_dwordx4 v[140:143], v197, s[8:11], s69 offen offset:0 sc1\n"
      "s_waitcnt vmcnt(11)\n"
      "s_waitcnt lgkmcnt(0)\n"
      "s_barrier\n"
      "v_mfma_f32_16x16x32_bf16 v[80:83], a[232:235], v[152:155], v[80:83]\n"
      "s_waitcnt vmcnt(10)\n"
      "ds_read_b128 v[8:11], v205 offset:0\n"
      "v_mfma_f32_16x16x32_bf16 v[80:83], a[248:251], v[156:159], v[80:83]\n"
      "ds_read_b128 v[12:15], v205 offset:1024\n"
      "v_mfma_f32_16x16x32_bf16 v[84:87], a[236:239], v[152:155], v[84:87]\n"
      "ds_read_b128 v[56:59], v205 offset:8192\n"
      "v_mfma_f32_16x16x32_bf16 v[84:87], a[252:255], v[156:159], v[84:87]\n"
      "s_add_u32 s69, s95, 18432\n"
      "buffer_load_dwordx4 v[144:147], v197, s[8:11], s69 offen offset:0 sc1\n"
      "ds_read_b128 v[60:63], v205 offset:9216\n"
      "v_mfma_f32_16x16x32_bf16 v[24:27], a[224:227], v[152:155], v[24:27]\n"
      "ds_read_b128 v[64:67], v205 offset:16384\n"
      "v_mfma_f32_16x16x32_bf16 v[24:27], a[240:243], v[156:159], v[24:27]\n"
      "ds_read_b128 v[68:71], v205 offset:17408\n"
      "v_mfma_f32_16x16x32_bf16 v[28:31], a[228:231], v[152:155], v[28:31]\n"
      "v_add_f32 v0, v0, v168\n"
      "v_add_f32 v1, v1, v169\n"
      "v_add_f32 v2, v2, v170\n"
      "v_mfma_f32_16x16x32_bf16 v[28:31], a[244:247], v[156:159], v[28:31]\n"
      "s_add_u32 s69, s95, 25600\n"
      "buffer_load_dwordx4 v[148:151], v197, s[8:11], s69 offen offset:0 sc1\n"
      "s_waitcnt vmcnt(11)\n"
      "v_add_f32 v3, v3, v171\n"
      "v_add_f32 v4, v4, v172\n"
      "v_add_f32 v5, v5, v173\n"
      "v_mfma_f32_16x16x32_bf16 v[72:75], a[200:203], v[160:163], v[72:75]\n"
      "s_waitcnt vmcnt(10)\n"
      "v_add_f32 v6, v6, v174\n"
      "v_add_f32 v7, v7, v175\n"
      "v_lshlrev_b32 v176, 16, v224\n"
      "v_mfma_f32_16x16x32_bf16 v[72:75], a[216:219], v[164:167], v[72:75]\n"
      "v_and_b32 v177, 0xffff0000, v224\n"
      "v_lshlrev_b32 v178, 16, v225\n"
      "v_and_b32 v179, 0xffff0000, v225\n"
      "v_mfma_f32_16x16x32_bf16 v[76:79], a[204:207], v[160:163], v[76:79]\n"
      "v_lshlrev_b32 v180, 16, v226\n"
      "v_and_b32 v181, 0xffff0000, v226\n"
      "v_lshlrev_b32 v182, 16, v227\n"
      "v_mfma_f32_16x16x32_bf16 v[76:79], a[220:223], v[164:167], v[76:79]\n"
      "s_add_u32 s69, s95, 4096\n"
      "buffer_load_dwordx4 v[152:155], v196, s[8:11], s69 offen offset:0 sc1\n"
      "ds_write_b128 v202, v[80:83] offset:2048\n"
      "v_and_b32 v183, 0xffff0000, v227\n"
      "s_waitcnt lgkmcnt(1)\n"
      "v_add_f32 v8, v8, v56\n"
      "v_mfma_f32_16x16x32_bf16 v[16:19], a[192:195], v[160:163], v[16:19]\n"
      "ds_write_b128 v202, v[84:87] offset:3072\n"
      "v_add_f32 v9, v9, v57\n"
      "v_add_f32 v10, v10, v58\n"
      "v_add_f32 v11, v11, v59\n"
      "v_mfma_f32_16x16x32_bf16 v[16:19], a[208:211], v[164:167], v[16:19]\n"
      "ds_write_b128 v200, v[24:27] offset:2048\n"
      "v_add_f32 v12, v12, v60\n"
      "v_add_f32 v13, v13, v61\n"
      "v_add_f32 v14, v14, v62\n"
      "v_mfma_f32_16x16x32_bf16 v[20:23], a[196:199], v[160:163], v[20:23]\n"
      "ds_write_b128 v200, v[28:31] offset:3072\n"
      "v_add_f32 v15, v15, v63\n"
      "v_add_f32 v0, v0, v64\n"
      "v_add_f32 v1, v1, v65\n"
      "v_mfma_f32_16x16x32_bf16 v[20:23], a[212:215], v[164:167], v[20:23]\n"
      "s_add_u32 s69, s95, 11264\n"
      "buffer_load_dwordx4 v[156:159], v196, s[8:11], s69 offen offset:0 sc1\n"
      "s_waitcnt vmcnt(11)\n"
      "v_add_f32 v2, v2, v66\n"
      "v_add_f32 v3, v3, v67\n"
      "v_add_f32 v4, v4, v68\n"
      "v_mfma_f32_16x16x32_bf16 v[72:75], a[232:235], v[112:115], v[72:75]\n"
      "s_waitcnt vmcnt(10)\n"
      "v_add_f32 v5, v5, v69\n"
      "v_add_f32 v6, v6, v70\n"
      "v_add_f32 v7, v7, v71\n"
      "v_mfma_f32_16x16x32_bf16 v[72:75], a[248:251], v[116:119], v[72:75]\n"
      "v_add_f32 v0, v0, v8\n"
      "v_add_f32 v1, v1, v9\n"
      "v_add_f32 v2, v2, v10\n"
      "v_mfma_f32_16x16x32_bf16 v[76:79], a[236:239], v[112:115], v[76:79]\n"
      "v_add_f32 v3, v3, v11\n"
      "v_add_f32 v4, v4, v12\n"
      "v_add_f32 v5, v5, v13\n"
      "v_mfma_f32_16x16x32_bf16 v[76:79], a[252:255], v[116:119], v[76:79]\n"
      "s_add_u32 s69, s95, 18432\n"
      "buffer_load_dwordx4 v[160:163], v196, s[8:11], s69 offen offset:0 sc1\n"
      "v_add_f32 v6, v6, v14\n"
      "v_add_f32 v7, v7, v15\n"
      "v_mul_f32 v0, v0, v176\n"
      "v_mfma_f32_16x16x32_bf16 v[16:19], a[224:227], v[112:115], v[16:19]\n"
      "v_mul_f32 v1, v1, v177\n"
      "v_mul_f32 v2, v2, v178\n"
      "v_mul_f32 v3, v3, v179\n"
      "v_mfma_f32_16x16x32_bf16 v[16:19], a[240:243], v[116:119], v[16:19]\n"
      "v_mul_f32 v4, v4, v180\n"
      "v_mul_f32 v5, v5, v181\n"
      "v_mul_f32 v6, v6, v182\n"
      "v_mfma_f32_16x16x32_bf16 v[20:23], a[228:231], v[112:115], v[20:23]\n"
      "v_mul_f32 v7, v7, v183\n"
      "v_cvt_pk_bf16_f32 v184, v0, v1\n"
      "v_cvt_pk_bf16_f32 v185, v2, v3\n"
      "v_mfma_f32_16x16x32_bf16 v[20:23], a[244:247], v[116:119], v[20:23]\n"
      "s_add_u32 s69, s95, 25600\n"
      "buffer_load_dwordx4 v[164:167], v196, s[8:11], s69 offen offset:0 sc1\n"
      "s_waitcnt vmcnt(11)\n"
      "v_cvt_pk_bf16_f32 v186, v4, v5\n"
      "v_cvt_pk_bf16_f32 v187, v6, v7\n"
      "buffer_store_dwordx4 v[184:187], v206, s[8:11], s71 offen offset:0 sc1\n"
      "v_mfma_f32_16x16x32_bf16 v[104:107], a[200:203], v[120:123], v[104:107]\n"
      "s_waitcnt vmcnt(11)\n"
      "v_mfma_f32_16x16x32_bf16 v[104:107], a[216:219], v[124:127], v[104:107]\n"
      "v_mfma_f32_16x16x32_bf16 v[108:111], a[204:207], v[120:123], v[108:111]\n"
      "v_mfma_f32_16x16x32_bf16 v[108:111], a[220:223], v[124:127], v[108:111]\n"
      "s_add_u32 m0, s42, 135168\n"
      "s_add_u32 s67, s99, 0\n"
      "buffer_load_dwordx4 v249, s[32:35], s67 offen lds\n"
      "ds_write_b128 v201, v[72:75] offset:2048\n"
      "v_mfma_f32_16x16x32_bf16 v[48:51], a[192:195], v[120:123], v[48:51]\n"
      "ds_write_b128 v201, v[76:79] offset:3072\n"
      "v_mfma_f32_16x16x32_bf16 v[48:51], a[208:211], v[124:127], v[48:51]\n"
      "v_mfma_f32_16x16x32_bf16 v[52:55], a[196:199], v[120:123], v[52:55]\n"
      "v_mfma_f32_16x16x32_bf16 v[52:55], a[212:215], v[124:127], v[52:55]\n"
      "s_add_u32 m0, s42, 137216\n"
      "s_add_u32 s67, s99, 16384\n"
      "buffer_load_dwordx4 v249, s[32:35], s67 offen lds\n"
      "s_waitcnt vmcnt(12)\n"
      "s_waitcnt lgkmcnt(0)\n"
      "s_barrier\n"
      "v_mfma_f32_16x16x32_bf16 v[104:107], a[232:235], v[128:131], v[104:107]\n"
      "s_waitcnt vmcnt(11)\n"
      "ds_read_b128 v[24:27], v205 offset:2048\n"
      "v_mfma_f32_16x16x32_bf16 v[104:107], a[248:251], v[132:135], v[104:107]\n"
      "ds_read_b128 v[28:31], v205 offset:3072\n"
      "v_mfma_f32_16x16x32_bf16 v[108:111], a[236:239], v[128:131], v[108:111]\n"
      "ds_read_b128 v[72:75], v205 offset:10240\n"
      "v_mfma_f32_16x16x32_bf16 v[108:111], a[252:255], v[132:135], v[108:111]\n"
      "s_add_u32 m0, s42, 139264\n"
      "s_add_u32 s67, s99, 32768\n"
      "buffer_load_dwordx4 v249, s[32:35], s67 offen lds\n"
      "ds_read_b128 v[76:79], v205 offset:11264\n"
      "v_mfma_f32_16x16x32_bf16 v[48:51], a[224:227], v[128:131], v[48:51]\n"
      "ds_read_b128 v[80:83], v205 offset:18432\n"
      "v_mfma_f32_16x16x32_bf16 v[48:51], a[240:243], v[132:135], v[48:51]\n"
      "ds_read_b128 v[84:87], v205 offset:19456\n"
      "v_mfma_f32_16x16x32_bf16 v[52:55], a[228:231], v[128:131], v[52:55]\n"
      "v_add_f32 v16, v16, v168\n"
      "v_add_f32 v17, v17, v169\n"
      "v_add_f32 v18, v18, v170\n"
      "v_mfma_f32_16x16x32_bf16 v[52:55], a[244:247], v[132:135], v[52:55]\n"
      "s_add_u32 m0, s42, 141312\n"
      "s_add_u32 s67, s99, 49152\n"
      "buffer_load_dwordx4 v249, s[32:35], s67 offen lds\n"
      "s_waitcnt vmcnt(12)\n"
      "v_add_f32 v19, v19, v171\n"
      "v_add_f32 v20, v20, v172\n"
      "v_add_f32 v21, v21, v173\n"
      "v_mfma_f32_16x16x32_bf16 v[96:99], a[200:203], v[136:139], v[96:99]\n"
      "s_waitcnt vmcnt(11)\n"
      "v_add_f32 v22, v22, v174\n"
      "v_add_f32 v23, v23, v175\n"
      "v_lshlrev_b32 v176, 16, v228\n"
      "v_mfma_f32_16x16x32_bf16 v[96:99], a[216:219], v[140:143], v[96:99]\n"
      "v_and_b32 v177, 0xffff0000, v228\n"
      "v_lshlrev_b32 v178, 16, v229\n"
      "v_and_b32 v179, 0xffff0000, v229\n"
      "v_mfma_f32_16x16x32_bf16 v[100:103], a[204:207], v[136:139], v[100:103]\n"
      "v_lshlrev_b32 v180, 16, v230\n"
      "v_and_b32 v181, 0xffff0000, v230\n"
      "v_lshlrev_b32 v182, 16, v231\n"
      "v_mfma_f32_16x16x32_bf16 v[100:103], a[220:223], v[140:143], v[100:103]\n"
      "s_add_u32 m0, s42, 143360\n"
      "s_add_u32 s67, s99, 65536\n"
      "buffer_load_dwordx4 v249, s[32:35], s67 offen lds\n"
      "ds_write_b128 v204, v[104:107] offset:6144\n"
      "v_and_b32 v183, 0xffff0000, v231\n"
      "s_waitcnt lgkmcnt(1)\n"
      "v_add_f32 v24, v24, v72\n"
      "v_mfma_f32_16x16x32_bf16 v[40:43], a[192:195], v[136:139], v[40:43]\n"
      "ds_write_b128 v204, v[108:111] offset:7168\n"
      "v_add_f32 v25, v25, v73\n"
      "v_add_f32 v26, v26, v74\n"
      "v_add_f32 v27, v27, v75\n"
      "v_mfma_f32_16x16x32_bf16 v[40:43], a[208:211], v[140:143], v[40:43]\n"
      "ds_write_b128 v203, v[48:51] offset:6144\n"
      "v_add_f32 v28, v28, v76\n"
      "v_add_f32 v29, v29, v77\n"
      "v_add_f32 v30, v30, v78\n"
      "v_mfma_f32_16x16x32_bf16 v[44:47], a[196:199], v[136:139], v[44:47]\n"
      "ds_write_b128 v203, v[52:55] offset:7168\n"
      "v_add_f32 v31, v31, v79\n"
      "v_add_f32 v16, v16, v80\n"
      "v_add_f32 v17, v17, v81\n"
      "v_mfma_f32_16x16x32_bf16 v[44:47], a[212:215], v[140:143], v[44:47]\n"
      "s_add_u32 m0, s42, 145408\n"
      "s_add_u32 s67, s99, 81920\n"
      "buffer_load_dwordx4 v249, s[32:35], s67 offen lds\n"
      "s_waitcnt vmcnt(12)\n"
      "v_add_f32 v18, v18, v82\n"
      "v_add_f32 v19, v19, v83\n"
      "v_add_f32 v20, v20, v84\n"
      "v_mfma_f32_16x16x32_bf16 v[96:99], a[232:235], v[144:147], v[96:99]\n"
      "s_waitcnt vmcnt(11)\n"
      "s_waitcnt lgkmcnt(0)\n"
      "s_barrier\n"
      "v_add_f32 v21, v21, v85\n"
      "v_add_f32 v22, v22, v86\n"
      "v_add_f32 v23, v23, v87\n"
      "v_mfma_f32_16x16x32_bf16 v[96:99], a[248:251], v[148:151], v[96:99]\n"
      "ds_read_b128 v[104:107], v205 offset:6144\n"
      "v_add_f32 v16, v16, v24\n"
      "v_add_f32 v17, v17, v25\n"
      "v_add_f32 v18, v18, v26\n"
      "v_mfma_f32_16x16x32_bf16 v[100:103], a[236:239], v[144:147], v[100:103]\n"
      "ds_read_b128 v[108:111], v205 offset:7168\n"
      "v_add_f32 v19, v19, v27\n"
      "v_add_f32 v20, v20, v28\n"
      "v_add_f32 v21, v21, v29\n"
      "v_mfma_f32_16x16x32_bf16 v[100:103], a[252:255], v[148:151], v[100:103]\n"
      "s_add_u32 m0, s42, 147456\n"
      "s_add_u32 s67, s99, 98304\n"
      "buffer_load_dwordx4 v249, s[32:35], s67 offen lds\n"
      "ds_read_b128 v[56:59], v205 offset:14336\n"
      "v_add_f32 v22, v22, v30\n"
      "v_add_f32 v23, v23, v31\n"
      "v_mul_f32 v16, v16, v176\n"
      "v_mfma_f32_16x16x32_bf16 v[40:43], a[224:227], v[144:147], v[40:43]\n"
      "ds_read_b128 v[60:63], v205 offset:15360\n"
      "v_mul_f32 v17, v17, v177\n"
      "v_mul_f32 v18, v18, v178\n"
      "v_mul_f32 v19, v19, v179\n"
      "v_mfma_f32_16x16x32_bf16 v[40:43], a[240:243], v[148:151], v[40:43]\n"
      "ds_read_b128 v[64:67], v205 offset:22528\n"
      "v_mul_f32 v20, v20, v180\n"
      "v_mul_f32 v21, v21, v181\n"
      "v_mul_f32 v22, v22, v182\n"
      "v_mfma_f32_16x16x32_bf16 v[44:47], a[228:231], v[144:147], v[44:47]\n"
      "ds_read_b128 v[68:71], v205 offset:23552\n"
      "v_mul_f32 v23, v23, v183\n"
      "v_cvt_pk_bf16_f32 v188, v16, v17\n"
      "v_cvt_pk_bf16_f32 v189, v18, v19\n"
      "v_mfma_f32_16x16x32_bf16 v[44:47], a[244:247], v[148:151], v[44:47]\n"
      "s_waitcnt vmcnt(11)\n"
      "v_cvt_pk_bf16_f32 v190, v20, v21\n"
      "v_cvt_pk_bf16_f32 v191, v22, v23\n"
      "buffer_store_dwordx4 v[188:191], v206, s[8:11], s71 offen offset:2048 sc1\n"
      "v_mfma_f32_16x16x32_bf16 v[88:91], a[200:203], v[152:155], v[88:91]\n"
      "s_waitcnt vmcnt(11)\n"
      "v_add_f32 v48, v48, v168\n"
      "v_add_f32 v49, v49, v169\n"
      "v_mfma_f32_16x16x32_bf16 v[88:91], a[216:219], v[156:159], v[88:91]\n"
      "v_add_f32 v50, v50, v170\n"
      "v_add_f32 v51, v51, v171\n"
      "v_add_f32 v52, v52, v172\n"
      "v_mfma_f32_16x16x32_bf16 v[92:95], a[204:207], v[152:155], v[92:95]\n"
      "v_add_f32 v53, v53, v173\n"
      "v_add_f32 v54, v54, v174\n"
      "v_add_f32 v55, v55, v175\n"
      "v_mfma_f32_16x16x32_bf16 v[92:95], a[220:223], v[156:159], v[92:95]\n"
      "ds_write_b128 v202, v[96:99] offset:4096\n"
      "v_lshlrev_b32 v176, 16, v236\n"
      "v_and_b32 v177, 0xffff0000, v236\n"
      "v_lshlrev_b32 v178, 16, v237\n"
      "v_mfma_f32_16x16x32_bf16 v[32:35], a[192:195], v[152:155], v[32:35]\n"
      "ds_write_b128 v202, v[100:103] offset:5120\n"
      "v_and_b32 v179, 0xffff0000, v237\n"
      "v_lshlrev_b32 v180, 16, v238\n"
      "v_and_b32 v181, 0xffff0000, v238\n"
      "v_mfma_f32_16x16x32_bf16 v[32:35], a[208:211], v[156:159], v[32:35]\n"
      "ds_write_b128 v200, v[40:43] offset:4096\n"
      "v_lshlrev_b32 v182, 16, v239\n"
      "v_and_b32 v183, 0xffff0000, v239\n"
      "s_waitcnt lgkmcnt(3)\n"
      "v_mfma_f32_16x16x32_bf16 v[36:39], a[196:199], v[152:155], v[36:39]\n"
      "ds_write_b128 v200, v[44:47] offset:5120\n"
      "v_add_f32 v104, v104, v56\n"
      "v_add_f32 v105, v105, v57\n"
      "v_add_f32 v106, v106, v58\n"
      "v_mfma_f32_16x16x32_bf16 v[36:39], a[212:215], v[156:159], v[36:39]\n"
      "s_waitcnt vmcnt(10)\n"
      "v_add_f32 v107, v107, v59\n"
      "v_add_f32 v108, v108, v60\n"
      "v_add_f32 v109, v109, v61\n"
      "v_mfma_f32_16x16x32_bf16 v[88:91], a[232:235], v[160:163], v[88:91]\n"
      "s_waitcnt vmcnt(9)\n"
      "v_add_f32 v110, v110, v62\n"
      "v_add_f32 v111, v111, v63\n"
      "v_add_f32 v48, v48, v64\n"
      "v_mfma_f32_16x16x32_bf16 v[88:91], a[248:251], v[164:167], v[88:91]\n"
      "v_add_f32 v49, v49, v65\n"
      "v_add_f32 v50, v50, v66\n"
      "v_add_f32 v51, v51, v67\n"
      "v_mfma_f32_16x16x32_bf16 v[92:95], a[236:239], v[160:163], v[92:95]\n"
      "v_add_f32 v52, v52, v68\n"
      "v_add_f32 v53, v53, v69\n"
      "v_add_f32 v54, v54, v70\n"
      "v_mfma_f32_16x16x32_bf16 v[92:95], a[252:255], v[164:167], v[92:95]\n"
      "v_add_f32 v55, v55, v71\n"
      "v_add_f32 v48, v48, v104\n"
      "v_add_f32 v49, v49, v105\n"
      "v_mfma_f32_16x16x32_bf16 v[32:35], a[224:227], v[160:163], v[32:35]\n"
      "v_add_f32 v50, v50, v106\n"
      "v_add_f32 v51, v51, v107\n"
      "v_add_f32 v52, v52, v108\n"
      "v_mfma_f32_16x16x32_bf16 v[32:35], a[240:243], v[164:167], v[32:35]\n"
      "v_add_f32 v53, v53, v109\n"
      "v_add_f32 v54, v54, v110\n"
      "v_add_f32 v55, v55, v111\n"
      "v_mfma_f32_16x16x32_bf16 v[36:39], a[228:231], v[160:163], v[36:39]\n"
      "v_mul_f32 v48, v48, v176\n"
      "v_mul_f32 v49, v49, v177\n"
      "v_mul_f32 v50, v50, v178\n"
      "v_mfma_f32_16x16x32_bf16 v[36:39], a[244:247], v[164:167], v[36:39]\n"
      "v_mul_f32 v51, v51, v179\n"
      "v_mul_f32 v52, v52, v180\n"
      "v_mul_f32 v53, v53, v181\n"
      "ds_write_b128 v201, v[88:91] offset:4096\n"
      "v_mul_f32 v54, v54, v182\n"
      "v_mul_f32 v55, v55, v183\n"
      "ds_write_b128 v201, v[92:95] offset:5120\n"
      "v_cvt_pk_bf16_f32 v188, v48, v49\n"
      "v_cvt_pk_bf16_f32 v189, v50, v51\n"
      "s_waitcnt lgkmcnt(0)\n"
      "s_barrier\n"
      "v_cvt_pk_bf16_f32 v190, v52, v53\n"
      "v_cvt_pk_bf16_f32 v191, v54, v55\n"
      "ds_read_b128 v[40:43], v205 offset:4096\n"
      "buffer_store_dwordx4 v[188:191], v207, s[8:11], s73 offen offset:0 sc1\n"
      "ds_read_b128 v[44:47], v205 offset:5120\n"
      "ds_read_b128 v[88:91], v205 offset:12288\n"
      "ds_read_b128 v[92:95], v205 offset:13312\n"
      "ds_read_b128 v[96:99], v205 offset:20480\n"
      "ds_read_b128 v[100:103], v205 offset:21504\n"
      "s_nop 0\n"
      "v_pk_add_f32 v[32:33], v[32:33], v[168:169]\n"
      "v_pk_add_f32 v[34:35], v[34:35], v[170:171]\n"
      "v_pk_add_f32 v[36:37], v[36:37], v[172:173]\n"
      "v_pk_add_f32 v[38:39], v[38:39], v[174:175]\n"
      "v_lshlrev_b32 v176, 16, v232\n"
      "v_and_b32 v177, 0xffff0000, v232\n"
      "v_lshlrev_b32 v178, 16, v233\n"
      "v_and_b32 v179, 0xffff0000, v233\n"
      "v_lshlrev_b32 v180, 16, v234\n"
      "v_and_b32 v181, 0xffff0000, v234\n"
      "v_lshlrev_b32 v182, 16, v235\n"
      "v_and_b32 v183, 0xffff0000, v235\n"
      "s_waitcnt lgkmcnt(0)\n"
      "s_nop 0\n"
      "v_pk_add_f32 v[40:41], v[40:41], v[88:89]\n"
      "v_pk_add_f32 v[42:43], v[42:43], v[90:91]\n"
      "v_pk_add_f32 v[44:45], v[44:45], v[92:93]\n"
      "v_pk_add_f32 v[46:47], v[46:47], v[94:95]\n"
      "v_pk_add_f32 v[32:33], v[32:33], v[96:97]\n"
      "v_pk_add_f32 v[34:35], v[34:35], v[98:99]\n"
      "v_pk_add_f32 v[36:37], v[36:37], v[100:101]\n"
      "v_pk_add_f32 v[38:39], v[38:39], v[102:103]\n"
      "v_pk_add_f32 v[32:33], v[32:33], v[40:41]\n"
      "v_pk_add_f32 v[34:35], v[34:35], v[42:43]\n"
      "v_pk_add_f32 v[36:37], v[36:37], v[44:45]\n"
      "v_pk_add_f32 v[38:39], v[38:39], v[46:47]\n"
      "v_pk_mul_f32 v[32:33], v[32:33], v[176:177]\n"
      "v_pk_mul_f32 v[34:35], v[34:35], v[178:179]\n"
      "v_pk_mul_f32 v[36:37], v[36:37], v[180:181]\n"
      "v_pk_mul_f32 v[38:39], v[38:39], v[182:183]\n"
      "v_cvt_pk_bf16_f32 v184, v32, v33\n"
      "v_cvt_pk_bf16_f32 v185, v34, v35\n"
      "v_cvt_pk_bf16_f32 v186, v36, v37\n"
      "v_cvt_pk_bf16_f32 v187, v38, v39\n"
      "buffer_store_dwordx4 v[184:187], v206, s[8:11], s72 offen offset:0 sc1\n"
      "s_lshl_b32 s63, s53, 8\n"
      "s_add_u32 s63, s63, 132096\n"
      "v_lshl_add_u32 v222, v215, 1, s63\n"
      "ds_read_b128 v[144:147], v222 offset:192\n"
      "ds_read_b128 v[148:151], v222 offset:208\n"
      "v_lshrrev_b32 v217, 4, v208\n"
      "v_lshl_add_u32 v217, v210, 1, v217\n"
      "v_lshl_add_u32 v218, v211, 3, v209\n"
      "v_lshl_add_u32 v218, v217, 2, v218\n"
      "v_and_b32 v219, 8, v208\n"
      "v_lshl_add_u32 v219, v217, 8, v219\n"
      "s_mul_i32 s63, s60, 57344\n"
      "v_add_u32 v219, s63, v219\n"
      "v_add_u32 v217, 0, v218\n"
      "v_and_b32 v217, 15, v217\n"
      "v_lshl_add_u32 v128, v217, 4, v219\n"
      "v_add_u32 v217, 4, v218\n"
      "v_and_b32 v217, 15, v217\n"
      "v_lshl_add_u32 v129, v217, 4, v219\n"
      "v_lshlrev_b32 v112, 16, v240\n"
      "v_and_b32 v113, 0xffff0000, v240\n"
      "v_lshlrev_b32 v114, 16, v241\n"
      "v_and_b32 v115, 0xffff0000, v241\n"
      "v_lshlrev_b32 v116, 16, v242\n"
      "v_and_b32 v117, 0xffff0000, v242\n"
      "v_lshlrev_b32 v118, 16, v243\n"
      "v_and_b32 v119, 0xffff0000, v243\n"
      "v_lshlrev_b32 v120, 16, v244\n"
      "v_and_b32 v121, 0xffff0000, v244\n"
      "v_lshlrev_b32 v122, 16, v245\n"
      "v_and_b32 v123, 0xffff0000, v245\n"
      "v_lshlrev_b32 v124, 16, v246\n"
      "v_and_b32 v125, 0xffff0000, v246\n"
      "v_lshlrev_b32 v126, 16, v247\n"
      "v_and_b32 v127, 0xffff0000, v247\n"
      "s_lshl_b32 s69, s61, 10\n"
      "s_add_u32 s69, s69, s63\n"
      "s_mul_i32 s68, s58, 114688\n"
      "s_add_u32 s68, s68, s69\n"
      "s_add_u32 s68, s68, 15728640\n"
      "s_xor_b32 s76, s59, 0\n"
      "s_sub_u32 s76, s76, 0\n"
      "s_mul_i32 s76, s76, 7168\n"
      "s_add_u32 s76, s76, s68\n"
      "s_xor_b32 s77, s59, 1\n"
      "s_sub_u32 s77, s77, 1\n"
      "s_mul_i32 s77, s77, 7168\n"
      "s_add_u32 s77, s77, s68\n"
      "s_xor_b32 s78, s59, 2\n"
      "s_sub_u32 s78, s78, 0\n"
      "s_mul_i32 s78, s78, 7168\n"
      "s_add_u32 s78, s78, s68\n"
      "s_xor_b32 s79, s59, 3\n"
      "s_sub_u32 s79, s79, 1\n"
      "s_mul_i32 s79, s79, 7168\n"
      "s_add_u32 s79, s79, s68\n"
      "s_xor_b32 s80, s59, 4\n"
      "s_sub_u32 s80, s80, 0\n"
      "s_mul_i32 s80, s80, 7168\n"
      "s_add_u32 s80, s80, s68\n"
      "s_xor_b32 s81, s59, 5\n"
      "s_sub_u32 s81, s81, 1\n"
      "s_mul_i32 s81, s81, 7168\n"
      "s_add_u32 s81, s81, s68\n"
      "s_xor_b32 s82, s59, 6\n"
      "s_sub_u32 s82, s82, 0\n"
      "s_mul_i32 s82, s82, 7168\n"
      "s_add_u32 s82, s82, s68\n"
      "s_xor_b32 s83, s59, 7\n"
      "s_sub_u32 s83, s83, 1\n"
      "s_mul_i32 s83, s83, 7168\n"
      "s_add_u32 s83, s83, s68\n"
      "s_cmp_eq_u32 s61, 0\n"
      "s_cselect_b32 s84, s76, s77\n"
      "s_cselect_b32 s85, s78, s79\n"
      "s_cselect_b32 s86, s80, s81\n"
      "s_cselect_b32 s87, s82, s83\n"
      "s_mul_i32 s70, s58, 256\n"
      "s_add_u32 s70, s70, 21594112\n"
      "v_lshl_add_u32 v217, v213, 10, v215\n"
      "s_lshl1_add_u32 s74, s58, s60\n"
      "s_lshl_b32 s74, s74, 15\n"
      "s_lshl1_add_u32 s63, s59, s61\n"
      "s_lshl_b32 s63, s63, 6\n"
      "s_add_u32 s74, s74, s63\n"
      "s_mul_i32 s63, s53, 2560\n"
      "s_add_u32 s63, s63, 114688\n"
      "v_mul_u32_u24 v130, 80, v213\n"
      "v_lshl_add_u32 v130, v211, 3, v130\n"
      "v_add_u32 v130, s63, v130\n"
      "v_lshrrev_b32 v132, 2, v255\n"
      "v_mul_u32_u24 v131, 80, v132\n"
      "v_lshl_add_u32 v131, v208, 1, v131\n"
      "v_add_u32 v131, s63, v131\n"
      "v_lshlrev_b32 v132, 10, v132\n"
      "v_lshl_add_u32 v132, v208, 1, v132\n"
      "v_add_u32 v133, 16384, v132\n"
      "s_cmp_lg_u32 s53, 0\n"
      "s_waitcnt vmcnt(0) lgkmcnt(0)\n"
      "s_barrier\n"
      "s_cbranch_scc1 Lf48_%=\n"
      "s_mov_b64 exec, 1\n"
      "buffer_store_dwordx2 v[252:253], off, s[8:11], s90 sc1\n"
      "s_mov_b64 exec, -1\n"
      "Lf48_%=:\n"
      "s_barrier\n"
      "s_add_u32 m0, s69, 0\n"
      "s_add_u32 s67, s76, 0\n"
      "buffer_load_dwordx4 v254, s[8:11], s67 offen sc1 lds\n"
      "s_add_u32 m0, s69, 2048\n"
      "s_add_u32 s67, s76, 2048\n"
      "buffer_load_dwordx4 v254, s[8:11], s67 offen sc1 lds\n"
      "s_add_u32 m0, s69, 4096\n"
      "s_add_u32 s67, s76, 4096\n"
      "buffer_load_dwordx4 v254, s[8:11], s67 offen sc1 lds\n"
      "v_add_u32 v198, 135168, v198\n"
      "ds_read_b64_tr_b16 a[168:169], v198 offset:0\n"
      "ds_read_b64_tr_b16 a[170:171], v198 offset:512\n"
      "ds_read_b64_tr_b16 a[172:173], v198 offset:2048\n"
      "ds_read_b64_tr_b16 a[174:175], v198 offset:2560\n"
      "ds_read_b64_tr_b16 a[176:177], v198 offset:4096\n"
      "ds_read_b64_tr_b16 a[178:179], v198 offset:4608\n"
      "ds_read_b64_tr_b16 a[180:181], v198 offset:6144\n"
      "ds_read_b64_tr_b16 a[182:183], v198 offset:6656\n"
      "ds_read_b64_tr_b16 a[184:185], v198 offset:8192\n"
      "ds_read_b64_tr_b16 a[186:187], v198 offset:8704\n"
      "ds_read_b64_tr_b16 a[188:189], v198 offset:10240\n"
      "ds_read_b64_tr_b16 a[190:191], v198 offset:10752\n"
      "ds_read_b64_tr_b16 a[192:193], v198 offset:12288\n"
      "ds_read_b64_tr_b16 a[194:195], v198 offset:12800\n"
      "ds_read_b64_tr_b16 a[196:197], v198 offset:14336\n"
      "ds_read_b64_tr_b16 a[198:199], v198 offset:14848\n"
      "ds_read_b64_tr_b16 a[200:201], v198 offset:16384\n"
      "ds_read_b64_tr_b16 a[202:203], v198 offset:16896\n"
      "ds_read_b64_tr_b16 a[204:205], v198 offset:18432\n"
      "ds_read_b64_tr_b16 a[206:207], v198 offset:18944\n"
      "ds_read_b64_tr_b16 a[208:209], v198 offset:20480\n"
      "ds_read_b64_tr_b16 a[210:211], v198 offset:20992\n"
      "ds_read_b64_tr_b16 a[212:213], v198 offset:22528\n"
      "ds_read_b64_tr_b16 a[214:215], v198 offset:23040\n"
      "ds_read_b64_tr_b16 a[216:217], v198 offset:24576\n"
      "ds_read_b64_tr_b16 a[218:219], v198 offset:25088\n"
      "ds_read_b64_tr_b16 a[220:221], v198 offset:26624\n"
      "ds_read_b64_tr_b16 a[222:223], v198 offset:27136\n"
      "s_sleep 7\n"
      "buffer_load_dwordx2 v[220:221], v248, s[8:11], s70 offen sc1\n"
      "v_lshlrev_b32 v96, 16, v144\n"
      "v_and_b32 v97, 0xffff0000, v144\n"
      "v_lshlrev_b32 v98, 16, v145\n"
      "v_and_b32 v99, 0xffff0000, v145\n"
      "v_lshlrev_b32 v100, 16, v146\n"
      "v_and_b32 v101, 0xffff0000, v146\n"
      "v_lshlrev_b32 v102, 16, v147\n"
      "v_and_b32 v103, 0xffff0000, v147\n"
      "v_lshlrev_b32 v104, 16, v148\n"
      "v_and_b32 v105, 0xffff0000, v148\n"
      "v_lshlrev_b32 v106, 16, v149\n"
      "v_and_b32 v107, 0xffff0000, v149\n"
      "v_lshlrev_b32 v108, 16, v150\n"
      "v_and_b32 v109, 0xffff0000, v150\n"
      "v_lshlrev_b32 v110, 16, v151\n"
      "v_and_b32 v111, 0xffff0000, v151\n"
      "Lp49_%=:\n"
      "s_waitcnt vmcnt(0)\n"
      "v_cmp_ne_u64 vcc, s[56:57], v[220:221]\n"
      "s_nop 0\n"
      "s_cmp_eq_u64 vcc, 0\n"
      "s_cbranch_scc1 Lpd50_%=\n"
      "s_cmp_eq_u32 s62, 0\n"
      "s_cbranch_scc1 Lpd50_%=\n"
      "s_sub_u32 s62, s62, 1\n"
      "s_sleep 1\n"
      "buffer_load_dwordx2 v[220:221], v248, s[8:11], s70 offen sc1\n"
      "s_branch Lp49_%=\n"
      "Lpd50_%=:\n"
      "s_add_u32 m0, s69, 6144\n"
      "s_add_u32 s67, s84, 6144\n"
      "buffer_load_dwordx4 v254, s[8:11], s67 offen sc1 lds\n"
      "s_add_u32 m0, s69, 8192\n"
      "s_add_u32 s67, s77, 8192\n"
      "buffer_load_dwordx4 v254, s[8:11], s67 offen sc1 lds\n"
      "s_add_u32 m0, s69, 10240\n"
      "s_add_u32 s67, s77, 10240\n"
      "buffer_load_dwordx4 v254, s[8:11], s67 offen sc1 lds\n"
      "s_add_u32 m0, s69, 12288\n"
      "s_add_u32 s67, s77, 12288\n"
      "buffer_load_dwordx4 v254, s[8:11], s67 offen sc1 lds\n"
      "s_add_u32 m0, s69, 14336\n"
      "s_add_u32 s67, s78, 0\n"
      "buffer_load_dwordx4 v254, s[8:11], s67 offen sc1 lds\n"
      "s_add_u32 m0, s69, 16384\n"
      "s_add_u32 s67, s78, 2048\n"
      "buffer_load_dwordx4 v254, s[8:11], s67 offen sc1 lds\n"
      "s_add_u32 m0, s69, 18432\n"
      "s_add_u32 s67, s78, 4096\n"
      "buffer_load_dwordx4 v254, s[8:11], s67 offen sc1 lds\n"
      "s_add_u32 m0, s69, 20480\n"
      "s_add_u32 s67, s85, 6144\n"
      "buffer_load_dwordx4 v254, s[8:11], s67 offen sc1 lds\n"
      "s_add_u32 m0, s69, 22528\n"
      "s_add_u32 s67, s79, 8192\n"
      "buffer_load_dwordx4 v254, s[8:11], s67 offen sc1 lds\n"
      "s_add_u32 m0, s69, 24576\n"
      "s_add_u32 s67, s79, 10240\n"
      "buffer_load_dwordx4 v254, s[8:11], s67 offen sc1 lds\n"
      "s_add_u32 m0, s69, 26624\n"
      "s_add_u32 s67, s79, 12288\n"
      "buffer_load_dwordx4 v254, s[8:11], s67 offen sc1 lds\n"
      "s_barrier\n"
      "ds_read_b64_tr_b16 v[0:1], v128 offset:0\n"
      "ds_read_b64_tr_b16 v[2:3], v129 offset:0\n"
      "ds_read_b64_tr_b16 v[4:5], v128 offset:1024\n"
      "ds_read_b64_tr_b16 v[6:7], v129 offset:1024\n"
      "s_add_u32 m0, s69, 28672\n"
      "s_add_u32 s67, s80, 0\n"
      "buffer_load_dwordx4 v254, s[8:11], s67 offen sc1 lds\n"
      "ds_read_b64_tr_b16 v[8:9], v128 offset:2048\n"
      "ds_read_b64_tr_b16 v[10:11], v129 offset:2048\n"
      "ds_read_b64_tr_b16 v[12:13], v128 offset:3072\n"
      "ds_read_b64_tr_b16 v[14:15], v129 offset:3072\n"
      "s_add_u32 m0, s69, 30720\n"
      "s_add_u32 s67, s80, 2048\n"
      "buffer_load_dwordx4 v254, s[8:11], s67 offen sc1 lds\n"
      "ds_read_b64_tr_b16 v[16:17], v128 offset:4096\n"
      "ds_read_b64_tr_b16 v[18:19], v129 offset:4096\n"
      "ds_read_b64_tr_b16 v[20:21], v128 offset:5120\n"
      "ds_read_b64_tr_b16 v[22:23], v129 offset:5120\n"
      "s_add_u32 m0, s69, 32768\n"
      "s_add_u32 s67, s80, 4096\n"
      "buffer_load_dwordx4 v254, s[8:11], s67 offen sc1 lds\n"
      "s_waitcnt vmcnt(10)\n"
      "s_barrier\n"
      "s_waitcnt lgkmcnt(10)\n"
      "v_mfma_f32_32x32x16_bf16 v[64:79], a[0:3], v[0:3], 0\n"
      "Lhs2_4_%=:\n"
      "s_cbranch_execz Lhr2_%=\n"
      "ds_read_b64_tr_b16 v[32:33], v128 offset:6144\n"
      "ds_read_b64_tr_b16 v[34:35], v129 offset:6144\n"
      "s_waitcnt lgkmcnt(10)\n"
      "v_mfma_f32_32x32x16_bf16 v[64:79], a[4:7], v[4:7], v[64:79]\n"
      "ds_read_b64_tr_b16 v[36:37], v128 offset:7168\n"
      "ds_read_b64_tr_b16 v[38:39], v129 offset:7168\n"
      "s_add_u32 m0, s69, 34816\n"
      "s_add_u32 s67, s86, 6144\n"
      "buffer_load_dwordx4 v254, s[8:11], s67 offen sc1 lds\n"
      "s_waitcnt lgkmcnt(10)\n"
      "v_mfma_f32_32x32x16_bf16 v[64:79], a[8:11], v[8:11], v[64:79]\n"
      "ds_read_b64_tr_b16 v[40:41], v128 offset:8192\n"
      "ds_read_b64_tr_b16 v[42:43], v129 offset:8192\n"
      "s_waitcnt lgkmcnt(10)\n"
      "v_mfma_f32_32x32x16_bf16 v[64:79], a[12:15], v[12:15], v[64:79]\n"
      "ds_read_b64_tr_b16 v[44:45], v128 offset:9216\n"
      "ds_read_b64_tr_b16 v[46:47], v129 offset:9216\n"
      "s_add_u32 m0, s69, 36864\n"
      "s_add_u32 s67, s81, 8192\n"
      "buffer_load_dwordx4 v254, s[8:11], s67 offen sc1 lds\n"
      "s_waitcnt lgkmcnt(10)\n"
      "v_mfma_f32_32x32x16_bf16 v[64:79], a[16:19], v[16:19], v[64:79]\n"
      "ds_read_b64_tr_b16 v[48:49], v128 offset:10240\n"
      "ds_read_b64_tr_b16 v[50:51], v129 offset:10240\n"
      "s_waitcnt lgkmcnt(10)\n"
      "v_mfma_f32_32x32x16_bf16 v[64:79], a[20:23], v[20:23], v[64:79]\n"
      "ds_read_b64_tr_b16 v[52:53], v128 offset:11264\n"
      "ds_read_b64_tr_b16 v[54:55], v129 offset:11264\n"
      "s_add_u32 m0, s69, 38912\n"
      "s_add_u32 s67, s81, 10240\n"
      "buffer_load_dwordx4 v254, s[8:11], s67 offen sc1 lds\n"
      "ds_read_b64_tr_b16 v[56:57], v128 offset:12288\n"
      "ds_read_b64_tr_b16 v[58:59], v129 offset:12288\n"
      "ds_read_b64_tr_b16 v[60:61], v128 offset:13312\n"
      "ds_read_b64_tr_b16 v[62:63], v129 offset:13312\n"
      "s_add_u32 m0, s69, 40960\n"
      "s_add_u32 s67, s81, 12288\n"
      "buffer_load_dwordx4 v254, s[8:11], s67 offen sc1 lds\n"
      "s_waitcnt vmcnt(10)\n"
      "s_barrier\n"
      "s_waitcnt lgkmcnt(14)\n"
      "v_mfma_f32_32x32x16_bf16 v[64:79], a[24:27], v[32:35], v[64:79]\n"
      "ds_read_b64_tr_b16 v[0:1], v128 offset:14336\n"
      "ds_read_b64_tr_b16 v[2:3], v129 offset:14336\n"
      "s_waitcnt lgkmcnt(14)\n"
      "v_mfma_f32_32x32x16_bf16 v[64:79], a[28:31], v[36:39], v[64:79]\n"
      "ds_read_b64_tr_b16 v[4:5], v128 offset:15360\n"
      "ds_read_b64_tr_b16 v[6:7], v129 offset:15360\n"
      "s_add_u32 m0, s69, 43008\n"
      "s_add_u32 s67, s82, 0\n"
      "buffer_load_dwordx4 v254, s[8:11], s67 offen sc1 lds\n"
      "s_waitcnt lgkmcnt(14)\n"
      "v_mfma_f32_32x32x16_bf16 v[64:79], a[32:35], v[40:43], v[64:79]\n"
      "ds_read_b64_tr_b16 v[8:9], v128 offset:16384\n"
      "ds_read_b64_tr_b16 v[10:11], v129 offset:16384\n"
      "s_waitcnt lgkmcnt(14)\n"
      "v_mfma_f32_32x32x16_bf16 v[64:79], a[36:39], v[44:47], v[64:79]\n"
      "ds_read_b64_tr_b16 v[12:13], v128 offset:17408\n"
      "ds_read_b64_tr_b16 v[14:15], v129 offset:17408\n"
      "s_add_u32 m0, s69, 45056\n"
      "s_add_u32 s67, s82, 2048\n"
      "buffer_load_dwordx4 v254, s[8:11], s67 offen sc1 lds\n"
      "s_waitcnt lgkmcnt(14)\n"
      "v_mfma_f32_32x32x16_bf16 v[64:79], a[40:43], v[48:51], v[64:79]\n"
      "ds_read_b64_tr_b16 v[16:17], v128 offset:18432\n"
      "ds_read_b64_tr_b16 v[18:19], v129 offset:18432\n"
      "s_waitcnt lgkmcnt(14)\n"
      "v_mfma_f32_32x32x16_bf16 v[64:79], a[44:47], v[52:55], v[64:79]\n"
      "ds_read_b64_tr_b16 v[20:21], v128 offset:19456\n"
      "ds_read_b64_tr_b16 v[22:23], v129 offset:19456\n"
      "s_add_u32 m0, s69, 47104\n"
      "s_add_u32 s67, s82, 4096\n"
      "buffer_load_dwordx4 v254, s[8:11], s67 offen sc1 lds\n"
      "s_waitcnt lgkmcnt(14)\n"
      "v_mfma_f32_32x32x16_bf16 v[64:79], a[48:51], v[56:59], v[64:79]\n"
      "ds_read_b64_tr_b16 v[24:25], v128 offset:20480\n"
      "ds_read_b64_tr_b16 v[26:27], v129 offset:20480\n"
      "s_waitcnt lgkmcnt(14)\n"
      "v_mfma_f32_32x32x16_bf16 v[64:79], a[52:55], v[60:63], v[64:79]\n"
      "ds_read_b64_tr_b16 v[28:29], v128 offset:21504\n"
      "ds_read_b64_tr_b16 v[30:31], v129 offset:21504\n"
      "s_add_u32 m0, s69, 49152\n"
      "s_add_u32 s67, s87, 6144\n"
      "buffer_load_dwordx4 v254, s[8:11], s67 offen sc1 lds\n"
      "s_waitcnt vmcnt(10)\n"
      "s_barrier\n"
      "s_waitcnt lgkmcnt(14)\n"
      "v_mfma_f32_32x32x16_bf16 v[64:79], a[56:59], v[0:3], v[64:79]\n"
      "ds_read_b64_tr_b16 v[32:33], v128 offset:22528\n"
      "ds_read_b64_tr_b16 v[34:35], v129 offset:22528\n"
      "s_waitcnt lgkmcnt(14)\n"
      "v_mfma_f32_32x32x16_bf16 v[64:79], a[60:63], v[4:7], v[64:79]\n"
      "ds_read_b64_tr_b16 v[36:37], v128 offset:23552\n"
      "ds_read_b64_tr_b16 v[38:39], v129 offset:23552\n"
      "s_add_u32 m0, s69, 51200\n"
      "s_add_u32 s67, s83, 8192\n"
      "buffer_load_dwordx4 v254, s[8:11], s67 offen sc1 lds\n"
      "s_waitcnt lgkmcnt(14)\n"
      "v_mfma_f32_32x32x16_bf16 v[64:79], a[64:67], v[8:11], v[64:79]\n"
      "ds_read_b64_tr_b16 v[40:41], v128 offset:24576\n"
      "ds_read_b64_tr_b16 v[42:43], v129 offset:24576\n"
      "s_waitcnt lgkmcnt(14)\n"
      "v_mfma_f32_32x32x16_bf16 v[64:79], a[68:71], v[12:15], v[64:79]\n"
      "ds_read_b64_tr_b16 v[44:45], v128 offset:25600\n"
      "ds_read_b64_tr_b16 v[46:47], v129 offset:25600\n"
      "s_add_u32 m0, s69, 53248\n"
      "s_add_u32 s67, s83, 10240\n"
      "buffer_load_dwordx4 v254, s[8:11], s67 offen sc1 lds\n"
      "s_waitcnt lgkmcnt(14)\n"
      "v_mfma_f32_32x32x16_bf16 v[64:79], a[72:75], v[16:19], v[64:79]\n"
      "ds_read_b64_tr_b16 v[48:49], v128 offset:26624\n"
      "ds_read_b64_tr_b16 v[50:51], v129 offset:26624\n"
      "s_waitcnt lgkmcnt(14)\n"
      "v_mfma_f32_32x32x16_bf16 v[64:79], a[76:79], v[20:23], v[64:79]\n"
      "ds_read_b64_tr_b16 v[52:53], v128 offset:27648\n"
      "ds_read_b64_tr_b16 v[54:55], v129 offset:27648\n"
      "s_add_u32 m0, s69, 55296\n"
      "s_add_u32 s67, s83, 12288\n"
      "buffer_load_dwordx4 v254, s[8:11], s67 offen sc1 lds\n"
      "s_waitcnt lgkmcnt(14)\n"
      "v_mfma_f32_32x32x16_bf16 v[64:79], a[80:83], v[24:27], v[64:79]\n"
      "ds_read_b64_tr_b16 v[56:57], v128 offset:28672\n"
      "ds_read_b64_tr_b16 v[58:59], v129 offset:28672\n"
      "s_waitcnt lgkmcnt(14)\n"
      "v_mfma_f32_32x32x16_bf16 v[64:79], a[84:87], v[28:31], v[64:79]\n"
      "ds_read_b64_tr_b16 v[60:61], v128 offset:29696\n"
      "ds_read_b64_tr_b16 v[62:63], v129 offset:29696\n"
      "s_waitcnt vmcnt(9)\n"
      "s_barrier\n"
      "s_waitcnt lgkmcnt(14)\n"
      "v_mfma_f32_32x32x16_bf16 v[64:79], a[88:91], v[32:35], v[64:79]\n"
      "ds_read_b64_tr_b16 v[0:1], v128 offset:30720\n"
      "ds_read_b64_tr_b16 v[2:3], v129 offset:30720\n"
      "s_waitcnt lgkmcnt(14)\n"
      "v_mfma_f32_32x32x16_bf16 v[64:79], a[92:95], v[36:39], v[64:79]\n"
      "ds_read_b64_tr_b16 v[4:5], v128 offset:31744\n"
      "ds_read_b64_tr_b16 v[6:7], v129 offset:31744\n"
      "s_waitcnt lgkmcnt(14)\n"
      "v_mfma_f32_32x32x16_bf16 v[64:79], a[96:99], v[40:43], v[64:79]\n"
      "ds_read_b64_tr_b16 v[8:9], v128 offset:32768\n"
      "ds_read_b64_tr_b16 v[10:11], v129 offset:32768\n"
      "s_waitcnt lgkmcnt(14)\n"
      "v_mfma_f32_32x32x16_bf16 v[64:79], a[100:103], v[44:47], v[64:79]\n"
      "ds_read_b64_tr_b16 v[12:13], v128 offset:33792\n"
      "ds_read_b64_tr_b16 v[14:15], v129 offset:33792\n"
      "s_waitcnt lgkmcnt(14)\n"
      "v_mfma_f32_32x32x16_bf16 v[64:79], a[104:107], v[48:51], v[64:79]\n"
      "ds_read_b64_tr_b16 v[16:17], v128 offset:34816\n"
      "ds_read_b64_tr_b16 v[18:19], v129 offset:34816\n"
      "s_waitcnt lgkmcnt(14)\n"
      "v_mfma_f32_32x32x16_bf16 v[64:79], a[108:111], v[52:55], v[64:79]\n"
      "ds_read_b64_tr_b16 v[20:21], v128 offset:35840\n"
      "ds_read_b64_tr_b16 v[22:23], v129 offset:35840\n"
      "s_waitcnt lgkmcnt(14)\n"
      "v_mfma_f32_32x32x16_bf16 v[64:79], a[112:115], v[56:59], v[64:79]\n"
      "ds_read_b64_tr_b16 v[24:25], v128 offset:36864\n"
      "ds_read_b64_tr_b16 v[26:27], v129 offset:36864\n"
      "s_waitcnt lgkmcnt(14)\n"
      "v_mfma_f32_32x32x16_bf16 v[64:79], a[116:119], v[60:63], v[64:79]\n"
      "ds_read_b64_tr_b16 v[28:29], v128 offset:37888\n"
      "ds_read_b64_tr_b16 v[30:31], v129 offset:37888\n"
      "s_waitcnt vmcnt(5)\n"
      "s_barrier\n"
      "s_waitcnt lgkmcnt(14)\n"
      "v_mfma_f32_32x32x16_bf16 v[64:79], a[120:123], v[0:3], v[64:79]\n"
      "ds_read_b64_tr_b16 v[32:33], v128 offset:38912\n"
      "ds_read_b64_tr_b16 v[34:35], v129 offset:38912\n"
      "s_waitcnt lgkmcnt(14)\n"
      "v_mfma_f32_32x32x16_bf16 v[64:79], a[124:127], v[4:7], v[64:79]\n"
      "ds_read_b64_tr_b16 v[36:37], v128 offset:39936\n"
      "ds_read_b64_tr_b16 v[38:39], v129 offset:39936\n"
      "s_waitcnt lgkmcnt(14)\n"
      "v_mfma_f32_32x32x16_bf16 v[64:79], a[128:131], v[8:11], v[64:79]\n"
      "ds_read_b64_tr_b16 v[40:41], v128 offset:40960\n"
      "ds_read_b64_tr_b16 v[42:43], v129 offset:40960\n"
      "s_waitcnt lgkmcnt(14)\n"
      "v_mfma_f32_32x32x16_bf16 v[64:79], a[132:135], v[12:15], v[64:79]\n"
      "ds_read_b64_tr_b16 v[44:45], v128 offset:41984\n"
      "ds_read_b64_tr_b16 v[46:47], v129 offset:41984\n"
      "s_waitcnt lgkmcnt(14)\n"
      "v_mfma_f32_32x32x16_bf16 v[64:79], a[136:139], v[16:19], v[64:79]\n"
      "ds_read_b64_tr_b16 v[48:49], v128 offset:43008\n"
      "ds_read_b64_tr_b16 v[50:51], v129 offset:43008\n"
      "s_waitcnt lgkmcnt(14)\n"
      "v_mfma_f32_32x32x16_bf16 v[64:79], a[140:143], v[20:23], v[64:79]\n"
      "ds_read_b64_tr_b16 v[52:53], v128 offset:44032\n"
      "ds_read_b64_tr_b16 v[54:55], v129 offset:44032\n"
      "s_waitcnt lgkmcnt(14)\n"
      "v_mfma_f32_32x32x16_bf16 v[64:79], a[144:147], v[24:27], v[64:79]\n"
      "ds_read_b64_tr_b16 v[56:57], v128 offset:45056\n"
      "ds_read_b64_tr_b16 v[58:59], v129 offset:45056\n"
      "s_waitcnt lgkmcnt(14)\n"
      "v_mfma_f32_32x32x16_bf16 v[64:79], a[148:151], v[28:31], v[64:79]\n"
      "ds_read_b64_tr_b16 v[60:61], v128 offset:46080\n"
      "ds_read_b64_tr_b16 v[62:63], v129 offset:46080\n"
      "s_waitcnt vmcnt(2)\n"
      "s_barrier\n"
      "s_waitcnt lgkmcnt(14)\n"
      "v_mfma_f32_32x32x16_bf16 v[64:79], a[152:155], v[32:35], v[64:79]\n"
      "ds_read_b64_tr_b16 v[0:1], v128 offset:47104\n"
      "ds_read_b64_tr_b16 v[2:3], v129 offset:47104\n"
      "s_waitcnt lgkmcnt(14)\n"
      "v_mfma_f32_32x32x16_bf16 v[64:79], a[156:159], v[36:39], v[64:79]\n"
      "ds_read_b64_tr_b16 v[4:5], v128 offset:48128\n"
      "ds_read_b64_tr_b16 v[6:7], v129 offset:48128\n"
      "s_waitcnt lgkmcnt(14)\n"
      "v_mfma_f32_32x32x16_bf16 v[64:79], a[160:163], v[40:43], v[64:79]\n"
      "ds_read_b64_tr_b16 v[8:9], v128 offset:49152\n"
      "ds_read_b64_tr_b16 v[10:11], v129 offset:49152\n"
      "s_waitcnt lgkmcnt(14)\n"
      "v_mfma_f32_32x32x16_bf16 v[64:79], a[164:167], v[44:47], v[64:79]\n"
      "ds_read_b64_tr_b16 v[12:13], v128 offset:50176\n"
      "ds_read_b64_tr_b16 v[14:15], v129 offset:50176\n"
      "s_waitcnt lgkmcnt(14)\n"
      "v_mfma_f32_32x32x16_bf16 v[64:79], a[168:171], v[48:51], v[64:79]\n"
      "ds_read_b64_tr_b16 v[16:17], v128 offset:51200\n"
      "ds_read_b64_tr_b16 v[18:19], v129 offset:51200\n"
      "s_waitcnt lgkmcnt(14)\n"
      "v_mfma_f32_32x32x16_bf16 v[64:79], a[172:175], v[52:55], v[64:79]\n"
      "ds_read_b64_tr_b16 v[20:21], v128 offset:52224\n"
      "ds_read_b64_tr_b16 v[22:23], v129 offset:52224\n"
      "s_waitcnt lgkmcnt(14)\n"
      "v_mfma_f32_32x32x16_bf16 v[64:79], a[176:179], v[56:59], v[64:79]\n"
      "s_waitcnt lgkmcnt(12)\n"
      "v_mfma_f32_32x32x16_bf16 v[64:79], a[180:183], v[60:63], v[64:79]\n"
      "s_waitcnt vmcnt(0)\n"
      "s_barrier\n"
      "s_waitcnt lgkmcnt(10)\n"
      "v_mfma_f32_32x32x16_bf16 v[64:79], a[184:187], v[0:3], v[64:79]\n"
      "ds_read_b64_tr_b16 v[32:33], v128 offset:53248\n"
      "ds_read_b64_tr_b16 v[34:35], v129 offset:53248\n"
      "s_waitcnt lgkmcnt(10)\n"
      "v_mfma_f32_32x32x16_bf16 v[64:79], a[188:191], v[4:7], v[64:79]\n"
      "ds_read_b64_tr_b16 v[36:37], v128 offset:54272\n"
      "ds_read_b64_tr_b16 v[38:39], v129 offset:54272\n"
      "s_waitcnt lgkmcnt(10)\n"
      "v_mfma_f32_32x32x16_bf16 v[64:79], a[192:195], v[8:11], v[64:79]\n"
      "ds_read_b64_tr_b16 v[40:41], v128 offset:55296\n"
      "ds_read_b64_tr_b16 v[42:43], v129 offset:55296\n"
      "s_waitcnt lgkmcnt(10)\n"
      "v_mfma_f32_32x32x16_bf16 v[64:79], a[196:199], v[12:15], v[64:79]\n"
      "ds_read_b64_tr_b16 v[44:45], v128 offset:56320\n"
      "ds_read_b64_tr_b16 v[46:47], v129 offset:56320\n"
      "s_waitcnt lgkmcnt(10)\n"
      "v_mfma_f32_32x32x16_bf16 v[64:79], a[200:203], v[16:19], v[64:79]\n"
      "s_waitcnt lgkmcnt(8)\n"
      "v_mfma_f32_32x32x16_bf16 v[64:79], a[204:207], v[20:23], v[64:79]\n"
      "s_waitcnt lgkmcnt(6)\n"
      "v_mfma_f32_32x32x16_bf16 v[64:79], a[208:211], v[32:35], v[64:79]\n"
      "s_waitcnt lgkmcnt(4)\n"
      "v_mfma_f32_32x32x16_bf16 v[64:79], a[212:215], v[36:39], v[64:79]\n"
      "s_waitcnt lgkmcnt(2)\n"
      "v_mfma_f32_32x32x16_bf16 v[64:79], a[216:219], v[40:43], v[64:79]\n"
      "s_waitcnt lgkmcnt(0)\n"
      "v_mfma_f32_32x32x16_bf16 v[64:79], a[220:223], v[44:47], v[64:79]\n"
      "s_nop 11\n"
      "s_nop 0\n"
      "v_pk_fma_f32 v[64:65], v[64:65], v[96:97], v[112:113]\n"
      "v_pk_fma_f32 v[66:67], v[66:67], v[98:99], v[114:115]\n"
      "v_cvt_pk_bf16_f32 v0, v64, v65\n"
      "v_cvt_pk_bf16_f32 v1, v66, v67\n"
      "ds_write_b64 v130, v[0:1] offset:0\n"
      "v_pk_fma_f32 v[68:69], v[68:69], v[100:101], v[116:117]\n"
      "v_pk_fma_f32 v[70:71], v[70:71], v[102:103], v[118:119]\n"
      "v_cvt_pk_bf16_f32 v2, v68, v69\n"
      "v_cvt_pk_bf16_f32 v3, v70, v71\n"
      "ds_write_b64 v130, v[2:3] offset:16\n"
      "v_pk_fma_f32 v[72:73], v[72:73], v[104:105], v[120:121]\n"
      "v_pk_fma_f32 v[74:75], v[74:75], v[106:107], v[122:123]\n"
      "v_cvt_pk_bf16_f32 v4, v72, v73\n"
      "v_cvt_pk_bf16_f32 v5, v74, v75\n"
      "ds_write_b64 v130, v[4:5] offset:32\n"
      "v_pk_fma_f32 v[76:77], v[76:77], v[108:109], v[124:125]\n"
      "v_pk_fma_f32 v[78:79], v[78:79], v[110:111], v[126:127]\n"
      "v_cvt_pk_bf16_f32 v6, v76, v77\n"
      "v_cvt_pk_bf16_f32 v7, v78, v79\n"
      "ds_write_b64 v130, v[6:7] offset:48\n"
      "ds_read_b128 v[8:11], v131 offset:0\n"
      "ds_read_b128 v[12:15], v131 offset:1280\n"
      "s_waitcnt lgkmcnt(1)\n"
      "buffer_store_dwordx4 v[8:11], v132, s[44:47], s74 offen\n"
      "s_waitcnt lgkmcnt(0)\n"
      "buffer_store_dwordx4 v[12:15], v133, s[44:47], s74 offen\n"
      "Lend_%=:\n"
      "s_endpgm\n"
      "Lm4_%=:\n"
      "v_lshl_add_u32 v128, v22, 3, v21\n"
      "v_lshlrev_b32 v128, 7, v128\n"
      "v_lshl_add_u32 v128, v20, 3, v128\n"
      "v_lshrrev_b32 v24, 1, v21\n"
      "v_lshl_add_u32 v128, v24, 5, v128\n"
      "v_and_b32 v24, 1, v22\n"
      "v_lshl_add_u32 v128, v24, 6, v128\n"
      "s_lshl_b32 s63, s53, 14\n"
      "v_add_u32 v128, s63, v128\n"
      "v_xor_b32 v129, 32, v128\n"
      "v_xor_b32 v130, 64, v128\n"
      "v_xor_b32 v131, 96, v128\n"
      "s_waitcnt vmcnt(8)\n"
      "s_add_u32 m0, m0, 1024\n"
      "s_add_u32 s98, s98, 49152\n"
      "buffer_load_dwordx4 v0, s[36:39], s98 offen lds\n"
      "s_add_u32 m0, m0, 1024\n"
      "s_add_u32 s98, s98, 49152\n"
      "buffer_load_dwordx4 v1, s[36:39], s98 offen lds\n"
      "v_readfirstlane_b32 s65, v158\n"
      "v_readfirstlane_b32 s66, v159\n"
      "s_mul_i32 s56, s65, 0x9E3779B1\n"
      "s_add_u32 s56, s56, 0x7F4A7C15\n"
      "s_mul_i32 s57, s66, 0x85EBCA75\n"
      "s_add_u32 s57, s57, 0x2545F491\n"
      "v_lshlrev_b32 v28, 16, v5\n"
      "v_and_b32 v29, 0xffff0000, v5\n"
      "v_mul_f32 v24, 0xbfb8aa3b, v28\n"
      "v_mul_f32 v25, 0xbfb8aa3b, v29\n"
      "v_exp_f32 v24, v24\n"
      "v_exp_f32 v25, v25\n"
      "v_mov_b32 v252, s56\n"
      "v_mov_b32 v253, s57\n"
      "v_add_f32 v24, 1.0, v24\n"
      "v_add_f32 v25, 1.0, v25\n"
      "v_rcp_f32 v24, v24\n"
      "v_rcp_f32 v25, v25\n"
      "v_mul_f32 v28, v28, v24\n"
      "v_mul_f32 v29, v29, v25\n"
      "v_cvt_pk_bf16_f32 v5, v28, v29\n"
      "s_lshl_b32 s63, s53, 8\n"
      "s_add_u32 s63, s63, 65536\n"
      "v_lshl_add_u32 v24, v22, 4, s63\n"
      "v_add_u32 v25, s63, v2\n"
      "v_and_b32 v26, 15, v255\n"
      "v_lshl_add_u32 v26, v26, 2, s63\n"
      "v_add_u32 v27, 65536, v254\n"
      "ds_write_b32 v25, v5\n"
      "ds_read_b128 v[80:83], v24 offset:0\n"
      "ds_read_b128 v[84:87], v24 offset:64\n"
      "ds_read_b128 v[88:91], v24 offset:128\n"
      "ds_read_b128 v[92:95], v24 offset:192\n"
      "s_add_u32 m0, m0, 1024\n"
      "s_add_u32 s98, s98, 49152\n"
      "buffer_load_dwordx4 v0, s[36:39], s98 offen lds\n"
      "s_add_u32 m0, m0, 1024\n"
      "s_add_u32 s98, s98, 49152\n"
      "buffer_load_dwordx4 v1, s[36:39], s98 offen lds\n"
      "s_add_u32 m0, m0, 1024\n"
      "s_add_u32 s98, s98, 49152\n"
      "buffer_load_dwordx4 v0, s[36:39], s98 offen lds\n"
      "s_add_u32 m0, m0, 1024\n"
      "s_add_u32 s98, s98, 49152\n"
      "buffer_load_dwordx4 v1, s[36:39], s98 offen lds\n"
      "s_waitcnt vmcnt(10)\n"
      "ds_read_b64_tr_b16 v[96:97], v128 offset:0\n"
      "ds_read_b64_tr_b16 v[98:99], v128 offset:512\n"
      "ds_read_b64_tr_b16 v[100:101], v129 offset:0\n"
      "ds_read_b64_tr_b16 v[102:103], v129 offset:512\n"
      "ds_read_b64_tr_b16 v[104:105], v130 offset:0\n"
      "ds_read_b64_tr_b16 v[106:107], v130 offset:512\n"
      "ds_read_b64_tr_b16 v[108:109], v131 offset:0\n"
      "ds_read_b64_tr_b16 v[110:111], v131 offset:512\n"
      "s_add_u32 m0, m0, 1024\n"
      "s_add_u32 s98, s98, 49152\n"
      "buffer_load_dwordx4 v0, s[36:39], s98 offen lds\n"
      "s_add_u32 m0, m0, 1024\n"
      "s_add_u32 s98, s98, 49152\n"
      "buffer_load_dwordx4 v1, s[36:39], s98 offen lds\n"
      "s_add_u32 m0, m0, 1024\n"
      "s_add_u32 s98, s98, 49152\n"
      "buffer_load_dwordx4 v0, s[36:39], s98 offen lds\n"
      "s_add_u32 m0, m0, 1024\n"
      "s_add_u32 s98, s98, 49152\n"
      "buffer_load_dwordx4 v1, s[36:39], s98 offen lds\n"
      "s_waitcnt vmcnt(8)\n"
      "ds_read_b64_tr_b16 v[112:113], v128 offset:4096\n"
      "ds_read_b64_tr_b16 v[114:115], v128 offset:4608\n"
      "ds_read_b64_tr_b16 v[116:117], v129 offset:4096\n"
      "ds_read_b64_tr_b16 v[118:119], v129 offset:4608\n"
      "ds_read_b64_tr_b16 v[120:121], v130 offset:4096\n"
      "ds_read_b64_tr_b16 v[122:123], v130 offset:4608\n"
      "ds_read_b64_tr_b16 v[124:125], v131 offset:4096\n"
      "ds_read_b64_tr_b16 v[126:127], v131 offset:4608\n"
      "s_waitcnt lgkmcnt(14)\n"
      "v_mfma_f32_16x16x32_bf16 v[132:135], v[80:83], v[96:99], 0\n"
      "s_waitcnt lgkmcnt(12)\n"
      "v_mfma_f32_16x16x32_bf16 v[136:139], v[80:83], v[100:103], 0\n"
      "s_waitcnt lgkmcnt(10)\n"
      "v_mfma_f32_16x16x32_bf16 v[140:143], v[80:83], v[104:107], 0\n"
      "s_waitcnt lgkmcnt(8)\n"
      "v_mfma_f32_16x16x32_bf16 v[144:147], v[80:83], v[108:111], 0\n"
      "buffer_load_dwordx4 v[160:163], v254, s[12:15], s71 offen offset:0\n"
      "buffer_load_dwordx4 v[164:167], v254, s[12:15], s71 offen offset:1024\n"
      "buffer_load_dwordx4 v[168:171], v254, s[12:15], s71 offen offset:2048\n"
      "buffer_load_dwordx4 v[172:175], v254, s[12:15], s71 offen offset:3072\n"
      "s_waitcnt vmcnt(8)\n"
      "ds_read_b64_tr_b16 v[96:97], v128 offset:8192\n"
      "ds_read_b64_tr_b16 v[98:99], v128 offset:8704\n"
      "ds_read_b64_tr_b16 v[100:101], v129 offset:8192\n"
      "ds_read_b64_tr_b16 v[102:103], v129 offset:8704\n"
      "ds_read_b64_tr_b16 v[104:105], v130 offset:8192\n"
      "ds_read_b64_tr_b16 v[106:107], v130 offset:8704\n"
      "ds_read_b64_tr_b16 v[108:109], v131 offset:8192\n"
      "ds_read_b64_tr_b16 v[110:111], v131 offset:8704\n"
      "s_waitcnt lgkmcnt(14)\n"
      "v_mfma_f32_16x16x32_bf16 v[132:135], v[84:87], v[112:115], v[132:135]\n"
      "s_waitcnt lgkmcnt(12)\n"
      "v_mfma_f32_16x16x32_bf16 v[136:139], v[84:87], v[116:119], v[136:139]\n"
      "s_waitcnt lgkmcnt(10)\n"
      "v_mfma_f32_16x16x32_bf16 v[140:143], v[84:87], v[120:123], v[140:143]\n"
      "s_waitcnt lgkmcnt(8)\n"
      "v_mfma_f32_16x16x32_bf16 v[144:147], v[84:87], v[124:127], v[144:147]\n"
      "s_xor_b32 s69, s71, 4096\n"
      "buffer_load_dwordx4 v[176:179], v254, s[12:15], s69 offen offset:0\n"
      "buffer_load_dwordx4 v[180:183], v254, s[12:15], s69 offen offset:1024\n"
      "buffer_load_dwordx4 v[184:187], v254, s[12:15], s69 offen offset:2048\n"
      "buffer_load_dwordx4 v[188:191], v254, s[12:15], s69 offen offset:3072\n"
      "s_waitcnt vmcnt(8)\n"
      "ds_read_b64_tr_b16 v[112:113], v128 offset:12288\n"
      "ds_read_b64_tr_b16 v[114:115], v128 offset:12800\n"
      "ds_read_b64_tr_b16 v[116:117], v129 offset:12288\n"
      "ds_read_b64_tr_b16 v[118:119], v129 offset:12800\n"
      "ds_read_b64_tr_b16 v[120:121], v130 offset:12288\n"
      "ds_read_b64_tr_b16 v[122:123], v130 offset:12800\n"
      "ds_read_b64_tr_b16 v[124:125], v131 offset:12288\n"
      "ds_read_b64_tr_b16 v[126:127], v131 offset:12800\n"
      "s_waitcnt lgkmcnt(14)\n"
      "v_mfma_f32_16x16x32_bf16 v[132:135], v[88:91], v[96:99], v[132:135]\n"
      "s_waitcnt lgkmcnt(12)\n"
      "v_mfma_f32_16x16x32_bf16 v[136:139], v[88:91], v[100:103], v[136:139]\n"
      "s_waitcnt lgkmcnt(10)\n"
      "v_mfma_f32_16x16x32_bf16 v[140:143], v[88:91], v[104:107], v[140:143]\n"
      "s_waitcnt lgkmcnt(8)\n"
      "v_mfma_f32_16x16x32_bf16 v[144:147], v[88:91], v[108:111], v[144:147]\n"
      "s_waitcnt lgkmcnt(6)\n"
      "v_mfma_f32_16x16x32_bf16 v[132:135], v[92:95], v[112:115], v[132:135]\n"
      "s_waitcnt lgkmcnt(4)\n"
      "v_mfma_f32_16x16x32_bf16 v[136:139], v[92:95], v[116:119], v[136:139]\n"
      "s_waitcnt lgkmcnt(2)\n"
      "v_mfma_f32_16x16x32_bf16 v[140:143], v[92:95], v[120:123], v[140:143]\n"
      "s_waitcnt lgkmcnt(0)\n"
      "v_mfma_f32_16x16x32_bf16 v[144:147], v[92:95], v[124:127], v[144:147]\n"
      "s_nop 1\n"
      "ds_write_b32 v26, v132 offset:1024\n"
      "s_nop 0\n"
      "ds_write_b32 v26, v136 offset:1088\n"
      "s_nop 0\n"
      "ds_write_b32 v26, v140 offset:1152\n"
      "s_nop 0\n"
      "ds_write_b32 v26, v144 offset:1216\n"
      "s_waitcnt lgkmcnt(0)\n"
      "s_barrier\n"
      "ds_read_b128 v[64:67], v27 offset:1024\n"
      "ds_read_b128 v[68:71], v27 offset:1280\n"
      "ds_read_b128 v[72:75], v27 offset:1536\n"
      "ds_read_b128 v[76:79], v27 offset:1792\n"
      "s_xor_b32 s69, s71, 8192\n"
      "buffer_load_dwordx4 v[192:195], v254, s[12:15], s69 offen offset:0\n"
      "s_waitcnt vmcnt(8)\n"
      "v_lshlrev_b32 v32, 16, v160\n"
      "v_and_b32 v33, 0xffff0000, v160\n"
      "v_lshlrev_b32 v34, 16, v161\n"
      "v_and_b32 v35, 0xffff0000, v161\n"
      "v_lshlrev_b32 v36, 16, v162\n"
      "v_and_b32 v37, 0xffff0000, v162\n"
      "v_lshlrev_b32 v38, 16, v163\n"
      "v_and_b32 v39, 0xffff0000, v163\n"
      "v_pk_add_f32 v[20:21], v[32:33], v[34:35]\n"
      "v_pk_mul_f32 v[160:161], v[32:33], v[32:33]\n"
      "v_pk_add_f32 v[28:29], v[36:37], v[38:39]\n"
      "v_pk_fma_f32 v[160:161], v[34:35], v[34:35], v[160:161]\n"
      "v_pk_add_f32 v[20:21], v[20:21], v[28:29]\n"
      "v_pk_fma_f32 v[160:161], v[36:37], v[36:37], v[160:161]\n"
      "v_add_f32 v162, v20, v21\n"
      "v_pk_fma_f32 v[160:161], v[38:39], v[38:39], v[160:161]\n"
      "v_add_f32 v160, v160, v161\n"
      "s_lshl_b32 s63, s75, 6\n"
      "s_add_u32 s63, s63, 53248\n"
      "s_waitcnt lgkmcnt(0)\n"
      "v_pk_add_f32 v[64:65], v[64:65], v[68:69]\n"
      "v_pk_add_f32 v[66:67], v[66:67], v[70:71]\n"
      "v_pk_add_f32 v[72:73], v[72:73], v[76:77]\n"
      "v_pk_add_f32 v[74:75], v[74:75], v[78:79]\n"
      "v_pk_add_f32 v[64:65], v[64:65], v[72:73]\n"
      "v_pk_add_f32 v[66:67], v[66:67], v[74:75]\n"
      "v_cvt_pk_bf16_f32 v68, v64, v65\n"
      "v_cvt_pk_bf16_f32 v69, v66, v67\n"
      "v_lshlrev_b32 v64, 16, v68\n"
      "v_and_b32 v65, 0xffff0000, v68\n"
      "v_lshlrev_b32 v66, 16, v69\n"
      "v_and_b32 v67, 0xffff0000, v69\n"
      "v_add_f32 v64, s76, v64\n"
      "v_add_f32 v65, s76, v65\n"
      "v_add_f32 v66, s76, v66\n"
      "v_add_f32 v67, s76, v67\n"
      "v_cvt_pk_bf16_f32 v24, v64, v65\n"
      "v_mov_b32 v25, s56\n"
      "v_cvt_pk_bf16_f32 v26, v66, v67\n"
      "v_mov_b32 v27, s57\n"
      "s_cmp_lg_u32 s53, 0\n"
      "s_cbranch_scc1 Lm6_%=\n"
      "s_mov_b64 exec, 0xffff\n"
      "buffer_store_dwordx4 v[24:27], v254, s[8:11], s63 offen sc1\n"
      "s_mov_b64 exec, -1\n"
      "Lm6_%=:\n"
      "s_branch Lm5_%=\n"
      "Lfb19_%=:\n"
      "s_cmp_lg_u32 s74, 0\n"
      "s_cbranch_scc1 Lok18_%=\n"
      "s_mov_b32 s74, 1\n"
      "s_waitcnt vmcnt(0) lgkmcnt(0)\n"
      "s_lshl_b32 s75, s68, 17\n"
      "s_add_u32 s75, s75, 3145728\n"
      "s_mov_b32 s76, 64\n"
      "v_mov_b32 v125, 0xff800000\n"
      "v_mov_b32 v126, 0xff800000\n"
      "Lfa20_%=:\n"
      "buffer_load_dwordx4 v[104:107], v254, s[8:11], s75 offen sc1\n"
      "s_waitcnt vmcnt(0)\n"
      "v_mfma_f32_32x32x16_bf16 v[0:15], v[104:107], v[96:99], 0\n"
      "v_mfma_f32_32x32x16_bf16 v[16:31], v[104:107], v[100:103], 0\n"
      "s_nop 10\n"
      "v_max3_f32 v125, v125, v0, v1\n"
      "v_max3_f32 v125, v125, v2, v3\n"
      "v_max3_f32 v125, v125, v4, v5\n"
      "v_max3_f32 v125, v125, v6, v7\n"
      "v_max3_f32 v125, v125, v8, v9\n"
      "v_max3_f32 v125, v125, v10, v11\n"
      "v_max3_f32 v125, v125, v12, v13\n"
      "v_max3_f32 v125, v125, v14, v15\n"
      "v_max3_f32 v126, v126, v16, v17\n"
      "v_max3_f32 v126, v126, v18, v19\n"
      "v_max3_f32 v126, v126, v20, v21\n"
      "v_max3_f32 v126, v126, v22, v23\n"
      "v_max3_f32 v126, v126, v24, v25\n"
      "v_max3_f32 v126, v126, v26, v27\n"
      "v_max3_f32 v126, v126, v28, v29\n"
      "v_max3_f32 v126, v126, v30, v31\n"
      "s_add_u32 s75, s75, 2048\n"
      "s_sub_u32 s76, s76, 1\n"
      "s_cmp_lg_u32 s76, 0\n"
      "s_cbranch_scc1 Lfa20_%=\n"
      "v_mov_b32 v64, v125\n"
      "s_nop 1\n"
      "v_permlane32_swap_b32 v64, v125\n"
      "v_max_f32 v64, v64, v125\n"
      "v_mul_f32 v64, -1.0, v64\n"
      "v_mov_b32 v80, v126\n"
      "s_nop 1\n"
      "v_permlane32_swap_b32 v80, v126\n"
      "v_max_f32 v80, v80, v126\n"
      "v_mul_f32 v80, -1.0, v80\n"
      "v_mov_b32 v32, 0\n"
      "v_mov_b32 v48, 0\n"
      "v_mov_b32 v33, 0\n"
      "v_mov_b32 v49, 0\n"
      "v_mov_b32 v34, 0\n"
      "v_mov_b32 v50, 0\n"
      "v_mov_b32 v35, 0\n"
      "v_mov_b32 v51, 0\n"
      "v_mov_b32 v36, 0\n"
      "v_mov_b32 v52, 0\n"
      "v_mov_b32 v37, 0\n"
      "v_mov_b32 v53, 0\n"
      "v_mov_b32 v38, 0\n"
      "v_mov_b32 v54, 0\n"
      "v_mov_b32 v39, 0\n"
      "v_mov_b32 v55, 0\n"
      "v_mov_b32 v40, 0\n"
      "v_mov_b32 v56, 0\n"
      "v_mov_b32 v41, 0\n"
      "v_mov_b32 v57, 0\n"
      "v_mov_b32 v42, 0\n"
      "v_mov_b32 v58, 0\n"
      "v_mov_b32 v43, 0\n"
      "v_mov_b32 v59, 0\n"
      "v_mov_b32 v44, 0\n"
      "v_mov_b32 v60, 0\n"
      "v_mov_b32 v45, 0\n"
      "v_mov_b32 v61, 0\n"
      "v_mov_b32 v46, 0\n"
      "v_mov_b32 v62, 0\n"
      "v_mov_b32 v47, 0\n"
      "v_mov_b32 v63, 0\n"
      "s_lshl_b32 s75, s68, 17\n"
      "s_add_u32 s75, s75, 3145728\n"
      "s_mov_b32 s76, 64\n"
      "s_mov_b32 s78, 0xffff\n"
      "s_mov_b32 s79, 0xffff\n"
      "Lfb21_%=:\n"
      "buffer_load_dwordx4 v[104:107], v254, s[8:11], s75 offen sc1\n"
      "s_mov_b64 exec, s[78:79]\n"
      "buffer_load_dwordx4 v[108:111], v134, s[8:11], s75 offen sc1\n"
      "buffer_load_dwordx4 v[112:115], v136, s[8:11], s75 offen sc1\n"
      "s_mov_b64 exec, -1\n"
      "s_waitcnt vmcnt(0)\n"
      "v_mfma_f32_32x32x16_bf16 v[0:15], v[104:107], v[96:99], 0\n"
      "v_mfma_f32_32x32x16_bf16 v[16:31], v[104:107], v[100:103], 0\n"
      "s_nop 10\n"
      "v_add_f32 v0, v0, v64\n"
      "v_add_f32 v1, v1, v64\n"
      "v_add_f32 v2, v2, v64\n"
      "v_add_f32 v3, v3, v64\n"
      "v_add_f32 v4, v4, v64\n"
      "v_add_f32 v5, v5, v64\n"
      "v_add_f32 v6, v6, v64\n"
      "v_add_f32 v7, v7, v64\n"
      "v_add_f32 v8, v8, v64\n"
      "v_add_f32 v9, v9, v64\n"
      "v_add_f32 v10, v10, v64\n"
      "v_add_f32 v11, v11, v64\n"
      "v_add_f32 v12, v12, v64\n"
      "v_add_f32 v13, v13, v64\n"
      "v_add_f32 v14, v14, v64\n"
      "v_add_f32 v15, v15, v64\n"
      "v_add_f32 v16, v16, v80\n"
      "v_add_f32 v17, v17, v80\n"
      "v_add_f32 v18, v18, v80\n"
      "v_add_f32 v19, v19, v80\n"
      "v_add_f32 v20, v20, v80\n"
      "v_add_f32 v21, v21, v80\n"
      "v_add_f32 v22, v22, v80\n"
      "v_add_f32 v23, v23, v80\n"
      "v_add_f32 v24, v24, v80\n"
      "v_add_f32 v25, v25, v80\n"
      "v_add_f32 v26, v26, v80\n"
      "v_add_f32 v27, v27, v80\n"
      "v_add_f32 v28, v28, v80\n"
      "v_add_f32 v29, v29, v80\n"
      "v_add_f32 v30, v30, v80\n"
      "v_add_f32 v31, v31, v80\n"
      "v_exp_f32 v0, v0\n"
      "v_exp_f32 v1, v1\n"
      "v_exp_f32 v2, v2\n"
      "v_exp_f32 v3, v3\n"
      "v_exp_f32 v4, v4\n"
      "v_exp_f32 v5, v5\n"
      "v_exp_f32 v6, v6\n"
      "v_exp_f32 v7, v7\n"
      "v_exp_f32 v8, v8\n"
      "v_exp_f32 v9, v9\n"
      "v_exp_f32 v10, v10\n"
      "v_exp_f32 v11, v11\n"
      "v_exp_f32 v12, v12\n"
      "v_exp_f32 v13, v13\n"
      "v_exp_f32 v14, v14\n"
      "v_exp_f32 v15, v15\n"
      "v_exp_f32 v16, v16\n"
      "v_exp_f32 v17, v17\n"
      "v_exp_f32 v18, v18\n"
      "v_exp_f32 v19, v19\n"
      "v_exp_f32 v20, v20\n"
      "v_exp_f32 v21, v21\n"
      "v_exp_f32 v22, v22\n"
      "v_exp_f32 v23, v23\n"
      "v_exp_f32 v24, v24\n"
      "v_exp_f32 v25, v25\n"
      "v_exp_f32 v26, v26\n"
      "v_exp_f32 v27, v27\n"
      "v_exp_f32 v28, v28\n"
      "v_exp_f32 v29, v29\n"
      "v_exp_f32 v30, v30\n"
      "v_exp_f32 v31, v31\n"
      "v_smfmac_f32_32x32x32_bf16 v[32:47], v[108:111], v[0:7], v138\n"
      "v_smfmac_f32_32x32x32_bf16 v[32:47], v[112:115], v[8:15], v138\n"
      "v_smfmac_f32_32x32x32_bf16 v[48:63], v[108:111], v[16:23], v138\n"
      "v_smfmac_f32_32x32x32_bf16 v[48:63], v[112:115], v[24:31], v138\n"
      "s_nop 15\n"
      "s_add_u32 s75, s75, 2048\n"
      "s_sub_u32 s76, s76, 1\n"
      "s_cmp_lg_u32 s76, 0\n"
      "s_cbranch_scc1 Lfb21_%=\n"
      "s_branch Lfin17_%=\n"
      "Lcode_%=:\n"
      ".p2align 12\n"
      :
      : "s"(__builtin_amdgcn_kernarg_segment_ptr()), "s"(blockIdx.x), "s"(wave)
      : "v0", "v1", "v2", "v3", "v4", "v5", "v6", "v7", "v8", "v9", "v10", "v11", "v12", "v13", "v14", "v15", "v16", "v17", "v18", "v19", "v20", "v21", "v22", "v23", "v24", "v25", "v26", "v27", "v28", "v29", "v30", "v31", "v32", "v33", "v34", "v35", "v36", "v37", "v38", "v39", "v40", "v41", "v42", "v43", "v44", "v45", "v46", "v47", "v48", "v49", "v50", "v51", "v52", "v53", "v54", "v55", "v56", "v57", "v58", "v59", "v60", "v61", "v62", "v63", "v64", "v65", "v66", "v67", "v68", "v69", "v70", "v71", "v72", "v73", "v74", "v75", "v76", "v77", "v78", "v79", "v80", "v81", "v82", "v83", "v84", "v85", "v86", "v87", "v88", "v89", "v90", "v91", "v92", "v93", "v94", "v95", "v96", "v97", "v98", "v99", "v100", "v101", "v102", "v103", "v104", "v105", "v106", "v107", "v108", "v109", "v110", "v111", "v112", "v113", "v114", "v115", "v116", "v117", "v118", "v119", "v120", "v121", "v122", "v123", "v124", "v125", "v126", "v127", "v128", "v129", "v130", "v131", "v132", "v133", "v134", "v135", "v136", "v137", "v138", "v139", "v140", "v141", "v142", "v143", "v144", "v145", "v146", "v147", "v148", "v149", "v150", "v151", "v152", "v153", "v154", "v155", "v156", "v157", "v158", "v159", "v160", "v161", "v162", "v163", "v164", "v165", "v166", "v167", "v168", "v169", "v170", "v171", "v172", "v173", "v174", "v175", "v176", "v177", "v178", "v179", "v180", "v181", "v182", "v183", "v184", "v185", "v186", "v187", "v188", "v189", "v190", "v191", "v192", "v193", "v194", "v195", "v196", "v197", "v198", "v199", "v200", "v201", "v202", "v203", "v204", "v205", "v206", "v207", "v208", "v209", "v210", "v211", "v212", "v213", "v214", "v215", "v216", "v217", "v218", "v219", "v220", "v221", "v222", "v223", "v224", "v225", "v226", "v227", "v228", "v229", "v230", "v231", "v232", "v233", "v234", "v235", "v236", "v237", "v238", "v239", "v240", "v241", "v242", "v243", "v244", "v245", "v246", "v247", "v248", "v249", "v250", "v251", "v252", "v253", "v254", "v255", "a0", "a1", "a2", "a3", "a4", "a5", "a6", "a7", "a8", "a9", "a10", "a11", "a12", "a13", "a14", "a15", "a16", "a17", "a18", "a19", "a20", "a21", "a22", "a23", "a24", "a25", "a26", "a27", "a28", "a29", "a30", "a31", "a32", "a33", "a34", "a35", "a36", "a37", "a38", "a39", "a40", "a41", "a42", "a43", "a44", "a45", "a46", "a47", "a48", "a49", "a50", "a51", "a52", "a53", "a54", "a55", "a56", "a57", "a58", "a59", "a60", "a61", "a62", "a63", "a64", "a65", "a66", "a67", "a68", "a69", "a70", "a71", "a72", "a73", "a74", "a75", "a76", "a77", "a78", "a79", "a80", "a81", "a82", "a83", "a84", "a85", "a86", "a87", "a88", "a89", "a90", "a91", "a92", "a93", "a94", "a95", "a96", "a97", "a98", "a99", "a100", "a101", "a102", "a103", "a104", "a105", "a106", "a107", "a108", "a109", "a110", "a111", "a112", "a113", "a114", "a115", "a116", "a117", "a118", "a119", "a120", "a121", "a122", "a123", "a124", "a125", "a126", "a127", "a128", "a129", "a130", "a131", "a132", "a133", "a134", "a135", "a136", "a137", "a138", "a139", "a140", "a141", "a142", "a143", "a144", "a145", "a146", "a147", "a148", "a149", "a150", "a151", "a152", "a153", "a154", "a155", "a156", "a157", "a158", "a159", "a160", "a161", "a162", "a163", "a164", "a165", "a166", "a167", "a168", "a169", "a170", "a171", "a172", "a173", "a174", "a175", "a176", "a177", "a178", "a179", "a180", "a181", "a182", "a183", "a184", "a185", "a186", "a187", "a188", "a189", "a190", "a191", "a192", "a193", "a194", "a195", "a196", "a197", "a198", "a199", "a200", "a201", "a202", "a203", "a204", "a205", "a206", "a207", "a208", "a209", "a210", "a211", "a212", "a213", "a214", "a215", "a216", "a217", "a218", "a219", "a220", "a221", "a222", "a223", "a224", "a225", "a226", "a227", "a228", "a229", "a230", "a231", "a232", "a233", "a234", "a235", "a236", "a237", "a238", "a239", "a240", "a241", "a242", "a243", "a244", "a245", "a246", "a247", "a248", "a249", "a250", "a251", "a252", "a253", "a254", "a255", "s4", "s5", "s6", "s7", "s8", "s9", "s10", "s11", "s12", "s13", "s14", "s15", "s16", "s17", "s18", "s19", "s20", "s21", "s22", "s23", "s24", "s25", "s26", "s27", "s28", "s29", "s30", "s31", "s32", "s33", "s34", "s35", "s36", "s37", "s38", "s39", "s40", "s41", "s42", "s43", "s44", "s45", "s46", "s47", "s48", "s49", "s50", "s51", "s52", "s53", "s54", "s55", "s56", "s57", "s58", "s59", "s60", "s61", "s62", "s63", "s64", "s65", "s66", "s67", "s68", "s69", "s70", "s71", "s72", "s73", "s74", "s75", "s76", "s77", "s78", "s79", "s80", "s81", "s82", "s83", "s84", "s85", "s86", "s87", "s88", "s89", "s90", "s91", "s92", "s93", "s94", "s95", "s96", "s97", "s98", "s99", "vcc", "scc", "memory");
}

static inline u64 desc(u64 bytes) { return bytes | (0x00020000ull << 32); }

extern "C" void kernel_launch(void* const* d_in, const int* in_sizes, int n_in, void* d_out, int out_size,
                              void* d_ws, size_t ws_size, hipStream_t stream) {
  (void)n_in; (void)ws_size;
  (void)hipFuncSetAttribute((const void*)adaln_block_kernel, hipFuncAttributeMaxDynamicSharedMemorySize, 163840);
  adaln_block_kernel<<<256, 256, 163840, stream>>>(
      d_ws, desc(21626880ull), d_in[0], desc(2ull * in_sizes[0]), d_in[1], desc(2ull * in_sizes[1]), d_in[2], desc(2ull * in_sizes[2]),
      d_in[4], desc(2ull * in_sizes[4]), d_in[5], desc(2ull * in_sizes[5]), d_in[7], desc(2ull * in_sizes[7]), d_in[9], desc(2ull * in_sizes[9]),
      d_in[8], desc(2ull * in_sizes[8]), d_out, desc(2ull * out_size), d_in[6], desc(2ull * in_sizes[6]));
}
```
